```python
import torch

def _softmax(x, axis=-1):
    return torch.softmax(x, dim=axis)
def _gelu(x):
    return torch.nn.functional.gelu(x, approximate="tanh")
def _silu(x):
    return torch.nn.functional.silu(x)
def _sigmoid(x):
    return torch.sigmoid(x)
def _layernorm(x, axis=-1, eps=1e-5):
    m = x.mean(dim=axis, keepdim=True)
    v = x.var(dim=axis, keepdim=True, unbiased=False)
    return (x - m) / torch.sqrt(v + eps)
def _scatter_add(tgt, idx, src, axis=0):
    return tgt.clone().index_add_(axis, idx, src)
def _segment_sum(src, seg, n):
    out = torch.zeros((n,) + src.shape[1:], dtype=src.dtype)
    return out.index_add_(0, seg, src)
def _pad(x, pw):
    flat = tuple(v for pair in reversed(pw) for v in pair)
    return torch.nn.functional.pad(x, flat)

kernel_name = 'recurrent_scan__sliding_block_cached__moe_arch_mod__prompt'

def setup_inputs():

    _w = (lambda a: (lambda t: t.to(torch.bfloat16) if t.is_floating_point() else t)(torch.as_tensor(a)))
    def _A_setup():
        B, Sq, D = 1, 2048, 512
        Hq, Hkv, Sk, Dh = 32, 32, 2048, 128
        assert Hq % Hkv == 0, (Hq, Hkv)
        return {
            "x": _w((lambda *s: torch.randn(*s, dtype=torch.bfloat16) * 0.05)(B, Sq, D)),
            "k_cache": _w((lambda *s: torch.randn(*s, dtype=torch.bfloat16) * 0.8)(B, Hkv, Sk, Dh)),
            "v_cache": _w((lambda *s: torch.randn(*s, dtype=torch.bfloat16) * 0.2)(B, Hkv, Sk, Dh)),
            "wq": _w((lambda *s: torch.randn(*s, dtype=torch.bfloat16) * 0.8)(D, Hq * Dh) * (D ** -0.5)),
            "wo": _w((lambda *s: torch.randn(*s, dtype=torch.bfloat16) * 0.2)(Hq * Dh, D) * ((Hq * Dh) ** -0.5)),
        }
    _A_in = _A_setup()

    def _B_setup():
        B, S, D, F, C = 1, 2048, 512, 1376, 2
        return {
            "x": _w((lambda *s: torch.randn(*s, dtype=torch.bfloat16) * 0.05)(B, S, D)),
            "w_r": _w((lambda *s: torch.randn(*s, dtype=torch.bfloat16) * 0.2)(D)),
            "w_gate": _w((lambda *s: torch.randn(*s, dtype=torch.bfloat16) * 0.1)(D, F)),
            "w_up": _w((lambda *s: torch.randn(*s, dtype=torch.bfloat16) * 0.1)(D, F)),
            "w_down": _w((lambda *s: torch.randn(*s, dtype=torch.bfloat16) * 0.1)(F, D)),
        }
    _B_in = _B_setup()

    return {
        "A_x": _A_in["x"],
        "A_k_cache": _A_in["k_cache"],
        "A_v_cache": _A_in["v_cache"],
        "A_wq": _A_in["wq"],
        "A_wo": _A_in["wo"],
        "B_x": _B_in["x"],
        "B_w_r": _B_in["w_r"],
        "B_w_gate": _B_in["w_gate"],
        "B_w_up": _B_in["w_up"],
        "B_w_down": _B_in["w_down"],
    }

def reference(A_x, A_k_cache, A_v_cache, A_wq, A_wo, B_x, B_w_r, B_w_gate, B_w_up, B_w_down):
    def _A_ref(x, k_cache, v_cache, wq, wo):
        B, Sq, D = x.shape
        Hq, Dh = 32, 128
        Hkv, Sk = k_cache.shape[1], k_cache.shape[2]
        W = min(4096, max(256, Sk // 4))
        rep = Hq // Hkv
        q = (lambda x, *d: x.permute(*d))((x @ wq).reshape(B, Sq, Hq, Dh), 0, 2, 1, 3)
        k = (lambda x, n, axis: torch.repeat_interleave(x, n, dim=axis))(k_cache[:, :, Sk - W:, :], rep, axis=1)
        v = (lambda x, n, axis: torch.repeat_interleave(x, n, dim=axis))(v_cache[:, :, Sk - W:, :], rep, axis=1)
        scores = torch.einsum("bhqd,bhkd->bhqk", q, k) * (Dh ** -0.5)
        p = _softmax(scores, axis=-1)
        o = torch.einsum("bhqk,bhkd->bhqd", p, v)
        return (lambda x, *d: x.permute(*d))(o, 0, 2, 1, 3).reshape(B, Sq, Hq * Dh) @ wo

    def _B_ref(x, w_r, w_gate, w_up, w_down):
        B, S, D = x.shape
        C = 2
        r = x @ w_r
        idx = torch.argsort(-r, axis=-1)[:, :C]
        idx_e = (lambda x, n, axis: torch.repeat_interleave(x, n, dim=axis))(idx.reshape(B, C, 1), D, axis=2)
        x_sel = (lambda x, i, axis: torch.take_along_dim(x, i, dim=axis))(x, idx_e, axis=1)
        g = _silu(x_sel @ w_gate)
        u = x_sel @ w_up
        y = (g * u) @ w_down
        r_sel = _sigmoid((lambda x, i, axis: torch.take_along_dim(x, i, dim=axis))(r, idx, axis=1))
        y = y * r_sel.reshape(B, C, 1)
        b_off = (torch.arange(B) * S).reshape(B, 1)
        flat_idx = (idx + b_off).reshape(B * C)
        zero = (x * 0.0).reshape(B * S, D)
        upd = _scatter_add(zero, flat_idx, y.reshape(B * C, D), axis=0)
        return x + upd.reshape(B, S, D)

    x = A_x
    xn = _layernorm(x, axis=-1)
    s = _A_ref(xn, A_k_cache, A_v_cache, A_wq, A_wo)
    g = _silu(_B_ref(xn, B_w_r, B_w_gate, B_w_up, B_w_down))
    return x + s * g
```

```cpp
#include <hip/hip_runtime.h>
#include <hip/hip_bf16.h>
#include <stdint.h>

typedef unsigned short u16;
__device__ __forceinline__ float bf2f(u16 v) { return __uint_as_float(((unsigned)v) << 16); }
__device__ __forceinline__ u16 f2bf(float f) { __bf16 b = (__bf16)f; return __builtin_bit_cast(u16, b); }
__device__ __forceinline__ float rbf(float f) { return bf2f(f2bf(f)); }

constexpr int S = 2048, D = 512, H = 32, DH = 128, SK = 2048, W = 512, F = 1376;

__global__ void k_ln(const u16* x, const u16* wr, u16* xn, float* r) {
  int row = blockIdx.x; int lane = threadIdx.x;
  float v[8]; float s = 0;
  for (int j = 0; j < 8; ++j) { v[j] = bf2f(x[row * D + lane * 8 + j]); s += v[j]; }
  for (int o = 32; o; o >>= 1) s += __shfl_xor(s, o);
  float mean = s / D; float ss = 0;
  for (int j = 0; j < 8; ++j) { float d = v[j] - mean; ss += d * d; }
  for (int o = 32; o; o >>= 1) ss += __shfl_xor(ss, o);
  float m = rbf(mean), var = rbf(ss / D);
  float sd = rbf(sqrtf(rbf(var + 1e-5f)));
  float acc = 0;
  for (int j = 0; j < 8; ++j) { float t = rbf(rbf(v[j] - m) / sd); xn[row * D + lane * 8 + j] = f2bf(t); acc += t * bf2f(wr[lane * 8 + j]); }
  for (int o = 32; o; o >>= 1) acc += __shfl_xor(acc, o);
  if (lane == 0) r[row] = rbf(acc);
}

__global__ void k_gemm(const u16* A, const u16* B, u16* C, int M, int N, int K) {
  __shared__ float As[16][17], Bs[16][17];
  int tx = threadIdx.x & 15, ty = threadIdx.x >> 4;
  int row = blockIdx.y * 16 + ty, col = blockIdx.x * 16 + tx;
  float acc = 0;
  for (int k0 = 0; k0 < K; k0 += 16) {
    As[ty][tx] = bf2f(A[(size_t)row * K + k0 + tx]);
    Bs[ty][tx] = bf2f(B[(size_t)(k0 + ty) * N + col]);
    __syncthreads();
    for (int k = 0; k < 16; ++k) acc += As[ty][k] * Bs[k][tx];
    __syncthreads();
  }
  C[(size_t)row * N + col] = f2bf(acc);
}

__global__ void k_attn(const u16* q, const u16* kc, const u16* vc, u16* o) {
  int row = blockIdx.x, h = blockIdx.y, t = threadIdx.x;
  __shared__ float qs[DH], p[W], red[256];
  if (t < DH) qs[t] = bf2f(q[(size_t)row * (H * DH) + h * DH + t]);
  __syncthreads();
  const u16* K = kc + ((size_t)h * SK + (SK - W)) * DH;
  const u16* V = vc + ((size_t)h * SK + (SK - W)) * DH;
  float mx = -1e30f;
  for (int k = t; k < W; k += 256) { float a = 0; for (int d = 0; d < DH; ++d) a += qs[d] * bf2f(K[k * DH + d]); a *= 0.08838834764831845f; p[k] = a; mx = fmaxf(mx, a); }
  red[t] = mx; __syncthreads();
  for (int s = 128; s; s >>= 1) { if (t < s) red[t] = fmaxf(red[t], red[t + s]); __syncthreads(); }
  mx = red[0]; __syncthreads();
  float sum = 0;
  for (int k = t; k < W; k += 256) { float e = __expf(p[k] - mx); p[k] = e; sum += e; }
  red[t] = sum; __syncthreads();
  for (int s = 128; s; s >>= 1) { if (t < s) red[t] += red[t + s]; __syncthreads(); }
  sum = red[0];
  if (t < DH) { float a = 0; for (int k = 0; k < W; ++k) a += p[k] * bf2f(V[k * DH + t]); o[(size_t)row * (H * DH) + h * DH + t] = f2bf(a / sum); }
}

__global__ void k_ffn(const float* r, const u16* xn, const u16* wg, const u16* wu, const u16* wd, float* y, int* idx) {
  __shared__ int sel[2]; __shared__ float hs[2][F];
  int t = threadIdx.x;
  if (t == 0) {
    int i0 = 0; for (int i = 1; i < S; ++i) if (r[i] > r[i0]) i0 = i;
    int i1 = (i0 == 0) ? 1 : 0; for (int i = 0; i < S; ++i) if (i != i0 && r[i] > r[i1]) i1 = i;
    sel[0] = i0; sel[1] = i1; idx[0] = i0; idx[1] = i1;
  }
  __syncthreads();
  for (int f = t; f < F; f += blockDim.x) for (int c = 0; c < 2; ++c) {
    float g = 0, u = 0;
    for (int d = 0; d < D; ++d) { float xv = bf2f(xn[sel[c] * D + d]); g += xv * bf2f(wg[d * F + f]); u += xv * bf2f(wu[d * F + f]); }
    g = rbf(g); u = rbf(u); g = rbf(g / (1.f + __expf(-g)));
    hs[c][f] = rbf(g * u);
  }
  __syncthreads();
  for (int d = t; d < D; d += blockDim.x) for (int c = 0; c < 2; ++c) {
    float a = 0; for (int f = 0; f < F; ++f) a += hs[c][f] * bf2f(wd[f * D + d]);
    float rs = rbf(1.f / (1.f + __expf(-r[sel[c]])));
    y[c * D + d] = rbf(rbf(a) * rs);
  }
}

__global__ void k_final(const u16* x, const u16* xn, const u16* s, const float* y, const int* idx, u16* out) {
  int i = blockIdx.x * 256 + threadIdx.x; int row = i / D, d = i % D;
  float b = bf2f(xn[i]);
  if (row == idx[0]) b = rbf(b + y[d]); else if (row == idx[1]) b = rbf(b + y[D + d]);
  float g = rbf(b / (1.f + __expf(-b)));
  out[i] = f2bf(bf2f(x[i]) + rbf(bf2f(s[i]) * g));
}

extern "C" void kernel_launch(void* const* d_in, const int* in_sizes, int n_in, void* d_out, int out_size, void* d_ws, size_t ws_size, hipStream_t stream) {
  const u16* x = (const u16*)d_in[0]; const u16* kc = (const u16*)d_in[1]; const u16* vc = (const u16*)d_in[2];
  const u16* wq = (const u16*)d_in[3]; const u16* wo = (const u16*)d_in[4];
  const u16* wr = (const u16*)d_in[6]; const u16* wg = (const u16*)d_in[7]; const u16* wu = (const u16*)d_in[8]; const u16* wd = (const u16*)d_in[9];
  char* ws = (char*)d_ws;
  u16* xn = (u16*)ws; u16* q = (u16*)(ws + (4 << 20)); u16* o = (u16*)(ws + (24 << 20)); u16* s = (u16*)(ws + (44 << 20));
  float* r = (float*)(ws + (48 << 20)); float* y = (float*)(ws + (49 << 20)); int* idx = (int*)(ws + (50 << 20));
  k_ln<<<S, 64, 0, stream>>>(x, wr, xn, r);
  k_gemm<<<dim3(H * DH / 16, S / 16), 256, 0, stream>>>(xn, wq, q, S, H * DH, D);
  k_attn<<<dim3(S, H), 256, 0, stream>>>(q, kc, vc, o);
  k_gemm<<<dim3(D / 16, S / 16), 256, 0, stream>>>(o, wo, s, S, D, H * DH);
  k_ffn<<<1, 1024, 0, stream>>>(r, xn, wg, wu, wd, y, idx);
  k_final<<<S * D / 256, 256, 0, stream>>>(x, xn, s, y, idx, (u16*)d_out);
}
```

```cpp
#include <hip/hip_runtime.h>
#include <hip/hip_bf16.h>
#include <stdint.h>

typedef unsigned short u16;
__device__ __forceinline__ float bf2f(u16 v) { return __uint_as_float(((unsigned)v) << 16); }
__device__ __forceinline__ u16 f2bf(float f) { __bf16 b = (__bf16)f; return __builtin_bit_cast(u16, b); }
__device__ __forceinline__ float rbf(float f) { return bf2f(f2bf(f)); }

constexpr int S = 2048, D = 512, H = 32, DH = 128, SK = 2048, W = 512, F = 1376;

__global__ void k_ln(const u16* x, const u16* wr, u16* xn, float* r) {
  int row = blockIdx.x; int lane = threadIdx.x;
  float v[8]; float s = 0;
  for (int j = 0; j < 8; ++j) { v[j] = bf2f(x[row * D + lane * 8 + j]); s += v[j]; }
  for (int o = 32; o; o >>= 1) s += __shfl_xor(s, o);
  float mean = s / D; float ss = 0;
  for (int j = 0; j < 8; ++j) { float d = v[j] - mean; ss += d * d; }
  for (int o = 32; o; o >>= 1) ss += __shfl_xor(ss, o);
  float m = rbf(mean), var = rbf(ss / D);
  float sd = rbf(sqrtf(rbf(var + 1e-5f)));
  float acc = 0;
  for (int j = 0; j < 8; ++j) { float t = rbf(rbf(v[j] - m) / sd); xn[row * D + lane * 8 + j] = f2bf(t); acc += t * bf2f(wr[lane * 8 + j]); }
  for (int o = 32; o; o >>= 1) acc += __shfl_xor(acc, o);
  if (lane == 0) r[row] = rbf(acc);
}

__global__ void k_gemm(const u16* A, const u16* B, u16* C, int M, int N, int K) {
  __shared__ float As[16][17], Bs[16][17];
  int tx = threadIdx.x & 15, ty = threadIdx.x >> 4;
  int row = blockIdx.y * 16 + ty, col = blockIdx.x * 16 + tx;
  float acc = 0;
  for (int k0 = 0; k0 < K; k0 += 16) {
    As[ty][tx] = bf2f(A[(size_t)row * K + k0 + tx]);
    Bs[ty][tx] = bf2f(B[(size_t)(k0 + ty) * N + col]);
    __syncthreads();
    for (int k = 0; k < 16; ++k) acc += As[ty][k] * Bs[k][tx];
    __syncthreads();
  }
  C[(size_t)row * N + col] = f2bf(acc);
}

__global__ void k_attn(const u16* q, const u16* kc, const u16* vc, u16* o) {
  int row = blockIdx.x, h = blockIdx.y, t = threadIdx.x;
  __shared__ float qs[DH], p[W], red[256];
  if (t < DH) qs[t] = bf2f(q[(size_t)row * (H * DH) + h * DH + t]);
  __syncthreads();
  const u16* K = kc + ((size_t)h * SK + (SK - W)) * DH;
  const u16* V = vc + ((size_t)h * SK + (SK - W)) * DH;
  float mx = -1e30f;
  for (int k = t; k < W; k += 256) { float a = 0; for (int d = 0; d < DH; ++d) a += qs[d] * bf2f(K[k * DH + d]); a *= 0.08838834764831845f; p[k] = a; mx = fmaxf(mx, a); }
  red[t] = mx; __syncthreads();
  for (int s = 128; s; s >>= 1) { if (t < s) red[t] = fmaxf(red[t], red[t + s]); __syncthreads(); }
  mx = red[0]; __syncthreads();
  float sum = 0;
  for (int k = t; k < W; k += 256) { float e = __expf(p[k] - mx); p[k] = e; sum += e; }
  red[t] = sum; __syncthreads();
  for (int s = 128; s; s >>= 1) { if (t < s) red[t] += red[t + s]; __syncthreads(); }
  sum = red[0];
  if (t < DH) { float a = 0; for (int k = 0; k < W; ++k) a += p[k] * bf2f(V[k * DH + t]); o[(size_t)row * (H * DH) + h * DH + t] = f2bf(a / sum); }
}

__global__ void k_ffn(const float* r, const u16* xn, const u16* wg, const u16* wu, const u16* wd, float* y, int* idx) {
  __shared__ int sel[2]; __shared__ float hs[2][F];
  int t = threadIdx.x;
  if (t == 0) {
    int i0 = 0; for (int i = 1; i < S; ++i) if (r[i] > r[i0]) i0 = i;
    int i1 = (i0 == 0) ? 1 : 0; for (int i = 0; i < S; ++i) if (i != i0 && r[i] > r[i1]) i1 = i;
    sel[0] = i0; sel[1] = i1; idx[0] = i0; idx[1] = i1;
  }
  __syncthreads();
  for (int f = t; f < F; f += blockDim.x) for (int c = 0; c < 2; ++c) {
    float g = 0, u = 0;
    for (int d = 0; d < D; ++d) { float xv = bf2f(xn[sel[c] * D + d]); g += xv * bf2f(wg[d * F + f]); u += xv * bf2f(wu[d * F + f]); }
    g = rbf(g); u = rbf(u); g = rbf(g / (1.f + __expf(-g)));
    hs[c][f] = rbf(g * u);
  }
  __syncthreads();
  for (int d = t; d < D; d += blockDim.x) for (int c = 0; c < 2; ++c) {
    float a = 0; for (int f = 0; f < F; ++f) a += hs[c][f] * bf2f(wd[f * D + d]);
    float rs = rbf(1.f / (1.f + __expf(-r[sel[c]])));
    y[c * D + d] = rbf(rbf(a) * rs);
  }
}

__global__ void k_final(const u16* x, const u16* xn, const u16* s, const float* y, const int* idx, u16* out) {
  int i = blockIdx.x * 256 + threadIdx.x; int row = i / D, d = i % D;
  float b = bf2f(xn[i]);
  if (row == idx[0]) b = rbf(b + y[d]); else if (row == idx[1]) b = rbf(b + y[D + d]);
  float g = rbf(b / (1.f + __expf(-b)));
  out[i] = f2bf(bf2f(x[i]) + rbf(bf2f(s[i]) * g));
}


__global__ void k_probe(const char* p, unsigned stride, int mode, unsigned* sink) {
  unsigned long long t0, t1; unsigned v = 0, off = 0;
  asm volatile("s_memrealtime %0\n\ts_waitcnt lgkmcnt(0)" : "=s"(t0) :: "memory");
  asm volatile("global_load_dword %0, %1, %2\n\ts_waitcnt vmcnt(0)" : "=v"(v) : "v"(off), "s"(p) : "memory");
  asm volatile("s_memrealtime %0\n\ts_waitcnt lgkmcnt(0)" : "=s"(t1) :: "memory");
  unsigned long long first = (t1 - t0) * 16;
  unsigned acc = v;
  asm volatile("s_memrealtime %0\n\ts_waitcnt lgkmcnt(0)" : "=s"(t0) :: "memory");
  for (int i = 1; i <= 16; ++i) { off = i * stride;
    asm volatile("global_load_dword %0, %1, %2\n\ts_waitcnt vmcnt(0)" : "=v"(v) : "v"(off), "s"(p) : "memory"); acc += v; }
  asm volatile("s_memrealtime %0\n\ts_waitcnt lgkmcnt(0)" : "=s"(t1) :: "memory");
  unsigned long long d = mode == 1 ? (t1 - t0) : (mode == 2 ? first : 0);
  d = d * 1000 / 16;
  unsigned long long s0 = t1, s1 = t1;
  while (s1 - s0 < d) { asm volatile("s_sleep 8\n\ts_memrealtime %0\n\ts_waitcnt lgkmcnt(0)" : "=s"(s1) :: "memory"); }
  if (acc == 0x12345678) sink[0] = acc;
}

extern "C" void kernel_launch(void* const* d_in, const int* in_sizes, int n_in, void* d_out, int out_size, void* d_ws, size_t ws_size, hipStream_t stream) {
  const u16* x = (const u16*)d_in[0]; const u16* kc = (const u16*)d_in[1]; const u16* vc = (const u16*)d_in[2];
  const u16* wq = (const u16*)d_in[3]; const u16* wo = (const u16*)d_in[4];
  const u16* wr = (const u16*)d_in[6]; const u16* wg = (const u16*)d_in[7]; const u16* wu = (const u16*)d_in[8]; const u16* wd = (const u16*)d_in[9];
  char* ws = (char*)d_ws;
  u16* xn = (u16*)ws; u16* q = (u16*)(ws + (4 << 20)); u16* o = (u16*)(ws + (24 << 20)); u16* s = (u16*)(ws + (44 << 20));
  float* r = (float*)(ws + (48 << 20)); float* y = (float*)(ws + (49 << 20)); int* idx = (int*)(ws + (50 << 20));
  k_probe<<<1, 64, 0, stream>>>((const char*)(wq), 4352, 0, (unsigned*)(ws + (60 << 20)));
  k_ln<<<S, 64, 0, stream>>>(x, wr, xn, r);
  k_gemm<<<dim3(H * DH / 16, S / 16), 256, 0, stream>>>(xn, wq, q, S, H * DH, D);
  k_attn<<<dim3(S, H), 256, 0, stream>>>(q, kc, vc, o);
  k_gemm<<<dim3(D / 16, S / 16), 256, 0, stream>>>(o, wo, s, S, D, H * DH);
  k_ffn<<<1, 1024, 0, stream>>>(r, xn, wg, wu, wd, y, idx);
  k_final<<<S * D / 256, 256, 0, stream>>>(x, xn, s, y, idx, (u16*)d_out);
}
```

```cpp
#include <hip/hip_runtime.h>

__global__ __launch_bounds__(256, 1) void k_fused(void* p_x, unsigned n_x, unsigned f_x, void* p_wq, unsigned n_wq, unsigned f_wq, void* p_k, unsigned n_k, unsigned f_k, void* p_v, unsigned n_v, unsigned f_v, void* p_wo, unsigned n_wo, unsigned f_wo, void* p_ws, unsigned n_ws, unsigned f_ws, void* p_out, unsigned n_out, unsigned f_out, void* p_wg, unsigned n_wg, unsigned f_wg, void* p_wu, unsigned n_wu, unsigned f_wu, void* p_wd, unsigned n_wd, unsigned f_wd, void* p_wr, unsigned n_wr, unsigned f_wr) {
  __shared__ __attribute__((aligned(16))) char lds[163840];
  const unsigned w = __builtin_amdgcn_readfirstlane(threadIdx.x >> 6);
  asm volatile(
"L_entry:\n"
"s_load_dwordx16 s[36:51], %0, 0\n"
"s_load_dwordx16 s[52:67], %0, 64\n"
"s_load_dwordx8 s[68:75], %0, 128\n"
"s_load_dwordx4 s[76:79], %0, 160\n"
"s_mov_b32 s11, %1\n"
"s_mov_b32 s10, %2\n"
"v_mbcnt_lo_u32_b32 v242, -1, 0\n"
"v_mbcnt_hi_u32_b32 v242, -1, v242\n"
"v_lshrrev_b32 v40, 4, v242\n"
"s_getpc_b64 s[24:25]\n"
"L_pc:\n"
"s_mov_b32 s26, L_end-L_pc\n"
"s_mov_b32 s27, 0x20000\n"
"s_lshr_b32 s21, s11, 3\n"
"s_lshl2_add_u32 s21, s21, s10\n"
"s_lshl_b32 s21, s21, 9\n"
"v_lshl_add_u32 v49, v40, 7, s21\n"
"buffer_load_dword a171, v49, s[24:27], 0 offen\n"
"s_and_b32 s31, s11, 7\n"
"s_bfe_u32 s85, s11, 0x30003\n"
"s_lshr_b32 s84, s11, 6\n"
"s_and_b32 s21, s11, 3\n"
"s_lshl3_add_u32 s12, s21, s85\n"
"s_and_b32 s21, s11, 4\n"
"s_add_u32 s13, s21, s84\n"
"s_mov_b32 s15, 0\n"
"s_lshl2_add_u32 s17, s13, s10\n"
"s_lshl_b32 s17, s17, 16\n"
"s_lshl_b32 s18, s10, 17\n"
"s_lshl_b32 s21, s12, 8\n"
"s_add_u32 s18, s18, s21\n"
"s_lshl_b32 s28, s10, 13\n"
"s_add_u32 s28, s28, 81920\n"
"s_lshl_b32 s90, s10, 12\n"
"s_add_u32 s29, s90, 32768\n"
"s_lshl_b32 s21, s13, 5\n"
"s_add_u32 s21, s21, s12\n"
"s_lshl2_add_u32 s21, s21, s10\n"
"s_lshl_b32 s94, s21, 1\n"
"s_lshl_b32 s95, s94, 10\n"
"v_lshlrev_b32 v243, 4, v242\n"
"v_and_b32 v41, 15, v242\n"
"v_lshrrev_b32 v42, 3, v242\n"
"v_and_b32 v43, 7, v242\n"
"v_and_b32 v44, 0x1f0, v243\n"
"v_lshrrev_b32 v46, 5, v242\n"
"v_lshl_add_u32 v45, v46, 10, v44\n"
"v_lshlrev_b32 v53, 1, v40\n"
"v_xor_b32 v53, v53, v41\n"
"v_lshlrev_b32 v53, 4, v53\n"
"v_lshl_add_u32 v234, v40, 13, v53\n"
"v_xor_b32 v48, 128, v234\n"
"v_add_u32 v235, 31744, v234\n"
"v_add_u32 v236, 63488, v48\n"
"v_add_u32 v237, 95232, v48\n"
"v_xor_b32 v47, v40, v43\n"
"v_lshlrev_b32 v47, 4, v47\n"
"v_lshl_add_u32 v230, v42, 10, v47\n"
"v_xor_b32 v48, 64, v230\n"
"v_add_u32 v231, 7168, v48\n"
"v_add_u32 v232, 14336, v230\n"
"v_add_u32 v233, 21504, v48\n"
"s_add_u32 s22, s15, 0\n"
"s_and_b32 s22, s22, 7\n"
"s_lshl_b32 s23, s22, 19\n"
"s_add_u32 s23, s23, s18\n"
"s_lshl_b32 s20, s22, 7\n"
"s_add_u32 s20, s20, s17\n"
"s_waitcnt lgkmcnt(0)\n"
"buffer_load_dwordx4 v[128:131], v45, s[36:39], s95 offen\n"
"buffer_load_dwordx4 v[132:135], v45, s[36:39], s95 offen offset:512\n"
"s_load_dword s14, s[56:57], 0\n"
"buffer_load_dwordx4 v[136:139], v44, s[76:79], 0 offen\n"
"buffer_load_dwordx4 v[140:143], v44, s[76:79], 0 offen offset:512\n"
"s_add_u32 m0, s29, 16384\n"
"s_nop 0\n"
"buffer_load_dwordx4 v234, s[40:43], s23 offen offset:0 lds\n"
"buffer_load_dwordx4 v235, s[40:43], s23 offen offset:1024 lds\n"
"buffer_load_dwordx4 v236, s[40:43], s23 offen offset:2048 lds\n"
"buffer_load_dwordx4 v237, s[40:43], s23 offen offset:3072 lds\n"
"s_lshl_b32 s96, s12, 19\n"
"s_and_b32 s97, s11, 1\n"
"s_lshl_b32 s97, s97, 21\n"
"s_mov_b32 s98, 2097152\n"
"s_cmp_lg_u32 s10, 0\n"
"s_cbranch_scc1 L_t1\n"
"s_buffer_load_dword s99, s[44:47], s96\n"
"s_buffer_load_dword s99, s[48:51], s96\n"
"L_t1:\n"
"s_cmp_lg_u32 s10, 1\n"
"s_cbranch_scc1 L_t2\n"
"s_buffer_load_dword s99, s[40:43], s98\n"
"s_buffer_load_dword s99, s[52:55], s97\n"
"L_t2:\n"
"s_cmp_lg_u32 s10, 2\n"
"s_cbranch_scc1 L_t3\n"
"s_buffer_load_dword s99, s[64:67], 0\n"
"s_buffer_load_dword s99, s[68:71], 0\n"
"L_t3:\n"
"s_cmp_lg_u32 s10, 3\n"
"s_cbranch_scc1 L_t4\n"
"s_buffer_load_dword s99, s[72:75], 0\n"
"L_t4:\n"
"s_add_u32 m0, s28, 0\n"
"s_nop 0\n"
"buffer_load_dwordx4 v230, s[36:39], s20 offen offset:0 lds\n"
"buffer_load_dwordx4 v231, s[36:39], s20 offen offset:1024 lds\n"
"buffer_load_dwordx4 v232, s[36:39], s20 offen offset:2048 lds\n"
"buffer_load_dwordx4 v233, s[36:39], s20 offen offset:3072 lds\n"
"s_add_u32 m0, s28, 4096\n"
"s_add_u32 s20, s20, 32768\n"
"buffer_load_dwordx4 v230, s[36:39], s20 offen offset:0 lds\n"
"buffer_load_dwordx4 v231, s[36:39], s20 offen offset:1024 lds\n"
"buffer_load_dwordx4 v232, s[36:39], s20 offen offset:2048 lds\n"
"buffer_load_dwordx4 v233, s[36:39], s20 offen offset:3072 lds\n"
"s_and_b32 s21, s11, 1\n"
"s_lshr_b32 s24, s31, 1\n"
"s_lshl2_add_u32 s87, s24, s84\n"
"s_lshl1_add_u32 s87, s87, s21\n"
"s_lshl_b32 s87, s87, 6\n"
"s_lshl2_add_u32 s86, s21, s10\n"
"s_lshl2_add_u32 s86, s86, s24\n"
"s_lshl_b32 s86, s86, 2\n"
"s_mul_i32 s16, s84, 8\n"
"s_lshr_b32 s21, s31, 2\n"
"s_mul_i32 s21, s21, 4\n"
"s_add_u32 s16, s16, s21\n"
"s_and_b32 s16, s16, 15\n"
"s_lshl_b32 s16, s16, 13\n"
"s_lshl_b32 s19, s12, 19\n"
"s_lshl_b32 s30, s10, 11\n"
"s_add_u32 s19, s19, s30\n"
"s_add_u32 s19, s19, 393216\n"
"s_add_u32 s19, s19, s16\n"
"s_lshl_b32 s89, s86, 15\n"
"s_lshl_b32 s21, s85, 7\n"
"s_add_u32 s89, s89, s21\n"
"s_lshr_b32 s88, s12, 3\n"
"s_lshl_b32 s88, s88, 21\n"
"s_add_u32 s88, s88, s17\n"
"s_lshl_b32 s21, s85, 12\n"
"s_add_u32 s88, s88, s21\n"
"s_add_u32 s88, s88, 1048576\n"
"s_lshr_b32 s91, s17, 8\n"
"s_lshl2_add_u32 s91, s12, s91\n"
"s_add_u32 s91, s91, 4096\n"
"s_mov_b64 s[92:93], 0\n"
"s_and_b32 s96, s11, 3\n"
"s_lshr_b32 s97, s11, 2\n"
"s_lshl2_add_u32 s2, s96, s10\n"
"s_mul_i32 s2, s2, 88064\n"
"s_lshl_b32 s21, s97, 6\n"
"s_add_u32 s2, s2, s21\n"
"s_cmp_lt_u32 s11, 172\n"
"s_cselect_b32 s3, 0, 2130706432\n"
"s_lshl_b32 s4, s96, 8\n"
"s_mul_i32 s6, s96, 44032\n"
"s_lshl_b32 s21, s97, 10\n"
"s_add_u32 s6, s6, s21\n"
"s_lshr_b32 s96, s10, 1\n"
"s_and_b32 s97, s10, 1\n"
"s_lshl_b32 s5, s96, 13\n"
"s_lshl_b32 s21, s97, 5\n"
"s_add_u32 s5, s5, s21\n"
"s_add_u32 s5, s5, 147456\n"
"s_lshl_b32 s21, s97, 9\n"
"s_add_u32 s6, s6, s21\n"
"s_lshl4_add_u32 s6, s96, s6\n"
"s_add_u32 s6, s6, 65536\n"
"s_lshr_b32 s96, s11, 5\n"
"s_bfe_u32 s97, s11, 0x20003\n"
"s_lshl2_add_u32 s97, s31, s97\n"
"s_lshl2_add_u32 s32, s96, s10\n"
"s_mul_i32 s32, s32, 1376\n"
"s_add_u32 s7, s32, s97\n"
"s_lshl_b32 s7, s7, 5\n"
"s_add_u32 s32, s32, 65536\n"
"s_lshl_b32 s33, s96, 12\n"
"s_lshl_b32 s21, s97, 7\n"
"s_add_u32 s33, s33, s21\n"
"s_add_u32 s33, s33, 262144\n"
"s_cmp_eq_u32 s10, 0\n"
"s_cselect_b32 s0, 0, 2130706432\n"
"s_add_u32 s1, s90, 147456\n"
"v_lshl_add_u32 v240, v40, 8, v53\n"
"v_bfe_u32 v48, v242, 2, 2\n"
"v_and_b32 v49, 3, v242\n"
"v_bfe_u32 v50, v242, 2, 3\n"
"s_lshl_b32 s21, s10, 1\n"
"v_lshl_add_u32 v51, v40, 3, v48\n"
"v_xor_b32 v52, s21, v50\n"
"v_lshlrev_b32 v52, 5, v52\n"
"v_lshl_add_u32 v52, v49, 3, v52\n"
"v_lshl_add_u32 v216, v51, 8, v52\n"
"v_add_u32 v216, 32768, v216\n"
"v_xor_b32 v217, 32, v216\n"
"v_xor_b32 v218, 64, v216\n"
"v_xor_b32 v219, 96, v216\n"
"v_xor_b32 v220, 128, v216\n"
"v_xor_b32 v221, 160, v216\n"
"v_xor_b32 v222, 192, v216\n"
"v_xor_b32 v223, 224, v216\n"
"v_lshrrev_b32 v51, 1, v41\n"
"v_xor_b32 v51, v51, v40\n"
"v_lshlrev_b32 v51, 4, v51\n"
"v_lshl_add_u32 v51, v41, 7, v51\n"
"v_add_u32 v228, s28, v51\n"
"v_xor_b32 v229, 64, v228\n"
"v_bfrev_b32 v51, v40\n"
"v_lshrrev_b32 v51, 30, v51\n"
"s_lshl_b32 s21, s10, 2\n"
"v_or_b32 v51, s21, v51\n"
"v_xor_b32 v51, v51, v41\n"
"v_lshlrev_b32 v51, 4, v51\n"
"v_lshl_add_u32 v224, v41, 8, v51\n"
"v_xor_b32 v225, 64, v224\n"
"v_xor_b32 v226, 128, v224\n"
"v_xor_b32 v227, 192, v224\n"
"s_and_b32 s21, s10, 1\n"
"s_lshl_b32 s21, s21, 3\n"
"v_or_b32 v51, s21, v40\n"
"v_xor_b32 v51, v51, v41\n"
"v_lshlrev_b32 v51, 4, v51\n"
"v_lshl_add_u32 v238, v40, 8, v51\n"
"v_xor_b32 v239, 64, v238\n"
"v_xor_b32 v241, 128, v240\n"
"v_cmp_eq_u32 vcc, 0, v49\n"
"v_mov_b32 v51, 0x3f803f80\n"
"v_mov_b32 v192, 0\n"
"v_cndmask_b32 v51, 0, v51, vcc\n"
"v_accvgpr_write_b32 a252, v51\n"
"v_accvgpr_write_b32 a253, v51\n"
"v_accvgpr_write_b32 a254, v51\n"
"v_accvgpr_write_b32 a255, v51\n"
"v_accvgpr_write_b32 a97, 0\n"
"v_accvgpr_write_b32 a98, 0\n"
"v_accvgpr_write_b32 a99, 0\n"
"v_accvgpr_write_b32 a101, 0\n"
"v_accvgpr_write_b32 a102, 0\n"
"v_accvgpr_write_b32 a103, 0\n"
"v_accvgpr_write_b32 a105, 0\n"
"v_accvgpr_write_b32 a106, 0\n"
"v_accvgpr_write_b32 a107, 0\n"
"v_accvgpr_write_b32 a109, 0\n"
"v_accvgpr_write_b32 a110, 0\n"
"v_accvgpr_write_b32 a111, 0\n"
"v_mov_b32 v177, 0\n"
"v_mov_b32 v179, 0\n"
"v_mov_b32 v181, 0\n"
"v_mov_b32 v183, 0\n"
"v_mov_b32 v185, 0\n"
"v_mov_b32 v187, 0\n"
"v_mov_b32 v189, 0\n"
"v_mov_b32 v191, 0\n"
"v_mov_b32 v193, 0\n"
"s_waitcnt vmcnt(14)\n"
"v_lshlrev_b32 v144, 16, v128\n"
"v_and_b32 v145, 0xffff0000, v128\n"
"s_add_u32 s22, s15, 1\n"
"v_lshlrev_b32 v146, 16, v129\n"
"v_and_b32 v147, 0xffff0000, v129\n"
"v_lshlrev_b32 v148, 16, v130\n"
"s_and_b32 s22, s22, 7\n"
"v_and_b32 v149, 0xffff0000, v130\n"
"v_lshlrev_b32 v150, 16, v131\n"
"v_and_b32 v151, 0xffff0000, v131\n"
"s_lshl_b32 s23, s22, 19\n"
"v_lshlrev_b32 v152, 16, v132\n"
"v_and_b32 v153, 0xffff0000, v132\n"
"v_lshlrev_b32 v154, 16, v133\n"
"s_add_u32 s23, s23, s18\n"
"v_and_b32 v155, 0xffff0000, v133\n"
"v_lshlrev_b32 v156, 16, v134\n"
"v_and_b32 v157, 0xffff0000, v134\n"
"s_add_u32 m0, s29, 32768\n"
"v_lshlrev_b32 v158, 16, v135\n"
"v_and_b32 v159, 0xffff0000, v135\n"
"v_add_f32 v160, v144, v145\n"
"buffer_load_dwordx4 v234, s[40:43], s23 offen offset:0 lds\n"
"v_add_f32 v161, v146, v147\n"
"v_add_f32 v162, v148, v149\n"
"v_add_f32 v163, v150, v151\n"
"buffer_load_dwordx4 v235, s[40:43], s23 offen offset:1024 lds\n"
"v_add_f32 v164, v152, v153\n"
"v_add_f32 v165, v154, v155\n"
"v_add_f32 v166, v156, v157\n"
"buffer_load_dwordx4 v236, s[40:43], s23 offen offset:2048 lds\n"
"v_add_f32 v167, v158, v159\n"
"v_add_f32 v160, v160, v164\n"
"v_add_f32 v161, v161, v165\n"
"buffer_load_dwordx4 v237, s[40:43], s23 offen offset:3072 lds\n"
"v_add_f32 v162, v162, v166\n"
"v_add_f32 v163, v163, v167\n"
"v_add_f32 v160, v160, v162\n"
"s_add_u32 s22, s15, 1\n"
"v_add_f32 v161, v161, v163\n"
"v_add_f32 v160, v160, v161\n"
"s_nop 1\n"
"v_add_f32_dpp v160, v160, v160 quad_perm:[1,0,3,2] row_mask:0xf bank_mask:0xf\n"
"s_and_b32 s22, s22, 7\n"
"s_nop 0\n"
"v_add_f32_dpp v160, v160, v160 quad_perm:[2,3,0,1] row_mask:0xf bank_mask:0xf\n"
"s_nop 1\n"
"v_add_f32_dpp v160, v160, v160 row_half_mirror row_mask:0xf bank_mask:0xf\n"
"s_nop 1\n"
"v_add_f32_dpp v160, v160, v160 row_mirror row_mask:0xf bank_mask:0xf\n"
"s_lshl_b32 s20, s22, 7\n"
"v_mov_b32 v161, v160\n"
"s_nop 1\n"
"v_permlane16_swap_b32 v160, v161\n"
"v_add_f32 v160, v160, v161\n"
"s_add_u32 s20, s20, s17\n"
"v_mul_f32 v160, 0x3b000000, v160\n"
"v_sub_f32 v168, v144, v160\n"
"v_sub_f32 v169, v145, v160\n"
"s_add_u32 m0, s28, 32768\n"
"v_sub_f32 v170, v146, v160\n"
"v_sub_f32 v171, v147, v160\n"
"v_sub_f32 v172, v148, v160\n"
"buffer_load_dwordx4 v230, s[36:39], s20 offen offset:0 lds\n"
"v_sub_f32 v173, v149, v160\n"
"v_sub_f32 v174, v150, v160\n"
"v_sub_f32 v175, v151, v160\n"
"buffer_load_dwordx4 v231, s[36:39], s20 offen offset:1024 lds\n"
"v_mul_f32 v162, v168, v168\n"
"v_mul_f32 v163, v169, v169\n"
"v_fmac_f32 v162, v170, v170\n"
"buffer_load_dwordx4 v232, s[36:39], s20 offen offset:2048 lds\n"
"v_fmac_f32 v163, v171, v171\n"
"v_fmac_f32 v162, v172, v172\n"
"v_fmac_f32 v163, v173, v173\n"
"buffer_load_dwordx4 v233, s[36:39], s20 offen offset:3072 lds\n"
"v_fmac_f32 v162, v174, v174\n"
"v_fmac_f32 v163, v175, v175\n"
"v_sub_f32 v168, v152, v160\n"
"s_add_u32 m0, s28, 36864\n"
"v_sub_f32 v169, v153, v160\n"
"v_sub_f32 v170, v154, v160\n"
"v_sub_f32 v171, v155, v160\n"
"s_add_u32 s20, s20, 32768\n"
"v_sub_f32 v172, v156, v160\n"
"v_sub_f32 v173, v157, v160\n"
"v_sub_f32 v174, v158, v160\n"
"buffer_load_dwordx4 v230, s[36:39], s20 offen offset:0 lds\n"
"v_sub_f32 v175, v159, v160\n"
"v_fmac_f32 v162, v168, v168\n"
"v_fmac_f32 v163, v169, v169\n"
"buffer_load_dwordx4 v231, s[36:39], s20 offen offset:1024 lds\n"
"v_fmac_f32 v162, v170, v170\n"
"v_fmac_f32 v163, v171, v171\n"
"v_fmac_f32 v162, v172, v172\n"
"buffer_load_dwordx4 v232, s[36:39], s20 offen offset:2048 lds\n"
"v_fmac_f32 v163, v173, v173\n"
"v_fmac_f32 v162, v174, v174\n"
"v_fmac_f32 v163, v175, v175\n"
"buffer_load_dwordx4 v233, s[36:39], s20 offen offset:3072 lds\n"
"v_add_f32 v162, v162, v163\n"
"s_nop 1\n"
"v_add_f32_dpp v162, v162, v162 quad_perm:[1,0,3,2] row_mask:0xf bank_mask:0xf\n"
"s_nop 1\n"
"v_add_f32_dpp v162, v162, v162 quad_perm:[2,3,0,1] row_mask:0xf bank_mask:0xf\n"
"s_nop 1\n"
"v_add_f32_dpp v162, v162, v162 row_half_mirror row_mask:0xf bank_mask:0xf\n"
"s_nop 1\n"
"v_add_f32_dpp v162, v162, v162 row_mirror row_mask:0xf bank_mask:0xf\n"
"v_mov_b32 v161, v162\n"
"s_nop 1\n"
"v_permlane16_swap_b32 v162, v161\n"
"v_add_f32 v162, v162, v161\n"
"v_mul_f32 v162, 0x3b000000, v162\n"
"s_waitcnt vmcnt(12)\n"
"s_barrier\n"
"ds_read_b128 a[32:35], v228 offset:0\n"
"ds_read_b128 a[36:39], v229 offset:0\n"
"ds_read_b64_tr_b16 a[0:1], v216 offset:16384\n"
"ds_read_b64_tr_b16 a[2:3], v216 offset:17408\n"
"ds_read_b64_tr_b16 a[4:5], v216 offset:24576\n"
"ds_read_b64_tr_b16 a[6:7], v216 offset:25600\n"
"ds_read_b128 a[40:43], v228 offset:2048\n"
"ds_read_b128 a[44:47], v229 offset:2048\n"
"ds_read_b128 a[48:51], v228 offset:4096\n"
"ds_read_b128 a[52:55], v229 offset:4096\n"
"ds_read_b128 a[56:59], v228 offset:6144\n"
"ds_read_b128 a[60:63], v229 offset:6144\n"
"ds_read_b64_tr_b16 a[8:9], v217 offset:16384\n"
"ds_read_b64_tr_b16 a[10:11], v217 offset:17408\n"
"ds_read_b64_tr_b16 a[12:13], v217 offset:24576\n"
"ds_read_b64_tr_b16 a[14:15], v217 offset:25600\n"
"ds_read_b64_tr_b16 a[16:17], v218 offset:16384\n"
"ds_read_b64_tr_b16 a[18:19], v218 offset:17408\n"
"ds_read_b64_tr_b16 a[20:21], v218 offset:24576\n"
"ds_read_b64_tr_b16 a[22:23], v218 offset:25600\n"
"s_waitcnt lgkmcnt(14)\n"
"v_mfma_f32_16x16x32_bf16 v[0:3], a[0:3], a[32:35], 0\n"
"ds_read_b64_tr_b16 a[24:25], v219 offset:16384\n"
"ds_read_b64_tr_b16 a[26:27], v219 offset:17408\n"
"v_mfma_f32_16x16x32_bf16 v[0:3], a[4:7], a[36:39], v[0:3]\n"
"s_add_u32 s22, s15, 2\n"
"s_and_b32 s22, s22, 7\n"
"s_lshl_b32 s23, s22, 19\n"
"v_cvt_pk_bf16_f32 v163, v160, v162\n"
"s_waitcnt lgkmcnt(14)\n"
"v_mfma_f32_16x16x32_bf16 v[32:35], a[4:7], a[44:47], 0\n"
"s_add_u32 s23, s23, s18\n"
"s_add_u32 m0, s29, 0\n"
"ds_read_b64_tr_b16 a[28:29], v219 offset:24576\n"
"v_mfma_f32_16x16x32_bf16 v[32:35], a[0:3], a[40:43], v[32:35]\n"
"buffer_load_dwordx4 v234, s[40:43], s23 offen offset:0 lds\n"
"s_waitcnt lgkmcnt(13)\n"
"v_mfma_f32_16x16x32_bf16 v[64:67], a[0:3], a[48:51], 0\n"
"ds_read_b64_tr_b16 a[30:31], v219 offset:25600\n"
"v_lshlrev_b32 v164, 16, v163\n"
"v_mfma_f32_16x16x32_bf16 v[64:67], a[4:7], a[52:55], v[64:67]\n"
"s_waitcnt lgkmcnt(12)\n"
"v_and_b32 v162, 0xffff0000, v163\n"
"v_add_f32 v162, 0x3727c5ac, v162\n"
"v_mfma_f32_16x16x32_bf16 v[96:99], a[4:7], a[60:63], 0\n"
"v_cvt_pk_bf16_f32 v162, v162, v162\n"
"v_lshlrev_b32 v162, 16, v162\n"
"v_sqrt_f32 v162, v162\n"
"v_mfma_f32_16x16x32_bf16 v[96:99], a[0:3], a[56:59], v[96:99]\n"
"buffer_load_dwordx4 v235, s[40:43], s23 offen offset:1024 lds\n"
"v_mfma_f32_16x16x32_bf16 v[196:199], a[0:3], a[252:255], 0\n"
"v_sub_f32 v144, v144, v164\n"
"v_sub_f32 v145, v145, v164\n"
"v_mfma_f32_16x16x32_bf16 v[196:199], a[4:7], a[252:255], v[196:199]\n"
"s_waitcnt lgkmcnt(8)\n"
"v_sub_f32 v146, v146, v164\n"
"v_sub_f32 v147, v147, v164\n"
"v_mfma_f32_16x16x32_bf16 v[4:7], a[8:11], a[32:35], 0\n"
"ds_read_b64_tr_b16 a[0:1], v220 offset:16384\n"
"ds_read_b64_tr_b16 a[2:3], v220 offset:17408\n"
"v_mfma_f32_16x16x32_bf16 v[4:7], a[12:15], a[36:39], v[4:7]\n"
"buffer_load_dwordx4 v236, s[40:43], s23 offen offset:2048 lds\n"
"v_mfma_f32_16x16x32_bf16 v[36:39], a[12:15], a[44:47], 0\n"
"ds_read_b64_tr_b16 a[4:5], v220 offset:24576\n"
"ds_read_b64_tr_b16 a[6:7], v220 offset:25600\n"
"v_mfma_f32_16x16x32_bf16 v[36:39], a[8:11], a[40:43], v[36:39]\n"
"v_sub_f32 v148, v148, v164\n"
"v_sub_f32 v149, v149, v164\n"
"v_mfma_f32_16x16x32_bf16 v[68:71], a[8:11], a[48:51], 0\n"
"v_sub_f32 v150, v150, v164\n"
"v_sub_f32 v151, v151, v164\n"
"v_mfma_f32_16x16x32_bf16 v[68:71], a[12:15], a[52:55], v[68:71]\n"
"buffer_load_dwordx4 v237, s[40:43], s23 offen offset:3072 lds\n"
"v_mfma_f32_16x16x32_bf16 v[100:103], a[12:15], a[60:63], 0\n"
"s_add_u32 s22, s15, 2\n"
"s_and_b32 s22, s22, 7\n"
"s_lshl_b32 s20, s22, 7\n"
"v_mfma_f32_16x16x32_bf16 v[100:103], a[8:11], a[56:59], v[100:103]\n"
"s_add_u32 s20, s20, s17\n"
"s_add_u32 m0, s28, 0\n"
"v_sub_f32 v152, v152, v164\n"
"v_mfma_f32_16x16x32_bf16 v[200:203], a[8:11], a[252:255], 0\n"
"s_waitcnt lgkmcnt(8)\n"
"v_sub_f32 v153, v153, v164\n"
"v_sub_f32 v154, v154, v164\n"
"v_mfma_f32_16x16x32_bf16 v[200:203], a[12:15], a[252:255], v[200:203]\n"
"buffer_load_dwordx4 v230, s[36:39], s20 offen offset:0 lds\n"
"s_mov_b64 exec, 0\n"
"s_cmp_eq_u32 s10, 0\n"
"s_cbranch_scc1 L_ic0\n"
"s_cmp_eq_u32 s10, 1\n"
"s_cbranch_scc1 L_ic1\n"
"s_cmp_eq_u32 s10, 2\n"
"s_cbranch_scc1 L_ic2\n"
"s_branch L_ic3\n"
"L_ic_ret:\n"
"s_mov_b64 exec, -1\n"
"v_mfma_f32_16x16x32_bf16 v[8:11], a[16:19], a[32:35], 0\n"
"ds_read_b64_tr_b16 a[8:9], v221 offset:16384\n"
"ds_read_b64_tr_b16 a[10:11], v221 offset:17408\n"
"v_mfma_f32_16x16x32_bf16 v[8:11], a[20:23], a[36:39], v[8:11]\n"
"ds_read_b64_tr_b16 a[12:13], v221 offset:24576\n"
"ds_read_b64_tr_b16 a[14:15], v221 offset:25600\n"
"v_mfma_f32_16x16x32_bf16 v[40:43], a[20:23], a[44:47], 0\n"
"v_sub_f32 v155, v155, v164\n"
"v_sub_f32 v156, v156, v164\n"
"v_mfma_f32_16x16x32_bf16 v[40:43], a[16:19], a[40:43], v[40:43]\n"
"buffer_load_dwordx4 v231, s[36:39], s20 offen offset:1024 lds\n"
"v_mfma_f32_16x16x32_bf16 v[72:75], a[16:19], a[48:51], 0\n"
"v_sub_f32 v157, v157, v164\n"
"v_sub_f32 v158, v158, v164\n"
"v_mfma_f32_16x16x32_bf16 v[72:75], a[20:23], a[52:55], v[72:75]\n"
"v_sub_f32 v159, v159, v164\n"
"v_cvt_pk_bf16_f32 v166, v160, v162\n"
"v_mfma_f32_16x16x32_bf16 v[104:107], a[20:23], a[60:63], 0\n"
"s_waitcnt lgkmcnt(8)\n"
"v_and_b32 v162, 0xffff0000, v166\n"
"v_rcp_f32 v162, v162\n"
"v_mfma_f32_16x16x32_bf16 v[104:107], a[16:19], a[56:59], v[104:107]\n"
"buffer_load_dwordx4 v232, s[36:39], s20 offen offset:2048 lds\n"
"v_mfma_f32_16x16x32_bf16 v[12:15], a[24:27], a[32:35], 0\n"
"ds_read_b64_tr_b16 a[16:17], v222 offset:16384\n"
"ds_read_b64_tr_b16 a[18:19], v222 offset:17408\n"
"v_mfma_f32_16x16x32_bf16 v[12:15], a[28:31], a[36:39], v[12:15]\n"
"ds_read_b64_tr_b16 a[20:21], v222 offset:24576\n"
"ds_read_b64_tr_b16 a[22:23], v222 offset:25600\n"
"v_mfma_f32_16x16x32_bf16 v[44:47], a[28:31], a[44:47], 0\n"
"v_cvt_pk_bf16_f32 v128, v144, v145\n"
"v_cvt_pk_bf16_f32 v129, v146, v147\n"
"v_mfma_f32_16x16x32_bf16 v[44:47], a[24:27], a[40:43], v[44:47]\n"
"buffer_load_dwordx4 v233, s[36:39], s20 offen offset:3072 lds\n"
"v_mfma_f32_16x16x32_bf16 v[76:79], a[24:27], a[48:51], 0\n"
"s_add_u32 m0, s28, 4096\n"
"s_add_u32 s20, s20, 32768\n"
"v_cvt_pk_bf16_f32 v130, v148, v149\n"
"v_mfma_f32_16x16x32_bf16 v[76:79], a[28:31], a[52:55], v[76:79]\n"
"v_cvt_pk_bf16_f32 v131, v150, v151\n"
"v_cvt_pk_bf16_f32 v132, v152, v153\n"
"v_mfma_f32_16x16x32_bf16 v[108:111], a[28:31], a[60:63], 0\n"
"s_waitcnt lgkmcnt(8)\n"
"v_cvt_pk_bf16_f32 v133, v154, v155\n"
"v_cvt_pk_bf16_f32 v134, v156, v157\n"
"v_cvt_pk_bf16_f32 v135, v158, v159\n"
"v_lshlrev_b32 v144, 16, v128\n"
"v_mfma_f32_16x16x32_bf16 v[108:111], a[24:27], a[56:59], v[108:111]\n"
"buffer_load_dwordx4 v230, s[36:39], s20 offen offset:0 lds\n"
"v_mfma_f32_16x16x32_bf16 v[16:19], a[0:3], a[32:35], 0\n"
"ds_read_b64_tr_b16 a[24:25], v223 offset:16384\n"
"ds_read_b64_tr_b16 a[26:27], v223 offset:17408\n"
"v_mfma_f32_16x16x32_bf16 v[16:19], a[4:7], a[36:39], v[16:19]\n"
"ds_read_b64_tr_b16 a[28:29], v223 offset:24576\n"
"ds_read_b64_tr_b16 a[30:31], v223 offset:25600\n"
"v_mfma_f32_16x16x32_bf16 v[48:51], a[4:7], a[44:47], 0\n"
"v_and_b32 v145, 0xffff0000, v128\n"
"v_lshlrev_b32 v146, 16, v129\n"
"v_mfma_f32_16x16x32_bf16 v[48:51], a[0:3], a[40:43], v[48:51]\n"
"buffer_load_dwordx4 v231, s[36:39], s20 offen offset:1024 lds\n"
"v_mfma_f32_16x16x32_bf16 v[80:83], a[0:3], a[48:51], 0\n"
"v_and_b32 v147, 0xffff0000, v129\n"
"v_lshlrev_b32 v148, 16, v130\n"
"v_mfma_f32_16x16x32_bf16 v[80:83], a[4:7], a[52:55], v[80:83]\n"
"v_and_b32 v149, 0xffff0000, v130\n"
"v_lshlrev_b32 v150, 16, v131\n"
"v_mfma_f32_16x16x32_bf16 v[112:115], a[4:7], a[60:63], 0\n"
"s_waitcnt lgkmcnt(8)\n"
"v_and_b32 v151, 0xffff0000, v131\n"
"v_lshlrev_b32 v152, 16, v132\n"
"v_mfma_f32_16x16x32_bf16 v[112:115], a[0:3], a[56:59], v[112:115]\n"
"buffer_load_dwordx4 v232, s[36:39], s20 offen offset:2048 lds\n"
"v_mfma_f32_16x16x32_bf16 v[20:23], a[8:11], a[32:35], 0\n"
"v_and_b32 v153, 0xffff0000, v132\n"
"v_lshlrev_b32 v154, 16, v133\n"
"v_mfma_f32_16x16x32_bf16 v[20:23], a[12:15], a[36:39], v[20:23]\n"
"v_and_b32 v155, 0xffff0000, v133\n"
"v_lshlrev_b32 v156, 16, v134\n"
"v_mfma_f32_16x16x32_bf16 v[52:55], a[12:15], a[44:47], 0\n"
"s_waitcnt vmcnt(11)\n"
"s_waitcnt lgkmcnt(0)\n"
"s_add_u32 s14, s14, 1\n"
"v_mfma_f32_16x16x32_bf16 v[52:55], a[8:11], a[40:43], v[52:55]\n"
"s_barrier\n"
"v_mfma_f32_16x16x32_bf16 v[84:87], a[8:11], a[48:51], 0\n"
"buffer_load_dwordx4 v233, s[36:39], s20 offen offset:3072 lds\n"
"v_mfma_f32_16x16x32_bf16 v[84:87], a[12:15], a[52:55], v[84:87]\n"
"s_add_u32 s22, s15, 3\n"
"s_and_b32 s22, s22, 7\n"
"s_lshl_b32 s23, s22, 19\n"
"v_mfma_f32_16x16x32_bf16 v[116:119], a[12:15], a[60:63], 0\n"
"s_add_u32 s23, s23, s18\n"
"s_add_u32 m0, s29, 16384\n"
"ds_read_b128 a[64:67], v228 offset:32768\n"
"v_mfma_f32_16x16x32_bf16 v[116:119], a[8:11], a[56:59], v[116:119]\n"
"ds_read_b128 a[68:71], v229 offset:32768\n"
"ds_read_b64_tr_b16 a[0:1], v216 offset:32768\n"
"v_mfma_f32_16x16x32_bf16 v[24:27], a[16:19], a[32:35], 0\n"
"buffer_load_dwordx4 v234, s[40:43], s23 offen offset:0 lds\n"
"v_mfma_f32_16x16x32_bf16 v[24:27], a[20:23], a[36:39], v[24:27]\n"
"ds_read_b64_tr_b16 a[2:3], v216 offset:33792\n"
"ds_read_b64_tr_b16 a[4:5], v216 offset:40960\n"
"v_mfma_f32_16x16x32_bf16 v[56:59], a[20:23], a[44:47], 0\n"
"ds_read_b64_tr_b16 a[6:7], v216 offset:41984\n"
"ds_read_b128 a[72:75], v228 offset:34816\n"
"v_mfma_f32_16x16x32_bf16 v[56:59], a[16:19], a[40:43], v[56:59]\n"
"ds_read_b128 a[76:79], v229 offset:34816\n"
"ds_read_b128 a[80:83], v228 offset:36864\n"
"v_mfma_f32_16x16x32_bf16 v[88:91], a[16:19], a[48:51], 0\n"
"buffer_load_dwordx4 v235, s[40:43], s23 offen offset:1024 lds\n"
"v_mfma_f32_16x16x32_bf16 v[88:91], a[20:23], a[52:55], v[88:91]\n"
"ds_read_b128 a[84:87], v229 offset:36864\n"
"ds_read_b128 a[88:91], v228 offset:38912\n"
"v_mfma_f32_16x16x32_bf16 v[120:123], a[20:23], a[60:63], 0\n"
"ds_read_b128 a[92:95], v229 offset:38912\n"
"ds_read_b64_tr_b16 a[8:9], v217 offset:32768\n"
"v_mfma_f32_16x16x32_bf16 v[120:123], a[16:19], a[56:59], v[120:123]\n"
"ds_read_b64_tr_b16 a[10:11], v217 offset:33792\n"
"ds_read_b64_tr_b16 a[12:13], v217 offset:40960\n"
"v_mfma_f32_16x16x32_bf16 v[28:31], a[24:27], a[32:35], 0\n"
"buffer_load_dwordx4 v236, s[40:43], s23 offen offset:2048 lds\n"
"v_mfma_f32_16x16x32_bf16 v[28:31], a[28:31], a[36:39], v[28:31]\n"
"ds_read_b64_tr_b16 a[14:15], v217 offset:41984\n"
"ds_read_b64_tr_b16 a[16:17], v218 offset:32768\n"
"v_mfma_f32_16x16x32_bf16 v[60:63], a[28:31], a[44:47], 0\n"
"ds_read_b64_tr_b16 a[18:19], v218 offset:33792\n"
"ds_read_b64_tr_b16 a[20:21], v218 offset:40960\n"
"v_mfma_f32_16x16x32_bf16 v[60:63], a[24:27], a[40:43], v[60:63]\n"
"ds_read_b64_tr_b16 a[22:23], v218 offset:41984\n"
"v_and_b32 v157, 0xffff0000, v134\n"
"v_lshlrev_b32 v158, 16, v135\n"
"v_and_b32 v159, 0xffff0000, v135\n"
"v_mfma_f32_16x16x32_bf16 v[92:95], a[24:27], a[48:51], 0\n"
"buffer_load_dwordx4 v237, s[40:43], s23 offen offset:3072 lds\n"
"v_mfma_f32_16x16x32_bf16 v[92:95], a[28:31], a[52:55], v[92:95]\n"
"v_mul_f32 v144, v144, v162\n"
"v_mul_f32 v145, v145, v162\n"
"v_mfma_f32_16x16x32_bf16 v[124:127], a[28:31], a[60:63], 0\n"
"v_mul_f32 v146, v146, v162\n"
"v_mul_f32 v147, v147, v162\n"
"v_mfma_f32_16x16x32_bf16 v[124:127], a[24:27], a[56:59], v[124:127]\n"
"s_waitcnt lgkmcnt(15)\n"
"s_waitcnt lgkmcnt(14)\n"
"v_mul_f32 v148, v148, v162\n"
"v_mul_f32 v149, v149, v162\n"
"v_mfma_f32_16x16x32_bf16 v[0:3], a[0:3], a[64:67], v[0:3]\n"
"ds_read_b64_tr_b16 a[24:25], v219 offset:32768\n"
"ds_read_b64_tr_b16 a[26:27], v219 offset:33792\n"
"v_mfma_f32_16x16x32_bf16 v[0:3], a[4:7], a[68:71], v[0:3]\n"
"ds_read_b64_tr_b16 a[28:29], v219 offset:40960\n"
"ds_read_b64_tr_b16 a[30:31], v219 offset:41984\n"
"s_waitcnt lgkmcnt(15)\n"
"v_mfma_f32_16x16x32_bf16 v[32:35], a[4:7], a[76:79], v[32:35]\n"
"v_mul_f32 v150, v150, v162\n"
"v_mul_f32 v151, v151, v162\n"
"v_mfma_f32_16x16x32_bf16 v[32:35], a[0:3], a[72:75], v[32:35]\n"
"s_waitcnt lgkmcnt(14)\n"
"v_mul_f32 v152, v152, v162\n"
"v_mul_f32 v153, v153, v162\n"
"v_mfma_f32_16x16x32_bf16 v[64:67], a[0:3], a[80:83], v[64:67]\n"
"v_mul_f32 v154, v154, v162\n"
"v_mul_f32 v155, v155, v162\n"
"v_mfma_f32_16x16x32_bf16 v[64:67], a[4:7], a[84:87], v[64:67]\n"
"s_waitcnt lgkmcnt(12)\n"
"v_mul_f32 v156, v156, v162\n"
"v_mul_f32 v157, v157, v162\n"
"v_mfma_f32_16x16x32_bf16 v[96:99], a[4:7], a[92:95], v[96:99]\n"
"v_mul_f32 v158, v158, v162\n"
"v_mul_f32 v159, v159, v162\n"
"v_cvt_pk_bf16_f32 v128, v144, v145\n"
"v_mfma_f32_16x16x32_bf16 v[96:99], a[0:3], a[88:91], v[96:99]\n"
"s_add_u32 s22, s15, 3\n"
"s_and_b32 s22, s22, 7\n"
"s_lshl_b32 s20, s22, 7\n"
"v_mfma_f32_16x16x32_bf16 v[196:199], a[0:3], a[252:255], v[196:199]\n"
"s_waitcnt lgkmcnt(8)\n"
"s_add_u32 s20, s20, s17\n"
"s_add_u32 m0, s28, 32768\n"
"v_mfma_f32_16x16x32_bf16 v[196:199], a[4:7], a[252:255], v[196:199]\n"
"buffer_load_dwordx4 v230, s[36:39], s20 offen offset:0 lds\n"
"v_mfma_f32_16x16x32_bf16 v[4:7], a[8:11], a[64:67], v[4:7]\n"
"ds_read_b64_tr_b16 a[0:1], v220 offset:32768\n"
"ds_read_b64_tr_b16 a[2:3], v220 offset:33792\n"
"v_mfma_f32_16x16x32_bf16 v[4:7], a[12:15], a[68:71], v[4:7]\n"
"ds_read_b64_tr_b16 a[4:5], v220 offset:40960\n"
"ds_read_b64_tr_b16 a[6:7], v220 offset:41984\n"
"v_mfma_f32_16x16x32_bf16 v[36:39], a[12:15], a[76:79], v[36:39]\n"
"v_cvt_pk_bf16_f32 v129, v146, v147\n"
"v_cvt_pk_bf16_f32 v130, v148, v149\n"
"v_mfma_f32_16x16x32_bf16 v[36:39], a[8:11], a[72:75], v[36:39]\n"
"buffer_load_dwordx4 v231, s[36:39], s20 offen offset:1024 lds\n"
"v_mfma_f32_16x16x32_bf16 v[68:71], a[8:11], a[80:83], v[68:71]\n"
"v_cvt_pk_bf16_f32 v131, v150, v151\n"
"v_cvt_pk_bf16_f32 v132, v152, v153\n"
"v_mfma_f32_16x16x32_bf16 v[68:71], a[12:15], a[84:87], v[68:71]\n"
"v_cvt_pk_bf16_f32 v133, v154, v155\n"
"v_cvt_pk_bf16_f32 v134, v156, v157\n"
"v_mfma_f32_16x16x32_bf16 v[100:103], a[12:15], a[92:95], v[100:103]\n"
"v_cvt_pk_bf16_f32 v135, v158, v159\n"
"v_lshlrev_b32 v144, 16, v128\n"
"v_mfma_f32_16x16x32_bf16 v[100:103], a[8:11], a[88:91], v[100:103]\n"
"buffer_load_dwordx4 v232, s[36:39], s20 offen offset:2048 lds\n"
"v_mfma_f32_16x16x32_bf16 v[200:203], a[8:11], a[252:255], v[200:203]\n"
"v_and_b32 v145, 0xffff0000, v128\n"
"v_lshlrev_b32 v146, 16, v129\n"
"v_mfma_f32_16x16x32_bf16 v[200:203], a[12:15], a[252:255], v[200:203]\n"
"s_waitcnt lgkmcnt(8)\n"
"v_and_b32 v147, 0xffff0000, v129\n"
"v_lshlrev_b32 v148, 16, v130\n"
"v_mfma_f32_16x16x32_bf16 v[8:11], a[16:19], a[64:67], v[8:11]\n"
"L_ic0:\n"
"s_cbranch_execz L_ic4\n"
"ds_read_b64_tr_b16 a[8:9], v221 offset:32768\n"
"ds_read_b64_tr_b16 a[10:11], v221 offset:33792\n"
"v_mfma_f32_16x16x32_bf16 v[8:11], a[20:23], a[68:71], v[8:11]\n"
"buffer_load_dwordx4 v233, s[36:39], s20 offen offset:3072 lds\n"
"v_mfma_f32_16x16x32_bf16 v[40:43], a[20:23], a[76:79], v[40:43]\n"
"s_add_u32 m0, s28, 36864\n"
"s_add_u32 s20, s20, 32768\n"
"ds_read_b64_tr_b16 a[12:13], v221 offset:40960\n"
"v_mfma_f32_16x16x32_bf16 v[40:43], a[16:19], a[72:75], v[40:43]\n"
"ds_read_b64_tr_b16 a[14:15], v221 offset:41984\n"
"v_and_b32 v149, 0xffff0000, v130\n"
"v_mfma_f32_16x16x32_bf16 v[72:75], a[16:19], a[80:83], v[72:75]\n"
"v_lshlrev_b32 v150, 16, v131\n"
"v_and_b32 v151, 0xffff0000, v131\n"
"v_lshlrev_b32 v152, 16, v132\n"
"v_and_b32 v153, 0xffff0000, v132\n"
"v_mfma_f32_16x16x32_bf16 v[72:75], a[20:23], a[84:87], v[72:75]\n"
"buffer_load_dwordx4 v230, s[36:39], s20 offen offset:0 lds\n"
"v_mfma_f32_16x16x32_bf16 v[104:107], a[20:23], a[92:95], v[104:107]\n"
"v_lshlrev_b32 v154, 16, v133\n"
"v_and_b32 v155, 0xffff0000, v133\n"
"v_mfma_f32_16x16x32_bf16 v[104:107], a[16:19], a[88:91], v[104:107]\n"
"s_waitcnt lgkmcnt(8)\n"
"v_lshlrev_b32 v156, 16, v134\n"
"v_and_b32 v157, 0xffff0000, v134\n"
"v_mfma_f32_16x16x32_bf16 v[12:15], a[24:27], a[64:67], v[12:15]\n"
"ds_read_b64_tr_b16 a[16:17], v222 offset:32768\n"
"ds_read_b64_tr_b16 a[18:19], v222 offset:33792\n"
"v_mfma_f32_16x16x32_bf16 v[12:15], a[28:31], a[68:71], v[12:15]\n"
"buffer_load_dwordx4 v231, s[36:39], s20 offen offset:1024 lds\n"
"v_mfma_f32_16x16x32_bf16 v[44:47], a[28:31], a[76:79], v[44:47]\n"
"ds_read_b64_tr_b16 a[20:21], v222 offset:40960\n"
"ds_read_b64_tr_b16 a[22:23], v222 offset:41984\n"
"v_mfma_f32_16x16x32_bf16 v[44:47], a[24:27], a[72:75], v[44:47]\n"
"v_lshlrev_b32 v158, 16, v135\n"
"v_and_b32 v159, 0xffff0000, v135\n"
"v_mfma_f32_16x16x32_bf16 v[76:79], a[24:27], a[80:83], v[76:79]\n"
"v_lshlrev_b32 v168, 16, v136\n"
"v_and_b32 v169, 0xffff0000, v136\n"
"v_lshlrev_b32 v170, 16, v137\n"
"v_mfma_f32_16x16x32_bf16 v[76:79], a[28:31], a[84:87], v[76:79]\n"
"buffer_load_dwordx4 v232, s[36:39], s20 offen offset:2048 lds\n"
"v_mfma_f32_16x16x32_bf16 v[108:111], a[28:31], a[92:95], v[108:111]\n"
"v_and_b32 v171, 0xffff0000, v137\n"
"v_lshlrev_b32 v172, 16, v138\n"
"v_mfma_f32_16x16x32_bf16 v[108:111], a[24:27], a[88:91], v[108:111]\n"
"s_waitcnt lgkmcnt(8)\n"
"v_and_b32 v173, 0xffff0000, v138\n"
"v_lshlrev_b32 v174, 16, v139\n"
"v_mfma_f32_16x16x32_bf16 v[16:19], a[0:3], a[64:67], v[16:19]\n"
"ds_read_b64_tr_b16 a[24:25], v223 offset:32768\n"
"ds_read_b64_tr_b16 a[26:27], v223 offset:33792\n"
"v_mfma_f32_16x16x32_bf16 v[16:19], a[4:7], a[68:71], v[16:19]\n"
"buffer_load_dwordx4 v233, s[36:39], s20 offen offset:3072 lds\n"
"v_mfma_f32_16x16x32_bf16 v[48:51], a[4:7], a[76:79], v[48:51]\n"
"ds_read_b64_tr_b16 a[28:29], v223 offset:40960\n"
"ds_read_b64_tr_b16 a[30:31], v223 offset:41984\n"
"v_mfma_f32_16x16x32_bf16 v[48:51], a[0:3], a[72:75], v[48:51]\n"
"v_and_b32 v175, 0xffff0000, v139\n"
"v_mul_f32 v160, v144, v168\n"
"v_mfma_f32_16x16x32_bf16 v[80:83], a[0:3], a[80:83], v[80:83]\n"
"v_mul_f32 v161, v145, v169\n"
"v_fmac_f32 v160, v146, v170\n"
"v_mfma_f32_16x16x32_bf16 v[80:83], a[4:7], a[84:87], v[80:83]\n"
"v_fmac_f32 v161, v147, v171\n"
"v_fmac_f32 v160, v148, v172\n"
"v_mfma_f32_16x16x32_bf16 v[112:115], a[4:7], a[92:95], v[112:115]\n"
"v_fmac_f32 v161, v149, v173\n"
"v_fmac_f32 v160, v150, v174\n"
"v_mfma_f32_16x16x32_bf16 v[112:115], a[0:3], a[88:91], v[112:115]\n"
"s_waitcnt lgkmcnt(8)\n"
"v_fmac_f32 v161, v151, v175\n"
"v_lshlrev_b32 v168, 16, v140\n"
"v_mfma_f32_16x16x32_bf16 v[20:23], a[8:11], a[64:67], v[20:23]\n"
"v_and_b32 v169, 0xffff0000, v140\n"
"v_lshlrev_b32 v170, 16, v141\n"
"v_mfma_f32_16x16x32_bf16 v[20:23], a[12:15], a[68:71], v[20:23]\n"
"v_and_b32 v171, 0xffff0000, v141\n"
"v_lshlrev_b32 v172, 16, v142\n"
"v_mfma_f32_16x16x32_bf16 v[52:55], a[12:15], a[76:79], v[52:55]\n"
"s_waitcnt vmcnt(12)\n"
"s_waitcnt lgkmcnt(0)\n"
"v_and_b32 v173, 0xffff0000, v142\n"
"v_mfma_f32_16x16x32_bf16 v[52:55], a[8:11], a[72:75], v[52:55]\n"
"s_barrier\n"
"v_mfma_f32_16x16x32_bf16 v[84:87], a[8:11], a[80:83], v[84:87]\n"
"ds_read_b128 a[32:35], v228 offset:0\n"
"ds_read_b128 a[36:39], v229 offset:0\n"
"v_mfma_f32_16x16x32_bf16 v[84:87], a[12:15], a[84:87], v[84:87]\n"
"s_add_u32 s22, s15, 4\n"
"s_and_b32 s22, s22, 7\n"
"s_lshl_b32 s23, s22, 19\n"
"v_mfma_f32_16x16x32_bf16 v[116:119], a[12:15], a[92:95], v[116:119]\n"
"s_add_u32 s23, s23, s18\n"
"s_add_u32 m0, s29, 32768\n"
"ds_read_b64_tr_b16 a[0:1], v216 offset:0\n"
"v_mfma_f32_16x16x32_bf16 v[116:119], a[8:11], a[88:91], v[116:119]\n"
"buffer_load_dwordx4 v234, s[40:43], s23 offen offset:0 lds\n"
"v_mfma_f32_16x16x32_bf16 v[24:27], a[16:19], a[64:67], v[24:27]\n"
"ds_read_b64_tr_b16 a[2:3], v216 offset:1024\n"
"ds_read_b64_tr_b16 a[4:5], v216 offset:8192\n"
"v_mfma_f32_16x16x32_bf16 v[24:27], a[20:23], a[68:71], v[24:27]\n"
"ds_read_b64_tr_b16 a[6:7], v216 offset:9216\n"
"ds_read_b128 a[40:43], v228 offset:2048\n"
"v_mfma_f32_16x16x32_bf16 v[56:59], a[20:23], a[76:79], v[56:59]\n"
"ds_read_b128 a[44:47], v229 offset:2048\n"
"ds_read_b128 a[48:51], v228 offset:4096\n"
"v_mfma_f32_16x16x32_bf16 v[56:59], a[16:19], a[72:75], v[56:59]\n"
"buffer_load_dwordx4 v235, s[40:43], s23 offen offset:1024 lds\n"
"v_mfma_f32_16x16x32_bf16 v[88:91], a[16:19], a[80:83], v[88:91]\n"
"ds_read_b128 a[52:55], v229 offset:4096\n"
"ds_read_b128 a[56:59], v228 offset:6144\n"
"v_mfma_f32_16x16x32_bf16 v[88:91], a[20:23], a[84:87], v[88:91]\n"
"ds_read_b128 a[60:63], v229 offset:6144\n"
"ds_read_b64_tr_b16 a[8:9], v217 offset:0\n"
"v_mfma_f32_16x16x32_bf16 v[120:123], a[20:23], a[92:95], v[120:123]\n"
"ds_read_b64_tr_b16 a[10:11], v217 offset:1024\n"
"ds_read_b64_tr_b16 a[12:13], v217 offset:8192\n"
"v_mfma_f32_16x16x32_bf16 v[120:123], a[16:19], a[88:91], v[120:123]\n"
"buffer_load_dwordx4 v236, s[40:43], s23 offen offset:2048 lds\n"
"v_mfma_f32_16x16x32_bf16 v[28:31], a[24:27], a[64:67], v[28:31]\n"
"ds_read_b64_tr_b16 a[14:15], v217 offset:9216\n"
"ds_read_b64_tr_b16 a[16:17], v218 offset:0\n"
"v_mfma_f32_16x16x32_bf16 v[28:31], a[28:31], a[68:71], v[28:31]\n"
"ds_read_b64_tr_b16 a[18:19], v218 offset:1024\n"
"ds_read_b64_tr_b16 a[20:21], v218 offset:8192\n"
"v_mfma_f32_16x16x32_bf16 v[60:63], a[28:31], a[76:79], v[60:63]\n"
"ds_read_b64_tr_b16 a[22:23], v218 offset:9216\n"
"v_lshlrev_b32 v174, 16, v143\n"
"v_and_b32 v175, 0xffff0000, v143\n"
"v_fmac_f32 v160, v152, v168\n"
"v_mfma_f32_16x16x32_bf16 v[60:63], a[24:27], a[72:75], v[60:63]\n"
"buffer_load_dwordx4 v237, s[40:43], s23 offen offset:3072 lds\n"
"v_mfma_f32_16x16x32_bf16 v[92:95], a[24:27], a[80:83], v[92:95]\n"
"v_fmac_f32 v161, v153, v169\n"
"v_fmac_f32 v160, v154, v170\n"
"v_mfma_f32_16x16x32_bf16 v[92:95], a[28:31], a[84:87], v[92:95]\n"
"v_fmac_f32 v161, v155, v171\n"
"v_fmac_f32 v160, v156, v172\n"
"v_mfma_f32_16x16x32_bf16 v[124:127], a[28:31], a[92:95], v[124:127]\n"
"v_fmac_f32 v161, v157, v173\n"
"v_fmac_f32 v160, v158, v174\n"
"v_mfma_f32_16x16x32_bf16 v[124:127], a[24:27], a[88:91], v[124:127]\n"
"s_waitcnt lgkmcnt(15)\n"
"s_waitcnt lgkmcnt(14)\n"
"v_fmac_f32 v161, v159, v175\n"
"v_add_f32 v160, v160, v161\n"
"v_mfma_f32_16x16x32_bf16 v[0:3], a[0:3], a[32:35], v[0:3]\n"
"ds_read_b64_tr_b16 a[24:25], v219 offset:0\n"
"ds_read_b64_tr_b16 a[26:27], v219 offset:1024\n"
"v_mfma_f32_16x16x32_bf16 v[0:3], a[4:7], a[36:39], v[0:3]\n"
"ds_read_b64_tr_b16 a[28:29], v219 offset:8192\n"
"ds_read_b64_tr_b16 a[30:31], v219 offset:9216\n"
"s_waitcnt lgkmcnt(15)\n"
"v_mfma_f32_16x16x32_bf16 v[32:35], a[4:7], a[44:47], v[32:35]\n"
"v_add_f32_dpp v160, v160, v160 quad_perm:[1,0,3,2] row_mask:0xf bank_mask:0xf\n"
"v_mfma_f32_16x16x32_bf16 v[32:35], a[0:3], a[40:43], v[32:35]\n"
"s_waitcnt lgkmcnt(14)\n"
"v_mfma_f32_16x16x32_bf16 v[64:67], a[0:3], a[48:51], v[64:67]\n"
"v_add_f32_dpp v160, v160, v160 quad_perm:[2,3,0,1] row_mask:0xf bank_mask:0xf\n"
"v_mfma_f32_16x16x32_bf16 v[64:67], a[4:7], a[52:55], v[64:67]\n"
"s_waitcnt lgkmcnt(12)\n"
"v_mfma_f32_16x16x32_bf16 v[96:99], a[4:7], a[60:63], v[96:99]\n"
"v_add_f32_dpp v160, v160, v160 row_half_mirror row_mask:0xf bank_mask:0xf\n"
"v_mfma_f32_16x16x32_bf16 v[96:99], a[0:3], a[56:59], v[96:99]\n"
"s_add_u32 s22, s15, 4\n"
"s_and_b32 s22, s22, 7\n"
"s_lshl_b32 s20, s22, 7\n"
"v_mfma_f32_16x16x32_bf16 v[196:199], a[0:3], a[252:255], v[196:199]\n"
"s_waitcnt lgkmcnt(8)\n"
"s_add_u32 s20, s20, s17\n"
"s_add_u32 m0, s28, 0\n"
"v_add_f32_dpp v160, v160, v160 row_mirror row_mask:0xf bank_mask:0xf\n"
"v_mfma_f32_16x16x32_bf16 v[196:199], a[4:7], a[252:255], v[196:199]\n"
"buffer_load_dwordx4 v230, s[36:39], s20 offen offset:0 lds\n"
"v_mfma_f32_16x16x32_bf16 v[4:7], a[8:11], a[32:35], v[4:7]\n"
"ds_read_b64_tr_b16 a[0:1], v220 offset:0\n"
"ds_read_b64_tr_b16 a[2:3], v220 offset:1024\n"
"v_mfma_f32_16x16x32_bf16 v[4:7], a[12:15], a[36:39], v[4:7]\n"
"ds_read_b64_tr_b16 a[4:5], v220 offset:8192\n"
"ds_read_b64_tr_b16 a[6:7], v220 offset:9216\n"
"v_mfma_f32_16x16x32_bf16 v[36:39], a[12:15], a[44:47], v[36:39]\n"
"v_mov_b32 v161, v160\n"
"v_mfma_f32_16x16x32_bf16 v[36:39], a[8:11], a[40:43], v[36:39]\n"
"buffer_load_dwordx4 v231, s[36:39], s20 offen offset:1024 lds\n"
"v_mfma_f32_16x16x32_bf16 v[68:71], a[8:11], a[48:51], v[68:71]\n"
"v_permlane16_swap_b32 v160, v161\n"
"v_add_f32 v160, v160, v161\n"
"v_mfma_f32_16x16x32_bf16 v[68:71], a[12:15], a[52:55], v[68:71]\n"
"v_lshrrev_b32 v163, 5, v242\n"
"v_lshlrev_b32 v165, 3, v163\n"
"v_mfma_f32_16x16x32_bf16 v[100:103], a[12:15], a[60:63], v[100:103]\n"
"v_add_u32 v163, s94, v163\n"
"v_sub_u32 v163, 0x7ff, v163\n"
"v_mfma_f32_16x16x32_bf16 v[100:103], a[8:11], a[56:59], v[100:103]\n"
"buffer_load_dwordx4 v232, s[36:39], s20 offen offset:2048 lds\n"
"v_mfma_f32_16x16x32_bf16 v[200:203], a[8:11], a[252:255], v[200:203]\n"
"v_cvt_pk_bf16_f32 v160, v160, v160\n"
"v_ashrrev_i32 v161, 31, v160\n"
"v_mfma_f32_16x16x32_bf16 v[200:203], a[12:15], a[252:255], v[200:203]\n"
"s_waitcnt lgkmcnt(8)\n"
"v_or_b32 v161, 0x80000000, v161\n"
"v_xor_b32 v160, v160, v161\n"
"v_mfma_f32_16x16x32_bf16 v[8:11], a[16:19], a[32:35], v[8:11]\n"
"ds_read_b64_tr_b16 a[8:9], v221 offset:0\n"
"ds_read_b64_tr_b16 a[10:11], v221 offset:1024\n"
"v_mfma_f32_16x16x32_bf16 v[8:11], a[20:23], a[36:39], v[8:11]\n"
"buffer_load_dwordx4 v233, s[36:39], s20 offen offset:3072 lds\n"
"v_mfma_f32_16x16x32_bf16 v[40:43], a[20:23], a[44:47], v[40:43]\n"
"s_add_u32 m0, s28, 4096\n"
"s_add_u32 s20, s20, 32768\n"
"ds_read_b64_tr_b16 a[12:13], v221 offset:8192\n"
"v_mfma_f32_16x16x32_bf16 v[40:43], a[16:19], a[40:43], v[40:43]\n"
"ds_read_b64_tr_b16 a[14:15], v221 offset:9216\n"
"v_and_b32 v160, 0xffff0000, v160\n"
"v_mfma_f32_16x16x32_bf16 v[72:75], a[16:19], a[48:51], v[72:75]\n"
"v_or_b32 v160, v160, v163\n"
"v_mfma_f32_16x16x32_bf16 v[72:75], a[20:23], a[52:55], v[72:75]\n"
"buffer_load_dwordx4 v230, s[36:39], s20 offen offset:0 lds\n"
"v_mfma_f32_16x16x32_bf16 v[104:107], a[20:23], a[60:63], v[104:107]\n"
"s_lshl_b32 s94, s94, 3\n"
"s_add_u32 s94, s94, 16384\n"
"v_and_b32 v163, 16, v242\n"
"v_mfma_f32_16x16x32_bf16 v[104:107], a[16:19], a[56:59], v[104:107]\n"
"s_waitcnt lgkmcnt(8)\n"
"v_cmp_eq_u32 vcc, 0, v163\n"
"v_lshl_add_u32 v165, v163, 10, v165\n"
"v_mov_b32 v161, s14\n"
"v_mfma_f32_16x16x32_bf16 v[12:15], a[24:27], a[32:35], v[12:15]\n"
"ds_read_b64_tr_b16 a[16:17], v222 offset:0\n"
"ds_read_b64_tr_b16 a[18:19], v222 offset:1024\n"
"v_mfma_f32_16x16x32_bf16 v[12:15], a[28:31], a[36:39], v[12:15]\n"
"buffer_load_dwordx4 v231, s[36:39], s20 offen offset:1024 lds\n"
"v_mfma_f32_16x16x32_bf16 v[44:47], a[28:31], a[44:47], v[44:47]\n"
"ds_read_b64_tr_b16 a[20:21], v222 offset:8192\n"
"ds_read_b64_tr_b16 a[22:23], v222 offset:9216\n"
"v_mfma_f32_16x16x32_bf16 v[44:47], a[24:27], a[40:43], v[44:47]\n"
"v_cndmask_b32 v160, v160, v166, vcc\n"
"v_mfma_f32_16x16x32_bf16 v[76:79], a[24:27], a[48:51], v[76:79]\n"
"buffer_store_dwordx2 v[160:161], v165, s[56:59], s94 offen sc1\n"
"v_mfma_f32_16x16x32_bf16 v[76:79], a[28:31], a[52:55], v[76:79]\n"
"buffer_load_dwordx4 v232, s[36:39], s20 offen offset:2048 lds\n"
"v_mfma_f32_16x16x32_bf16 v[108:111], a[28:31], a[60:63], v[108:111]\n"
"v_mfma_f32_16x16x32_bf16 v[108:111], a[24:27], a[56:59], v[108:111]\n"
"s_waitcnt lgkmcnt(8)\n"
"v_mfma_f32_16x16x32_bf16 v[16:19], a[0:3], a[32:35], v[16:19]\n"
"ds_read_b64_tr_b16 a[24:25], v223 offset:0\n"
"ds_read_b64_tr_b16 a[26:27], v223 offset:1024\n"
"v_mfma_f32_16x16x32_bf16 v[16:19], a[4:7], a[36:39], v[16:19]\n"
"buffer_load_dwordx4 v233, s[36:39], s20 offen offset:3072 lds\n"
"v_mfma_f32_16x16x32_bf16 v[48:51], a[4:7], a[44:47], v[48:51]\n"
"ds_read_b64_tr_b16 a[28:29], v223 offset:8192\n"
"ds_read_b64_tr_b16 a[30:31], v223 offset:9216\n"
"v_mfma_f32_16x16x32_bf16 v[48:51], a[0:3], a[40:43], v[48:51]\n"
"v_mfma_f32_16x16x32_bf16 v[80:83], a[0:3], a[48:51], v[80:83]\n"
"v_mfma_f32_16x16x32_bf16 v[80:83], a[4:7], a[52:55], v[80:83]\n"
"v_mfma_f32_16x16x32_bf16 v[112:115], a[4:7], a[60:63], v[112:115]\n"
"v_mfma_f32_16x16x32_bf16 v[112:115], a[0:3], a[56:59], v[112:115]\n"
"s_waitcnt lgkmcnt(8)\n"
"v_mfma_f32_16x16x32_bf16 v[20:23], a[8:11], a[32:35], v[20:23]\n"
"v_mfma_f32_16x16x32_bf16 v[20:23], a[12:15], a[36:39], v[20:23]\n"
"v_mfma_f32_16x16x32_bf16 v[52:55], a[12:15], a[44:47], v[52:55]\n"
"s_waitcnt vmcnt(13)\n"
"s_waitcnt lgkmcnt(0)\n"
"v_mfma_f32_16x16x32_bf16 v[52:55], a[8:11], a[40:43], v[52:55]\n"
"s_barrier\n"
"v_mfma_f32_16x16x32_bf16 v[84:87], a[8:11], a[48:51], v[84:87]\n"
"ds_read_b128 a[64:67], v228 offset:32768\n"
"ds_read_b128 a[68:71], v229 offset:32768\n"
"v_mfma_f32_16x16x32_bf16 v[84:87], a[12:15], a[52:55], v[84:87]\n"
"s_add_u32 s22, s15, 5\n"
"s_and_b32 s22, s22, 7\n"
"s_lshl_b32 s23, s22, 19\n"
"v_mfma_f32_16x16x32_bf16 v[116:119], a[12:15], a[60:63], v[116:119]\n"
"s_add_u32 s23, s23, s18\n"
"s_add_u32 m0, s29, 0\n"
"ds_read_b64_tr_b16 a[0:1], v216 offset:16384\n"
"v_mfma_f32_16x16x32_bf16 v[116:119], a[8:11], a[56:59], v[116:119]\n"
"buffer_load_dwordx4 v234, s[40:43], s23 offen offset:0 lds\n"
"v_mfma_f32_16x16x32_bf16 v[24:27], a[16:19], a[32:35], v[24:27]\n"
"ds_read_b64_tr_b16 a[2:3], v216 offset:17408\n"
"ds_read_b64_tr_b16 a[4:5], v216 offset:24576\n"
"v_mfma_f32_16x16x32_bf16 v[24:27], a[20:23], a[36:39], v[24:27]\n"
"ds_read_b64_tr_b16 a[6:7], v216 offset:25600\n"
"ds_read_b128 a[72:75], v228 offset:34816\n"
"v_mfma_f32_16x16x32_bf16 v[56:59], a[20:23], a[44:47], v[56:59]\n"
"ds_read_b128 a[76:79], v229 offset:34816\n"
"ds_read_b128 a[80:83], v228 offset:36864\n"
"v_mfma_f32_16x16x32_bf16 v[56:59], a[16:19], a[40:43], v[56:59]\n"
"buffer_load_dwordx4 v235, s[40:43], s23 offen offset:1024 lds\n"
"v_mfma_f32_16x16x32_bf16 v[88:91], a[16:19], a[48:51], v[88:91]\n"
"ds_read_b128 a[84:87], v229 offset:36864\n"
"ds_read_b128 a[88:91], v228 offset:38912\n"
"v_mfma_f32_16x16x32_bf16 v[88:91], a[20:23], a[52:55], v[88:91]\n"
"ds_read_b128 a[92:95], v229 offset:38912\n"
"ds_read_b64_tr_b16 a[8:9], v217 offset:16384\n"
"v_mfma_f32_16x16x32_bf16 v[120:123], a[20:23], a[60:63], v[120:123]\n"
"ds_read_b64_tr_b16 a[10:11], v217 offset:17408\n"
"ds_read_b64_tr_b16 a[12:13], v217 offset:24576\n"
"v_mfma_f32_16x16x32_bf16 v[120:123], a[16:19], a[56:59], v[120:123]\n"
"buffer_load_dwordx4 v236, s[40:43], s23 offen offset:2048 lds\n"
"v_mfma_f32_16x16x32_bf16 v[28:31], a[24:27], a[32:35], v[28:31]\n"
"ds_read_b64_tr_b16 a[14:15], v217 offset:25600\n"
"ds_read_b64_tr_b16 a[16:17], v218 offset:16384\n"
"v_mfma_f32_16x16x32_bf16 v[28:31], a[28:31], a[36:39], v[28:31]\n"
"ds_read_b64_tr_b16 a[18:19], v218 offset:17408\n"
"ds_read_b64_tr_b16 a[20:21], v218 offset:24576\n"
"v_mfma_f32_16x16x32_bf16 v[60:63], a[28:31], a[44:47], v[60:63]\n"
"ds_read_b64_tr_b16 a[22:23], v218 offset:25600\n"
"v_mfma_f32_16x16x32_bf16 v[60:63], a[24:27], a[40:43], v[60:63]\n"
"buffer_load_dwordx4 v237, s[40:43], s23 offen offset:3072 lds\n"
"v_mfma_f32_16x16x32_bf16 v[92:95], a[24:27], a[48:51], v[92:95]\n"
"v_mfma_f32_16x16x32_bf16 v[92:95], a[28:31], a[52:55], v[92:95]\n"
"v_mfma_f32_16x16x32_bf16 v[124:127], a[28:31], a[60:63], v[124:127]\n"
"v_mfma_f32_16x16x32_bf16 v[124:127], a[24:27], a[56:59], v[124:127]\n"
"s_waitcnt lgkmcnt(15)\n"
"s_waitcnt lgkmcnt(14)\n"
"v_mfma_f32_16x16x32_bf16 v[0:3], a[0:3], a[64:67], v[0:3]\n"
"ds_read_b64_tr_b16 a[24:25], v219 offset:16384\n"
"ds_read_b64_tr_b16 a[26:27], v219 offset:17408\n"
"v_mfma_f32_16x16x32_bf16 v[0:3], a[4:7], a[68:71], v[0:3]\n"
"ds_read_b64_tr_b16 a[28:29], v219 offset:24576\n"
"ds_read_b64_tr_b16 a[30:31], v219 offset:25600\n"
"s_waitcnt lgkmcnt(15)\n"
"v_mfma_f32_16x16x32_bf16 v[32:35], a[4:7], a[76:79], v[32:35]\n"
"v_mfma_f32_16x16x32_bf16 v[32:35], a[0:3], a[72:75], v[32:35]\n"
"s_waitcnt lgkmcnt(14)\n"
"v_mfma_f32_16x16x32_bf16 v[64:67], a[0:3], a[80:83], v[64:67]\n"
"v_mfma_f32_16x16x32_bf16 v[64:67], a[4:7], a[84:87], v[64:67]\n"
"s_waitcnt lgkmcnt(12)\n"
"v_mfma_f32_16x16x32_bf16 v[96:99], a[4:7], a[92:95], v[96:99]\n"
"v_mfma_f32_16x16x32_bf16 v[96:99], a[0:3], a[88:91], v[96:99]\n"
"s_add_u32 s22, s15, 5\n"
"s_and_b32 s22, s22, 7\n"
"s_lshl_b32 s20, s22, 7\n"
"v_mfma_f32_16x16x32_bf16 v[196:199], a[0:3], a[252:255], v[196:199]\n"
"s_waitcnt lgkmcnt(8)\n"
"s_add_u32 s20, s20, s17\n"
"s_add_u32 m0, s28, 32768\n"
"v_mfma_f32_16x16x32_bf16 v[196:199], a[4:7], a[252:255], v[196:199]\n"
"buffer_load_dwordx4 v230, s[36:39], s20 offen offset:0 lds\n"
"v_mfma_f32_16x16x32_bf16 v[4:7], a[8:11], a[64:67], v[4:7]\n"
"ds_read_b64_tr_b16 a[0:1], v220 offset:16384\n"
"ds_read_b64_tr_b16 a[2:3], v220 offset:17408\n"
"v_mfma_f32_16x16x32_bf16 v[4:7], a[12:15], a[68:71], v[4:7]\n"
"ds_read_b64_tr_b16 a[4:5], v220 offset:24576\n"
"ds_read_b64_tr_b16 a[6:7], v220 offset:25600\n"
"v_mfma_f32_16x16x32_bf16 v[36:39], a[12:15], a[76:79], v[36:39]\n"
"v_mfma_f32_16x16x32_bf16 v[36:39], a[8:11], a[72:75], v[36:39]\n"
"buffer_load_dwordx4 v231, s[36:39], s20 offen offset:1024 lds\n"
"v_mfma_f32_16x16x32_bf16 v[68:71], a[8:11], a[80:83], v[68:71]\n"
"v_mfma_f32_16x16x32_bf16 v[68:71], a[12:15], a[84:87], v[68:71]\n"
"v_mfma_f32_16x16x32_bf16 v[100:103], a[12:15], a[92:95], v[100:103]\n"
"v_mfma_f32_16x16x32_bf16 v[100:103], a[8:11], a[88:91], v[100:103]\n"
"buffer_load_dwordx4 v232, s[36:39], s20 offen offset:2048 lds\n"
"v_mfma_f32_16x16x32_bf16 v[200:203], a[8:11], a[252:255], v[200:203]\n"
"v_mfma_f32_16x16x32_bf16 v[200:203], a[12:15], a[252:255], v[200:203]\n"
"s_waitcnt lgkmcnt(8)\n"
"v_mfma_f32_16x16x32_bf16 v[8:11], a[16:19], a[64:67], v[8:11]\n"
"ds_read_b64_tr_b16 a[8:9], v221 offset:16384\n"
"ds_read_b64_tr_b16 a[10:11], v221 offset:17408\n"
"v_mfma_f32_16x16x32_bf16 v[8:11], a[20:23], a[68:71], v[8:11]\n"
"buffer_load_dwordx4 v233, s[36:39], s20 offen offset:3072 lds\n"
"v_mfma_f32_16x16x32_bf16 v[40:43], a[20:23], a[76:79], v[40:43]\n"
"s_add_u32 m0, s28, 36864\n"
"s_add_u32 s20, s20, 32768\n"
"ds_read_b64_tr_b16 a[12:13], v221 offset:24576\n"
"v_mfma_f32_16x16x32_bf16 v[40:43], a[16:19], a[72:75], v[40:43]\n"
"ds_read_b64_tr_b16 a[14:15], v221 offset:25600\n"
"v_mfma_f32_16x16x32_bf16 v[72:75], a[16:19], a[80:83], v[72:75]\n"
"v_mfma_f32_16x16x32_bf16 v[72:75], a[20:23], a[84:87], v[72:75]\n"
"buffer_load_dwordx4 v230, s[36:39], s20 offen offset:0 lds\n"
"v_mfma_f32_16x16x32_bf16 v[104:107], a[20:23], a[92:95], v[104:107]\n"
"v_mfma_f32_16x16x32_bf16 v[104:107], a[16:19], a[88:91], v[104:107]\n"
"s_waitcnt lgkmcnt(8)\n"
"v_mfma_f32_16x16x32_bf16 v[12:15], a[24:27], a[64:67], v[12:15]\n"
"ds_read_b64_tr_b16 a[16:17], v222 offset:16384\n"
"ds_read_b64_tr_b16 a[18:19], v222 offset:17408\n"
"v_mfma_f32_16x16x32_bf16 v[12:15], a[28:31], a[68:71], v[12:15]\n"
"buffer_load_dwordx4 v231, s[36:39], s20 offen offset:1024 lds\n"
"v_mfma_f32_16x16x32_bf16 v[44:47], a[28:31], a[76:79], v[44:47]\n"
"ds_read_b64_tr_b16 a[20:21], v222 offset:24576\n"
"ds_read_b64_tr_b16 a[22:23], v222 offset:25600\n"
"v_mfma_f32_16x16x32_bf16 v[44:47], a[24:27], a[72:75], v[44:47]\n"
"v_mfma_f32_16x16x32_bf16 v[76:79], a[24:27], a[80:83], v[76:79]\n"
"v_mfma_f32_16x16x32_bf16 v[76:79], a[28:31], a[84:87], v[76:79]\n"
"buffer_load_dwordx4 v232, s[36:39], s20 offen offset:2048 lds\n"
"v_mfma_f32_16x16x32_bf16 v[108:111], a[28:31], a[92:95], v[108:111]\n"
"v_mfma_f32_16x16x32_bf16 v[108:111], a[24:27], a[88:91], v[108:111]\n"
"s_waitcnt lgkmcnt(8)\n"
"v_mfma_f32_16x16x32_bf16 v[16:19], a[0:3], a[64:67], v[16:19]\n"
"ds_read_b64_tr_b16 a[24:25], v223 offset:16384\n"
"ds_read_b64_tr_b16 a[26:27], v223 offset:17408\n"
"v_mfma_f32_16x16x32_bf16 v[16:19], a[4:7], a[68:71], v[16:19]\n"
"buffer_load_dwordx4 v233, s[36:39], s20 offen offset:3072 lds\n"
"v_mfma_f32_16x16x32_bf16 v[48:51], a[4:7], a[76:79], v[48:51]\n"
"ds_read_b64_tr_b16 a[28:29], v223 offset:24576\n"
"ds_read_b64_tr_b16 a[30:31], v223 offset:25600\n"
"v_mfma_f32_16x16x32_bf16 v[48:51], a[0:3], a[72:75], v[48:51]\n"
"v_mfma_f32_16x16x32_bf16 v[80:83], a[0:3], a[80:83], v[80:83]\n"
"v_mfma_f32_16x16x32_bf16 v[80:83], a[4:7], a[84:87], v[80:83]\n"
"v_mfma_f32_16x16x32_bf16 v[112:115], a[4:7], a[92:95], v[112:115]\n"
"v_mfma_f32_16x16x32_bf16 v[112:115], a[0:3], a[88:91], v[112:115]\n"
"s_waitcnt lgkmcnt(8)\n"
"v_mfma_f32_16x16x32_bf16 v[20:23], a[8:11], a[64:67], v[20:23]\n"
"v_mfma_f32_16x16x32_bf16 v[20:23], a[12:15], a[68:71], v[20:23]\n"
"v_mfma_f32_16x16x32_bf16 v[52:55], a[12:15], a[76:79], v[52:55]\n"
"s_waitcnt vmcnt(12)\n"
"s_waitcnt lgkmcnt(0)\n"
"v_mfma_f32_16x16x32_bf16 v[52:55], a[8:11], a[72:75], v[52:55]\n"
"s_barrier\n"
"v_mfma_f32_16x16x32_bf16 v[84:87], a[8:11], a[80:83], v[84:87]\n"
"ds_read_b128 a[32:35], v228 offset:0\n"
"ds_read_b128 a[36:39], v229 offset:0\n"
"v_mfma_f32_16x16x32_bf16 v[84:87], a[12:15], a[84:87], v[84:87]\n"
"s_add_u32 s22, s15, 6\n"
"s_and_b32 s22, s22, 7\n"
"s_lshl_b32 s23, s22, 19\n"
"v_mfma_f32_16x16x32_bf16 v[116:119], a[12:15], a[92:95], v[116:119]\n"
"s_add_u32 s23, s23, s18\n"
"s_add_u32 m0, s29, 16384\n"
"ds_read_b64_tr_b16 a[0:1], v216 offset:32768\n"
"v_mfma_f32_16x16x32_bf16 v[116:119], a[8:11], a[88:91], v[116:119]\n"
"buffer_load_dwordx4 v234, s[40:43], s23 offen offset:0 lds\n"
"v_mfma_f32_16x16x32_bf16 v[24:27], a[16:19], a[64:67], v[24:27]\n"
"ds_read_b64_tr_b16 a[2:3], v216 offset:33792\n"
"ds_read_b64_tr_b16 a[4:5], v216 offset:40960\n"
"v_mfma_f32_16x16x32_bf16 v[24:27], a[20:23], a[68:71], v[24:27]\n"
"ds_read_b64_tr_b16 a[6:7], v216 offset:41984\n"
"ds_read_b128 a[40:43], v228 offset:2048\n"
"v_mfma_f32_16x16x32_bf16 v[56:59], a[20:23], a[76:79], v[56:59]\n"
"ds_read_b128 a[44:47], v229 offset:2048\n"
"ds_read_b128 a[48:51], v228 offset:4096\n"
"v_mfma_f32_16x16x32_bf16 v[56:59], a[16:19], a[72:75], v[56:59]\n"
"buffer_load_dwordx4 v235, s[40:43], s23 offen offset:1024 lds\n"
"v_mfma_f32_16x16x32_bf16 v[88:91], a[16:19], a[80:83], v[88:91]\n"
"ds_read_b128 a[52:55], v229 offset:4096\n"
"ds_read_b128 a[56:59], v228 offset:6144\n"
"v_mfma_f32_16x16x32_bf16 v[88:91], a[20:23], a[84:87], v[88:91]\n"
"ds_read_b128 a[60:63], v229 offset:6144\n"
"ds_read_b64_tr_b16 a[8:9], v217 offset:32768\n"
"v_mfma_f32_16x16x32_bf16 v[120:123], a[20:23], a[92:95], v[120:123]\n"
"ds_read_b64_tr_b16 a[10:11], v217 offset:33792\n"
"ds_read_b64_tr_b16 a[12:13], v217 offset:40960\n"
"v_mfma_f32_16x16x32_bf16 v[120:123], a[16:19], a[88:91], v[120:123]\n"
"buffer_load_dwordx4 v236, s[40:43], s23 offen offset:2048 lds\n"
"v_mfma_f32_16x16x32_bf16 v[28:31], a[24:27], a[64:67], v[28:31]\n"
"ds_read_b64_tr_b16 a[14:15], v217 offset:41984\n"
"ds_read_b64_tr_b16 a[16:17], v218 offset:32768\n"
"v_mfma_f32_16x16x32_bf16 v[28:31], a[28:31], a[68:71], v[28:31]\n"
"ds_read_b64_tr_b16 a[18:19], v218 offset:33792\n"
"ds_read_b64_tr_b16 a[20:21], v218 offset:40960\n"
"v_mfma_f32_16x16x32_bf16 v[60:63], a[28:31], a[76:79], v[60:63]\n"
"ds_read_b64_tr_b16 a[22:23], v218 offset:41984\n"
"v_mfma_f32_16x16x32_bf16 v[60:63], a[24:27], a[72:75], v[60:63]\n"
"buffer_load_dwordx4 v237, s[40:43], s23 offen offset:3072 lds\n"
"v_mfma_f32_16x16x32_bf16 v[92:95], a[24:27], a[80:83], v[92:95]\n"
"v_mfma_f32_16x16x32_bf16 v[92:95], a[28:31], a[84:87], v[92:95]\n"
"v_mfma_f32_16x16x32_bf16 v[124:127], a[28:31], a[92:95], v[124:127]\n"
"v_mfma_f32_16x16x32_bf16 v[124:127], a[24:27], a[88:91], v[124:127]\n"
"s_waitcnt lgkmcnt(15)\n"
"s_waitcnt lgkmcnt(14)\n"
"v_mfma_f32_16x16x32_bf16 v[0:3], a[0:3], a[32:35], v[0:3]\n"
"ds_read_b64_tr_b16 a[24:25], v219 offset:32768\n"
"ds_read_b64_tr_b16 a[26:27], v219 offset:33792\n"
"v_mfma_f32_16x16x32_bf16 v[0:3], a[4:7], a[36:39], v[0:3]\n"
"ds_read_b64_tr_b16 a[28:29], v219 offset:40960\n"
"ds_read_b64_tr_b16 a[30:31], v219 offset:41984\n"
"s_waitcnt lgkmcnt(15)\n"
"v_mfma_f32_16x16x32_bf16 v[32:35], a[4:7], a[44:47], v[32:35]\n"
"v_mfma_f32_16x16x32_bf16 v[32:35], a[0:3], a[40:43], v[32:35]\n"
"s_waitcnt lgkmcnt(14)\n"
"v_mfma_f32_16x16x32_bf16 v[64:67], a[0:3], a[48:51], v[64:67]\n"
"v_mfma_f32_16x16x32_bf16 v[64:67], a[4:7], a[52:55], v[64:67]\n"
"s_waitcnt lgkmcnt(12)\n"
"v_mfma_f32_16x16x32_bf16 v[96:99], a[4:7], a[60:63], v[96:99]\n"
"v_mfma_f32_16x16x32_bf16 v[96:99], a[0:3], a[56:59], v[96:99]\n"
"s_add_u32 s22, s15, 6\n"
"s_and_b32 s22, s22, 7\n"
"s_lshl_b32 s20, s22, 7\n"
"v_mfma_f32_16x16x32_bf16 v[196:199], a[0:3], a[252:255], v[196:199]\n"
"s_waitcnt lgkmcnt(8)\n"
"s_add_u32 s20, s20, s17\n"
"s_add_u32 m0, s28, 0\n"
"v_mfma_f32_16x16x32_bf16 v[196:199], a[4:7], a[252:255], v[196:199]\n"
"buffer_load_dwordx4 v230, s[36:39], s20 offen offset:0 lds\n"
"v_mfma_f32_16x16x32_bf16 v[4:7], a[8:11], a[32:35], v[4:7]\n"
"ds_read_b64_tr_b16 a[0:1], v220 offset:32768\n"
"ds_read_b64_tr_b16 a[2:3], v220 offset:33792\n"
"v_mfma_f32_16x16x32_bf16 v[4:7], a[12:15], a[36:39], v[4:7]\n"
"ds_read_b64_tr_b16 a[4:5], v220 offset:40960\n"
"ds_read_b64_tr_b16 a[6:7], v220 offset:41984\n"
"v_mfma_f32_16x16x32_bf16 v[36:39], a[12:15], a[44:47], v[36:39]\n"
"v_mfma_f32_16x16x32_bf16 v[36:39], a[8:11], a[40:43], v[36:39]\n"
"buffer_load_dwordx4 v231, s[36:39], s20 offen offset:1024 lds\n"
"v_mfma_f32_16x16x32_bf16 v[68:71], a[8:11], a[48:51], v[68:71]\n"
"v_mfma_f32_16x16x32_bf16 v[68:71], a[12:15], a[52:55], v[68:71]\n"
"v_mfma_f32_16x16x32_bf16 v[100:103], a[12:15], a[60:63], v[100:103]\n"
"v_mfma_f32_16x16x32_bf16 v[100:103], a[8:11], a[56:59], v[100:103]\n"
"buffer_load_dwordx4 v232, s[36:39], s20 offen offset:2048 lds\n"
"v_mfma_f32_16x16x32_bf16 v[200:203], a[8:11], a[252:255], v[200:203]\n"
"v_mfma_f32_16x16x32_bf16 v[200:203], a[12:15], a[252:255], v[200:203]\n"
"s_waitcnt lgkmcnt(8)\n"
"v_mfma_f32_16x16x32_bf16 v[8:11], a[16:19], a[32:35], v[8:11]\n"
"ds_read_b64_tr_b16 a[8:9], v221 offset:32768\n"
"ds_read_b64_tr_b16 a[10:11], v221 offset:33792\n"
"v_mfma_f32_16x16x32_bf16 v[8:11], a[20:23], a[36:39], v[8:11]\n"
"buffer_load_dwordx4 v233, s[36:39], s20 offen offset:3072 lds\n"
"v_mfma_f32_16x16x32_bf16 v[40:43], a[20:23], a[44:47], v[40:43]\n"
"s_add_u32 m0, s28, 4096\n"
"s_add_u32 s20, s20, 32768\n"
"ds_read_b64_tr_b16 a[12:13], v221 offset:40960\n"
"v_mfma_f32_16x16x32_bf16 v[40:43], a[16:19], a[40:43], v[40:43]\n"
"ds_read_b64_tr_b16 a[14:15], v221 offset:41984\n"
"v_mfma_f32_16x16x32_bf16 v[72:75], a[16:19], a[48:51], v[72:75]\n"
"v_mfma_f32_16x16x32_bf16 v[72:75], a[20:23], a[52:55], v[72:75]\n"
"buffer_load_dwordx4 v230, s[36:39], s20 offen offset:0 lds\n"
"v_mfma_f32_16x16x32_bf16 v[104:107], a[20:23], a[60:63], v[104:107]\n"
"v_mfma_f32_16x16x32_bf16 v[104:107], a[16:19], a[56:59], v[104:107]\n"
"s_waitcnt lgkmcnt(8)\n"
"v_mfma_f32_16x16x32_bf16 v[12:15], a[24:27], a[32:35], v[12:15]\n"
"ds_read_b64_tr_b16 a[16:17], v222 offset:32768\n"
"ds_read_b64_tr_b16 a[18:19], v222 offset:33792\n"
"v_mfma_f32_16x16x32_bf16 v[12:15], a[28:31], a[36:39], v[12:15]\n"
"buffer_load_dwordx4 v231, s[36:39], s20 offen offset:1024 lds\n"
"v_mfma_f32_16x16x32_bf16 v[44:47], a[28:31], a[44:47], v[44:47]\n"
"ds_read_b64_tr_b16 a[20:21], v222 offset:40960\n"
"ds_read_b64_tr_b16 a[22:23], v222 offset:41984\n"
"v_mfma_f32_16x16x32_bf16 v[44:47], a[24:27], a[40:43], v[44:47]\n"
"v_mfma_f32_16x16x32_bf16 v[76:79], a[24:27], a[48:51], v[76:79]\n"
"v_mfma_f32_16x16x32_bf16 v[76:79], a[28:31], a[52:55], v[76:79]\n"
"buffer_load_dwordx4 v232, s[36:39], s20 offen offset:2048 lds\n"
"v_mfma_f32_16x16x32_bf16 v[108:111], a[28:31], a[60:63], v[108:111]\n"
"v_mfma_f32_16x16x32_bf16 v[108:111], a[24:27], a[56:59], v[108:111]\n"
"s_waitcnt lgkmcnt(8)\n"
"v_mfma_f32_16x16x32_bf16 v[16:19], a[0:3], a[32:35], v[16:19]\n"
"ds_read_b64_tr_b16 a[24:25], v223 offset:32768\n"
"ds_read_b64_tr_b16 a[26:27], v223 offset:33792\n"
"v_mfma_f32_16x16x32_bf16 v[16:19], a[4:7], a[36:39], v[16:19]\n"
"buffer_load_dwordx4 v233, s[36:39], s20 offen offset:3072 lds\n"
"v_mfma_f32_16x16x32_bf16 v[48:51], a[4:7], a[44:47], v[48:51]\n"
"ds_read_b64_tr_b16 a[28:29], v223 offset:40960\n"
"ds_read_b64_tr_b16 a[30:31], v223 offset:41984\n"
"v_mfma_f32_16x16x32_bf16 v[48:51], a[0:3], a[40:43], v[48:51]\n"
"v_mfma_f32_16x16x32_bf16 v[80:83], a[0:3], a[48:51], v[80:83]\n"
"v_mfma_f32_16x16x32_bf16 v[80:83], a[4:7], a[52:55], v[80:83]\n"
"v_mfma_f32_16x16x32_bf16 v[112:115], a[4:7], a[60:63], v[112:115]\n"
"v_mfma_f32_16x16x32_bf16 v[112:115], a[0:3], a[56:59], v[112:115]\n"
"s_waitcnt lgkmcnt(8)\n"
"v_mfma_f32_16x16x32_bf16 v[20:23], a[8:11], a[32:35], v[20:23]\n"
"v_mfma_f32_16x16x32_bf16 v[20:23], a[12:15], a[36:39], v[20:23]\n"
"v_mfma_f32_16x16x32_bf16 v[52:55], a[12:15], a[44:47], v[52:55]\n"
"s_waitcnt vmcnt(12)\n"
"s_waitcnt lgkmcnt(0)\n"
"v_mfma_f32_16x16x32_bf16 v[52:55], a[8:11], a[40:43], v[52:55]\n"
"s_barrier\n"
"v_mfma_f32_16x16x32_bf16 v[84:87], a[8:11], a[48:51], v[84:87]\n"
"ds_read_b128 a[64:67], v228 offset:32768\n"
"ds_read_b128 a[68:71], v229 offset:32768\n"
"v_mfma_f32_16x16x32_bf16 v[84:87], a[12:15], a[52:55], v[84:87]\n"
"s_lshl_b32 s96, s10, 12\n"
"s_add_u32 s96, s96, 32768\n"
"ds_read_b64_tr_b16 a[0:1], v216 offset:0\n"
"v_mfma_f32_16x16x32_bf16 v[116:119], a[12:15], a[60:63], v[116:119]\n"
"buffer_load_dwordx4 v[128:131], v243, s[56:59], s96 offen offset:0 sc1\n"
"v_mfma_f32_16x16x32_bf16 v[116:119], a[8:11], a[56:59], v[116:119]\n"
"ds_read_b64_tr_b16 a[2:3], v216 offset:1024\n"
"ds_read_b64_tr_b16 a[4:5], v216 offset:8192\n"
"v_mfma_f32_16x16x32_bf16 v[24:27], a[16:19], a[32:35], v[24:27]\n"
"ds_read_b64_tr_b16 a[6:7], v216 offset:9216\n"
"L_ic1:\n"
"s_cbranch_execz L_ic5\n"
"ds_read_b128 a[72:75], v228 offset:34816\n"
"v_mfma_f32_16x16x32_bf16 v[24:27], a[20:23], a[36:39], v[24:27]\n"
"ds_read_b128 a[76:79], v229 offset:34816\n"
"ds_read_b128 a[80:83], v228 offset:36864\n"
"v_mfma_f32_16x16x32_bf16 v[56:59], a[20:23], a[44:47], v[56:59]\n"
"buffer_load_dwordx4 v[132:135], v243, s[56:59], s96 offen offset:1024 sc1\n"
"v_mfma_f32_16x16x32_bf16 v[56:59], a[16:19], a[40:43], v[56:59]\n"
"ds_read_b128 a[84:87], v229 offset:36864\n"
"ds_read_b128 a[88:91], v228 offset:38912\n"
"v_mfma_f32_16x16x32_bf16 v[88:91], a[16:19], a[48:51], v[88:91]\n"
"ds_read_b128 a[92:95], v229 offset:38912\n"
"ds_read_b64_tr_b16 a[8:9], v217 offset:0\n"
"v_mfma_f32_16x16x32_bf16 v[88:91], a[20:23], a[52:55], v[88:91]\n"
"ds_read_b64_tr_b16 a[10:11], v217 offset:1024\n"
"ds_read_b64_tr_b16 a[12:13], v217 offset:8192\n"
"v_mfma_f32_16x16x32_bf16 v[120:123], a[20:23], a[60:63], v[120:123]\n"
"buffer_load_dwordx4 v[136:139], v243, s[56:59], s96 offen offset:2048 sc1\n"
"v_mfma_f32_16x16x32_bf16 v[120:123], a[16:19], a[56:59], v[120:123]\n"
"ds_read_b64_tr_b16 a[14:15], v217 offset:9216\n"
"v_mfma_f32_16x16x32_bf16 v[28:31], a[24:27], a[32:35], v[28:31]\n"
"ds_read_b64_tr_b16 a[16:17], v218 offset:0\n"
"ds_read_b64_tr_b16 a[18:19], v218 offset:1024\n"
"v_mfma_f32_16x16x32_bf16 v[28:31], a[28:31], a[36:39], v[28:31]\n"
"ds_read_b64_tr_b16 a[20:21], v218 offset:8192\n"
"ds_read_b64_tr_b16 a[22:23], v218 offset:9216\n"
"v_mfma_f32_16x16x32_bf16 v[60:63], a[28:31], a[44:47], v[60:63]\n"
"buffer_load_dwordx4 v[140:143], v243, s[56:59], s96 offen offset:3072 sc1\n"
"v_mfma_f32_16x16x32_bf16 v[60:63], a[24:27], a[40:43], v[60:63]\n"
"s_lshr_b32 s97, s17, 7\n"
"s_add_u32 s97, s97, 16384\n"
"v_and_b32 v168, 15, v242\n"
"v_mfma_f32_16x16x32_bf16 v[92:95], a[24:27], a[48:51], v[92:95]\n"
"v_lshlrev_b32 v168, 3, v168\n"
"v_mfma_f32_16x16x32_bf16 v[92:95], a[28:31], a[52:55], v[92:95]\n"
"v_mfma_f32_16x16x32_bf16 v[124:127], a[28:31], a[60:63], v[124:127]\n"
"buffer_load_dwordx2 v[160:161], v168, s[56:59], s97 offen offset:0 sc1\n"
"v_mfma_f32_16x16x32_bf16 v[124:127], a[24:27], a[56:59], v[124:127]\n"
"s_waitcnt lgkmcnt(15)\n"
"s_waitcnt lgkmcnt(14)\n"
"v_mfma_f32_16x16x32_bf16 v[0:3], a[0:3], a[64:67], v[0:3]\n"
"ds_read_b64_tr_b16 a[24:25], v219 offset:0\n"
"ds_read_b64_tr_b16 a[26:27], v219 offset:1024\n"
"v_mfma_f32_16x16x32_bf16 v[0:3], a[4:7], a[68:71], v[0:3]\n"
"ds_read_b64_tr_b16 a[28:29], v219 offset:8192\n"
"ds_read_b64_tr_b16 a[30:31], v219 offset:9216\n"
"s_waitcnt lgkmcnt(15)\n"
"v_mfma_f32_16x16x32_bf16 v[32:35], a[4:7], a[76:79], v[32:35]\n"
"buffer_load_dwordx2 v[162:163], v168, s[56:59], s97 offen offset:128 sc1\n"
"v_mfma_f32_16x16x32_bf16 v[32:35], a[0:3], a[72:75], v[32:35]\n"
"s_waitcnt lgkmcnt(14)\n"
"v_mfma_f32_16x16x32_bf16 v[64:67], a[0:3], a[80:83], v[64:67]\n"
"v_mfma_f32_16x16x32_bf16 v[64:67], a[4:7], a[84:87], v[64:67]\n"
"s_waitcnt lgkmcnt(12)\n"
"v_mfma_f32_16x16x32_bf16 v[96:99], a[4:7], a[92:95], v[96:99]\n"
"buffer_load_dwordx2 v[164:165], v168, s[56:59], s97 offen offset:256 sc1\n"
"v_mfma_f32_16x16x32_bf16 v[96:99], a[0:3], a[88:91], v[96:99]\n"
"v_mfma_f32_16x16x32_bf16 v[196:199], a[0:3], a[252:255], v[196:199]\n"
"v_mfma_f32_16x16x32_bf16 v[196:199], a[4:7], a[252:255], v[196:199]\n"
"s_waitcnt lgkmcnt(8)\n"
"v_mfma_f32_16x16x32_bf16 v[4:7], a[8:11], a[64:67], v[4:7]\n"
"buffer_load_dwordx2 v[166:167], v168, s[56:59], s97 offen offset:384 sc1\n"
"v_mfma_f32_16x16x32_bf16 v[4:7], a[12:15], a[68:71], v[4:7]\n"
"s_add_u32 s22, s15, 7\n"
"s_and_b32 s22, s22, 7\n"
"s_lshl_b32 s23, s22, 19\n"
"v_mfma_f32_16x16x32_bf16 v[36:39], a[12:15], a[76:79], v[36:39]\n"
"s_add_u32 s23, s23, s18\n"
"s_add_u32 m0, s29, 32768\n"
"ds_read_b64_tr_b16 a[0:1], v220 offset:0\n"
"v_mfma_f32_16x16x32_bf16 v[36:39], a[8:11], a[72:75], v[36:39]\n"
"ds_read_b64_tr_b16 a[2:3], v220 offset:1024\n"
"ds_read_b64_tr_b16 a[4:5], v220 offset:8192\n"
"v_mfma_f32_16x16x32_bf16 v[68:71], a[8:11], a[80:83], v[68:71]\n"
"buffer_load_dwordx4 v234, s[40:43], s23 offen offset:0 lds\n"
"v_mfma_f32_16x16x32_bf16 v[68:71], a[12:15], a[84:87], v[68:71]\n"
"ds_read_b64_tr_b16 a[6:7], v220 offset:9216\n"
"v_mfma_f32_16x16x32_bf16 v[100:103], a[12:15], a[92:95], v[100:103]\n"
"v_mfma_f32_16x16x32_bf16 v[100:103], a[8:11], a[88:91], v[100:103]\n"
"v_mfma_f32_16x16x32_bf16 v[200:203], a[8:11], a[252:255], v[200:203]\n"
"buffer_load_dwordx4 v235, s[40:43], s23 offen offset:1024 lds\n"
"v_mfma_f32_16x16x32_bf16 v[200:203], a[12:15], a[252:255], v[200:203]\n"
"s_waitcnt lgkmcnt(8)\n"
"v_mfma_f32_16x16x32_bf16 v[8:11], a[16:19], a[64:67], v[8:11]\n"
"ds_read_b64_tr_b16 a[8:9], v221 offset:0\n"
"ds_read_b64_tr_b16 a[10:11], v221 offset:1024\n"
"v_mfma_f32_16x16x32_bf16 v[8:11], a[20:23], a[68:71], v[8:11]\n"
"ds_read_b64_tr_b16 a[12:13], v221 offset:8192\n"
"ds_read_b64_tr_b16 a[14:15], v221 offset:9216\n"
"v_mfma_f32_16x16x32_bf16 v[40:43], a[20:23], a[76:79], v[40:43]\n"
"buffer_load_dwordx4 v236, s[40:43], s23 offen offset:2048 lds\n"
"v_mfma_f32_16x16x32_bf16 v[40:43], a[16:19], a[72:75], v[40:43]\n"
"v_mfma_f32_16x16x32_bf16 v[72:75], a[16:19], a[80:83], v[72:75]\n"
"v_mfma_f32_16x16x32_bf16 v[72:75], a[20:23], a[84:87], v[72:75]\n"
"v_mfma_f32_16x16x32_bf16 v[104:107], a[20:23], a[92:95], v[104:107]\n"
"buffer_load_dwordx4 v237, s[40:43], s23 offen offset:3072 lds\n"
"v_mfma_f32_16x16x32_bf16 v[104:107], a[16:19], a[88:91], v[104:107]\n"
"s_add_u32 s22, s15, 7\n"
"s_and_b32 s22, s22, 7\n"
"s_lshl_b32 s20, s22, 7\n"
"s_waitcnt lgkmcnt(8)\n"
"v_mfma_f32_16x16x32_bf16 v[12:15], a[24:27], a[64:67], v[12:15]\n"
"s_add_u32 s20, s20, s17\n"
"s_add_u32 m0, s28, 32768\n"
"ds_read_b64_tr_b16 a[16:17], v222 offset:0\n"
"v_mfma_f32_16x16x32_bf16 v[12:15], a[28:31], a[68:71], v[12:15]\n"
"ds_read_b64_tr_b16 a[18:19], v222 offset:1024\n"
"ds_read_b64_tr_b16 a[20:21], v222 offset:8192\n"
"v_mfma_f32_16x16x32_bf16 v[44:47], a[28:31], a[76:79], v[44:47]\n"
"buffer_load_dwordx4 v230, s[36:39], s20 offen offset:0 lds\n"
"v_mfma_f32_16x16x32_bf16 v[44:47], a[24:27], a[72:75], v[44:47]\n"
"ds_read_b64_tr_b16 a[22:23], v222 offset:9216\n"
"v_mfma_f32_16x16x32_bf16 v[76:79], a[24:27], a[80:83], v[76:79]\n"
"v_mfma_f32_16x16x32_bf16 v[76:79], a[28:31], a[84:87], v[76:79]\n"
"v_mfma_f32_16x16x32_bf16 v[108:111], a[28:31], a[92:95], v[108:111]\n"
"buffer_load_dwordx4 v231, s[36:39], s20 offen offset:1024 lds\n"
"v_mfma_f32_16x16x32_bf16 v[108:111], a[24:27], a[88:91], v[108:111]\n"
"s_waitcnt lgkmcnt(8)\n"
"v_mfma_f32_16x16x32_bf16 v[16:19], a[0:3], a[64:67], v[16:19]\n"
"ds_read_b64_tr_b16 a[24:25], v223 offset:0\n"
"ds_read_b64_tr_b16 a[26:27], v223 offset:1024\n"
"v_mfma_f32_16x16x32_bf16 v[16:19], a[4:7], a[68:71], v[16:19]\n"
"ds_read_b64_tr_b16 a[28:29], v223 offset:8192\n"
"ds_read_b64_tr_b16 a[30:31], v223 offset:9216\n"
"v_mfma_f32_16x16x32_bf16 v[48:51], a[4:7], a[76:79], v[48:51]\n"
"buffer_load_dwordx4 v232, s[36:39], s20 offen offset:2048 lds\n"
"v_mfma_f32_16x16x32_bf16 v[48:51], a[0:3], a[72:75], v[48:51]\n"
"v_mfma_f32_16x16x32_bf16 v[80:83], a[0:3], a[80:83], v[80:83]\n"
"v_mfma_f32_16x16x32_bf16 v[80:83], a[4:7], a[84:87], v[80:83]\n"
"v_mfma_f32_16x16x32_bf16 v[112:115], a[4:7], a[92:95], v[112:115]\n"
"buffer_load_dwordx4 v233, s[36:39], s20 offen offset:3072 lds\n"
"v_mfma_f32_16x16x32_bf16 v[112:115], a[0:3], a[88:91], v[112:115]\n"
"s_waitcnt lgkmcnt(8)\n"
"s_add_u32 m0, s28, 36864\n"
"s_add_u32 s20, s20, 32768\n"
"v_mfma_f32_16x16x32_bf16 v[20:23], a[8:11], a[64:67], v[20:23]\n"
"v_mfma_f32_16x16x32_bf16 v[20:23], a[12:15], a[68:71], v[20:23]\n"
"s_waitcnt vmcnt(8)\n"
"s_waitcnt lgkmcnt(0)\n"
"v_mfma_f32_16x16x32_bf16 v[52:55], a[12:15], a[76:79], v[52:55]\n"
"buffer_load_dwordx4 v230, s[36:39], s20 offen offset:0 lds\n"
"v_mfma_f32_16x16x32_bf16 v[52:55], a[8:11], a[72:75], v[52:55]\n"
"s_barrier\n"
"v_mfma_f32_16x16x32_bf16 v[84:87], a[8:11], a[80:83], v[84:87]\n"
"ds_read_b128 a[32:35], v228 offset:0\n"
"ds_read_b128 a[36:39], v229 offset:0\n"
"v_min3_u32 v144, v129, v131, v133\n"
"v_max3_u32 v145, v129, v131, v133\n"
"v_min3_u32 v144, v144, v135, v137\n"
"v_max3_u32 v145, v145, v135, v137\n"
"v_min3_u32 v144, v144, v139, v141\n"
"v_max3_u32 v145, v145, v139, v141\n"
"v_min3_u32 v144, v144, v143, s14\n"
"v_mfma_f32_16x16x32_bf16 v[84:87], a[12:15], a[84:87], v[84:87]\n"
"ds_read_b64_tr_b16 a[0:1], v216 offset:16384\n"
"ds_read_b64_tr_b16 a[2:3], v216 offset:17408\n"
"v_mfma_f32_16x16x32_bf16 v[116:119], a[12:15], a[92:95], v[116:119]\n"
"buffer_load_dwordx4 v231, s[36:39], s20 offen offset:1024 lds\n"
"v_mfma_f32_16x16x32_bf16 v[116:119], a[8:11], a[88:91], v[116:119]\n"
"ds_read_b64_tr_b16 a[4:5], v216 offset:24576\n"
"ds_read_b64_tr_b16 a[6:7], v216 offset:25600\n"
"v_mfma_f32_16x16x32_bf16 v[24:27], a[16:19], a[64:67], v[24:27]\n"
"ds_read_b128 a[40:43], v228 offset:2048\n"
"ds_read_b128 a[44:47], v229 offset:2048\n"
"v_mfma_f32_16x16x32_bf16 v[24:27], a[20:23], a[68:71], v[24:27]\n"
"ds_read_b128 a[48:51], v228 offset:4096\n"
"ds_read_b128 a[52:55], v229 offset:4096\n"
"v_mfma_f32_16x16x32_bf16 v[56:59], a[20:23], a[76:79], v[56:59]\n"
"buffer_load_dwordx4 v232, s[36:39], s20 offen offset:2048 lds\n"
"v_mfma_f32_16x16x32_bf16 v[56:59], a[16:19], a[72:75], v[56:59]\n"
"ds_read_b128 a[56:59], v228 offset:6144\n"
"ds_read_b128 a[60:63], v229 offset:6144\n"
"v_mfma_f32_16x16x32_bf16 v[88:91], a[16:19], a[80:83], v[88:91]\n"
"ds_read_b64_tr_b16 a[8:9], v217 offset:16384\n"
"ds_read_b64_tr_b16 a[10:11], v217 offset:17408\n"
"v_mfma_f32_16x16x32_bf16 v[88:91], a[20:23], a[84:87], v[88:91]\n"
"ds_read_b64_tr_b16 a[12:13], v217 offset:24576\n"
"ds_read_b64_tr_b16 a[14:15], v217 offset:25600\n"
"v_mfma_f32_16x16x32_bf16 v[120:123], a[20:23], a[92:95], v[120:123]\n"
"buffer_load_dwordx4 v233, s[36:39], s20 offen offset:3072 lds\n"
"v_mfma_f32_16x16x32_bf16 v[120:123], a[16:19], a[88:91], v[120:123]\n"
"s_add_u32 m0, s30, 0\n"
"s_xor_b32 s25, s19, 0\n"
"v_mfma_f32_16x16x32_bf16 v[28:31], a[24:27], a[64:67], v[28:31]\n"
"ds_read_b64_tr_b16 a[16:17], v218 offset:16384\n"
"ds_read_b64_tr_b16 a[18:19], v218 offset:17408\n"
"v_mfma_f32_16x16x32_bf16 v[28:31], a[28:31], a[68:71], v[28:31]\n"
"ds_read_b64_tr_b16 a[20:21], v218 offset:24576\n"
"ds_read_b64_tr_b16 a[22:23], v218 offset:25600\n"
"v_mfma_f32_16x16x32_bf16 v[60:63], a[28:31], a[76:79], v[60:63]\n"
"s_lshl_b32 s98, s10, 7\n"
"v_max3_u32 v145, v145, v143, s14\n"
"v_cmp_ne_u32 vcc, v144, v145\n"
"v_mfma_f32_16x16x32_bf16 v[60:63], a[24:27], a[72:75], v[60:63]\n"
"s_add_u32 s98, s98, 147456\n"
"v_and_b32 v175, 48, v242\n"
"v_add_u32 v175, s98, v175\n"
"v_mfma_f32_16x16x32_bf16 v[92:95], a[24:27], a[80:83], v[92:95]\n"
"s_lshl_b32 s98, s10, 7\n"
"v_and_b32 v171, 15, v242\n"
"v_lshlrev_b32 v171, 2, v171\n"
"v_mfma_f32_16x16x32_bf16 v[92:95], a[28:31], a[84:87], v[92:95]\n"
"s_cbranch_vccnz L_keys_retry\n"
"L_keys_back:\n"
"v_max_u32 v146, v128, v130\n"
"v_min_u32 v147, v128, v130\n"
"v_mfma_f32_16x16x32_bf16 v[124:127], a[28:31], a[92:95], v[124:127]\n"
"v_med3_u32 v147, v146, v147, v132\n"
"v_max_u32 v146, v146, v132\n"
"v_mfma_f32_16x16x32_bf16 v[124:127], a[24:27], a[88:91], v[124:127]\n"
"s_waitcnt lgkmcnt(15)\n"
"s_waitcnt lgkmcnt(14)\n"
"v_med3_u32 v147, v146, v147, v134\n"
"v_max_u32 v146, v146, v134\n"
"v_med3_u32 v147, v146, v147, v136\n"
"v_max_u32 v146, v146, v136\n"
"v_med3_u32 v147, v146, v147, v138\n"
"v_max_u32 v146, v146, v138\n"
"v_med3_u32 v147, v146, v147, v140\n"
"v_max_u32 v146, v146, v140\n"
"v_med3_u32 v147, v146, v147, v142\n"
"v_mfma_f32_16x16x32_bf16 v[0:3], a[0:3], a[32:35], v[0:3]\n"
"buffer_load_dwordx4 v238, s[44:47], s25 offen offset:0 lds\n"
"v_mfma_f32_16x16x32_bf16 v[0:3], a[4:7], a[36:39], v[0:3]\n"
"ds_read_b64_tr_b16 a[24:25], v219 offset:16384\n"
"ds_read_b64_tr_b16 a[26:27], v219 offset:17408\n"
"s_waitcnt lgkmcnt(14)\n"
"v_mfma_f32_16x16x32_bf16 v[32:35], a[4:7], a[44:47], v[32:35]\n"
"ds_read_b64_tr_b16 a[28:29], v219 offset:24576\n"
"ds_read_b64_tr_b16 a[30:31], v219 offset:25600\n"
"v_mfma_f32_16x16x32_bf16 v[32:35], a[0:3], a[40:43], v[32:35]\n"
"s_waitcnt lgkmcnt(14)\n"
"v_max_u32 v146, v146, v142\n"
"v_mov_b32 v148, v146\n"
"v_mfma_f32_16x16x32_bf16 v[64:67], a[0:3], a[48:51], v[64:67]\n"
"s_nop 0\n"
"v_max_u32_dpp v148, v148, v148 quad_perm:[1,0,3,2] row_mask:0xf bank_mask:0xf\n"
"v_add_u32 v171, 147456, v171\n"
"v_mfma_f32_16x16x32_bf16 v[64:67], a[4:7], a[52:55], v[64:67]\n"
"s_waitcnt lgkmcnt(12)\n"
"v_xor_b32 v171, s98, v171\n"
"v_xor_b32 v172, 128, v171\n"
"v_mfma_f32_16x16x32_bf16 v[96:99], a[4:7], a[60:63], v[96:99]\n"
"v_max_u32_dpp v148, v148, v148 quad_perm:[2,3,0,1] row_mask:0xf bank_mask:0xf\n"
"v_xor_b32 v173, 256, v171\n"
"v_mfma_f32_16x16x32_bf16 v[96:99], a[0:3], a[56:59], v[96:99]\n"
"v_xor_b32 v174, 384, v171\n"
"v_lshrrev_b32 v204, 3, v242\n"
"v_mfma_f32_16x16x32_bf16 v[196:199], a[0:3], a[252:255], v[196:199]\n"
"v_max_u32_dpp v148, v148, v148 row_half_mirror row_mask:0xf bank_mask:0xf\n"
"v_and_b32 v205, 6, v204\n"
"v_mfma_f32_16x16x32_bf16 v[196:199], a[4:7], a[252:255], v[196:199]\n"
"s_waitcnt lgkmcnt(8)\n"
"v_and_b32 v206, 7, v242\n"
"v_xor_b32 v205, v205, v206\n"
"v_mfma_f32_16x16x32_bf16 v[4:7], a[8:11], a[32:35], v[4:7]\n"
"buffer_load_dwordx4 v239, s[44:47], s25 offen offset:1024 lds\n"
"v_max_u32_dpp v148, v148, v148 row_mirror row_mask:0xf bank_mask:0xf\n"
"v_mfma_f32_16x16x32_bf16 v[4:7], a[12:15], a[36:39], v[4:7]\n"
"s_add_u32 m0, s30, 8192\n"
"ds_read_b64_tr_b16 a[0:1], v220 offset:16384\n"
"v_mfma_f32_16x16x32_bf16 v[36:39], a[12:15], a[44:47], v[36:39]\n"
"ds_read_b64_tr_b16 a[2:3], v220 offset:17408\n"
"ds_read_b64_tr_b16 a[4:5], v220 offset:24576\n"
"v_mfma_f32_16x16x32_bf16 v[36:39], a[8:11], a[40:43], v[36:39]\n"
"ds_read_b64_tr_b16 a[6:7], v220 offset:25600\n"
"v_max_u32_dpp v148, v148, v148 row_bcast:15 row_mask:0xa bank_mask:0xf\n"
"v_mfma_f32_16x16x32_bf16 v[68:71], a[8:11], a[48:51], v[68:71]\n"
"v_lshlrev_b32 v205, 4, v205\n"
"v_lshl_add_u32 v230, v204, 10, v205\n"
"v_mfma_f32_16x16x32_bf16 v[68:71], a[12:15], a[52:55], v[68:71]\n"
"v_max_u32_dpp v148, v148, v148 row_bcast:31 row_mask:0xc bank_mask:0xf\n"
"v_add_u32 v231, 7168, v230\n"
"v_mfma_f32_16x16x32_bf16 v[100:103], a[12:15], a[60:63], v[100:103]\n"
"v_add_u32 v232, 14336, v230\n"
"v_add_u32 v233, 21504, v230\n"
"v_mfma_f32_16x16x32_bf16 v[100:103], a[8:11], a[56:59], v[100:103]\n"
"s_add_u32 s8, s90, 65536\n"
"v_readlane_b32 s80, v148, 63\n"
"v_lshrrev_b32 v205, 2, v242\n"
"v_mfma_f32_16x16x32_bf16 v[200:203], a[8:11], a[252:255], v[200:203]\n"
"v_and_b32 v204, 3, v204\n"
"v_lshl_add_u32 v204, v204, 5, s8\n"
"v_mfma_f32_16x16x32_bf16 v[200:203], a[12:15], a[252:255], v[200:203]\n"
"s_waitcnt lgkmcnt(8)\n"
"v_cmp_eq_u32 vcc, s80, v146\n"
"v_and_b32 v206, 3, v242\n"
"v_mfma_f32_16x16x32_bf16 v[8:11], a[16:19], a[32:35], v[8:11]\n"
"buffer_load_dwordx4 v240, s[48:51], s25 offen offset:0 lds\n"
"v_mfma_f32_16x16x32_bf16 v[8:11], a[20:23], a[36:39], v[8:11]\n"
"ds_read_b64_tr_b16 a[8:9], v221 offset:16384\n"
"ds_read_b64_tr_b16 a[10:11], v221 offset:17408\n"
"v_cndmask_b32 v148, v146, v147, vcc\n"
"v_mfma_f32_16x16x32_bf16 v[40:43], a[20:23], a[44:47], v[40:43]\n"
"ds_read_b64_tr_b16 a[12:13], v221 offset:24576\n"
"ds_read_b64_tr_b16 a[14:15], v221 offset:25600\n"
"v_mfma_f32_16x16x32_bf16 v[40:43], a[16:19], a[40:43], v[40:43]\n"
"v_max_u32_dpp v148, v148, v148 quad_perm:[1,0,3,2] row_mask:0xf bank_mask:0xf\n"
"v_lshl_add_u32 v204, v206, 3, v204\n"
"v_mfma_f32_16x16x32_bf16 v[72:75], a[16:19], a[48:51], v[72:75]\n"
"v_lshl_add_u32 v234, v205, 7, v204\n"
"v_xor_b32 v235, 32, v234\n"
"v_mfma_f32_16x16x32_bf16 v[72:75], a[20:23], a[52:55], v[72:75]\n"
"v_max_u32_dpp v148, v148, v148 quad_perm:[2,3,0,1] row_mask:0xf bank_mask:0xf\n"
"v_xor_b32 v236, 64, v234\n"
"v_mfma_f32_16x16x32_bf16 v[104:107], a[20:23], a[60:63], v[104:107]\n"
"v_xor_b32 v237, 96, v234\n"
"v_bfe_u32 v204, v242, 2, 4\n"
"v_mfma_f32_16x16x32_bf16 v[104:107], a[16:19], a[56:59], v[104:107]\n"
"s_waitcnt lgkmcnt(8)\n"
"v_max_u32_dpp v148, v148, v148 row_half_mirror row_mask:0xf bank_mask:0xf\n"
"v_bfe_u32 v205, v242, 2, 3\n"
"v_mfma_f32_16x16x32_bf16 v[12:15], a[24:27], a[32:35], v[12:15]\n"
"ds_read_b64_tr_b16 a[16:17], v222 offset:16384\n"
"ds_read_b64_tr_b16 a[18:19], v222 offset:17408\n"
"v_mfma_f32_16x16x32_bf16 v[12:15], a[28:31], a[36:39], v[12:15]\n"
"ds_read_b64_tr_b16 a[20:21], v222 offset:24576\n"
"ds_read_b64_tr_b16 a[22:23], v222 offset:25600\n"
"v_mfma_f32_16x16x32_bf16 v[44:47], a[28:31], a[44:47], v[44:47]\n"
"buffer_load_dwordx4 v241, s[48:51], s25 offen offset:1024 lds\n"
"v_max_u32_dpp v148, v148, v148 row_mirror row_mask:0xf bank_mask:0xf\n"
"v_mfma_f32_16x16x32_bf16 v[44:47], a[24:27], a[40:43], v[44:47]\n"
"s_add_u32 m0, s30, 16384\n"
"s_xor_b32 s25, s19, 8192\n"
"v_and_b32 v206, 3, v242\n"
"v_mfma_f32_16x16x32_bf16 v[76:79], a[24:27], a[48:51], v[76:79]\n"
"v_max_u32_dpp v148, v148, v148 row_bcast:15 row_mask:0xa bank_mask:0xf\n"
"v_lshlrev_b32 v205, 5, v205\n"
"v_mfma_f32_16x16x32_bf16 v[76:79], a[28:31], a[52:55], v[76:79]\n"
"v_lshl_add_u32 v205, v206, 3, v205\n"
"v_lshl_add_u32 v207, v204, 8, v205\n"
"v_mfma_f32_16x16x32_bf16 v[108:111], a[28:31], a[60:63], v[108:111]\n"
"v_max_u32_dpp v148, v148, v148 row_bcast:31 row_mask:0xc bank_mask:0xf\n"
"v_lshrrev_b32 v248, 2, v242\n"
"v_mfma_f32_16x16x32_bf16 v[108:111], a[24:27], a[56:59], v[108:111]\n"
"s_waitcnt lgkmcnt(8)\n"
"v_mul_u32_u24 v248, 2752, v248\n"
"v_and_b32 v249, 48, v243\n"
"v_mfma_f32_16x16x32_bf16 v[16:19], a[0:3], a[32:35], v[16:19]\n"
"ds_read_b64_tr_b16 a[24:25], v223 offset:16384\n"
"ds_read_b64_tr_b16 a[26:27], v223 offset:17408\n"
"v_readlane_b32 s81, v148, 63\n"
"v_mfma_f32_16x16x32_bf16 v[16:19], a[4:7], a[36:39], v[16:19]\n"
"ds_read_b64_tr_b16 a[28:29], v223 offset:24576\n"
"ds_read_b64_tr_b16 a[30:31], v223 offset:25600\n"
"v_mfma_f32_16x16x32_bf16 v[48:51], a[4:7], a[44:47], v[48:51]\n"
"v_add3_u32 v248, v248, v249, s3\n"
"v_add_u32 v249, 43008, v248\n"
"v_mfma_f32_16x16x32_bf16 v[48:51], a[0:3], a[40:43], v[48:51]\n"
"s_lshl_b32 s96, s10, 3\n"
"v_mov_b32 v148, s80\n"
"v_mov_b32 v149, s81\n"
"v_mov_b32 v151, 147968\n"
"v_add_u32 v150, s96, v151\n"
"v_mfma_f32_16x16x32_bf16 v[80:83], a[0:3], a[48:51], v[80:83]\n"
"buffer_load_dwordx4 v238, s[44:47], s25 offen offset:0 lds\n"
"v_mfma_f32_16x16x32_bf16 v[80:83], a[4:7], a[52:55], v[80:83]\n"
"ds_write_b64 v150, v[148:149]\n"
"v_mfma_f32_16x16x32_bf16 v[112:115], a[4:7], a[60:63], v[112:115]\n"
"v_mfma_f32_16x16x32_bf16 v[112:115], a[0:3], a[56:59], v[112:115]\n"
"s_waitcnt lgkmcnt(9)\n"
"v_mfma_f32_16x16x32_bf16 v[20:23], a[8:11], a[32:35], v[20:23]\n"
"v_mfma_f32_16x16x32_bf16 v[20:23], a[12:15], a[36:39], v[20:23]\n"
"v_mfma_f32_16x16x32_bf16 v[52:55], a[12:15], a[44:47], v[52:55]\n"
"s_waitcnt lgkmcnt(0)\n"
"s_waitcnt vmcnt(5)\n"
"v_mfma_f32_16x16x32_bf16 v[52:55], a[8:11], a[40:43], v[52:55]\n"
"s_barrier\n"
"v_mfma_f32_16x16x32_bf16 v[84:87], a[8:11], a[48:51], v[84:87]\n"
"ds_read_b128 a[64:67], v228 offset:32768\n"
"ds_read_b128 a[68:71], v229 offset:32768\n"
"v_min3_u32 v169, v161, v163, v165\n"
"v_max3_u32 v170, v161, v163, v165\n"
"v_min3_u32 v169, v169, v167, s14\n"
"v_max3_u32 v170, v170, v167, s14\n"
"v_cmp_ne_u32 vcc, v169, v170\n"
"v_mfma_f32_16x16x32_bf16 v[84:87], a[12:15], a[52:55], v[84:87]\n"
"ds_read_b64_tr_b16 a[0:1], v216 offset:32768\n"
"ds_read_b64_tr_b16 a[2:3], v216 offset:33792\n"
"v_mfma_f32_16x16x32_bf16 v[116:119], a[12:15], a[60:63], v[116:119]\n"
"buffer_load_dwordx4 v239, s[44:47], s25 offen offset:1024 lds\n"
"v_mfma_f32_16x16x32_bf16 v[116:119], a[8:11], a[56:59], v[116:119]\n"
"s_add_u32 m0, s30, 24576\n"
"ds_read_b64_tr_b16 a[4:5], v216 offset:40960\n"
"v_mfma_f32_16x16x32_bf16 v[24:27], a[16:19], a[32:35], v[24:27]\n"
"ds_read_b64_tr_b16 a[6:7], v216 offset:41984\n"
"ds_read_b128 a[72:75], v228 offset:34816\n"
"v_mfma_f32_16x16x32_bf16 v[24:27], a[20:23], a[36:39], v[24:27]\n"
"ds_read_b128 a[76:79], v229 offset:34816\n"
"ds_read_b128 a[80:83], v228 offset:36864\n"
"v_mfma_f32_16x16x32_bf16 v[56:59], a[20:23], a[44:47], v[56:59]\n"
"ds_read_b128 a[84:87], v229 offset:36864\n"
"ds_read_b128 a[88:91], v228 offset:38912\n"
"v_mfma_f32_16x16x32_bf16 v[56:59], a[16:19], a[40:43], v[56:59]\n"
"ds_read_b128 a[92:95], v229 offset:38912\n"
"ds_read_b64_tr_b16 a[8:9], v217 offset:32768\n"
"v_mfma_f32_16x16x32_bf16 v[88:91], a[16:19], a[48:51], v[88:91]\n"
"ds_read_b64_tr_b16 a[10:11], v217 offset:33792\n"
"ds_read_b64_tr_b16 a[12:13], v217 offset:40960\n"
"ds_read_b64_tr_b16 a[14:15], v217 offset:41984\n"
"v_mfma_f32_16x16x32_bf16 v[88:91], a[20:23], a[52:55], v[88:91]\n"
"ds_read_b64_tr_b16 a[112:113], v220 offset:32768\n"
"ds_read_b64_tr_b16 a[114:115], v220 offset:33792\n"
"v_mfma_f32_16x16x32_bf16 v[120:123], a[20:23], a[60:63], v[120:123]\n"
"ds_read_b64_tr_b16 a[116:117], v220 offset:40960\n"
"ds_read_b64_tr_b16 a[118:119], v220 offset:41984\n"
"v_mfma_f32_16x16x32_bf16 v[120:123], a[16:19], a[56:59], v[120:123]\n"
"ds_read_b64_tr_b16 a[120:121], v221 offset:32768\n"
"ds_read_b64_tr_b16 a[122:123], v221 offset:33792\n"
"s_waitcnt lgkmcnt(15)\n"
"v_mfma_f32_16x16x32_bf16 v[196:199], a[0:3], a[252:255], v[196:199]\n"
"buffer_load_dwordx4 v240, s[48:51], s25 offen offset:0 lds\n"
"v_mfma_f32_16x16x32_bf16 v[196:199], a[4:7], a[252:255], v[196:199]\n"
"ds_read_b64_tr_b16 a[16:17], v218 offset:32768\n"
"ds_read_b64_tr_b16 a[18:19], v218 offset:33792\n"
"s_waitcnt lgkmcnt(8)\n"
"v_mfma_f32_16x16x32_bf16 v[200:203], a[8:11], a[252:255], v[200:203]\n"
"ds_read_b64_tr_b16 a[20:21], v218 offset:40960\n"
"ds_read_b64_tr_b16 a[22:23], v218 offset:41984\n"
"v_mfma_f32_16x16x32_bf16 v[200:203], a[12:15], a[252:255], v[200:203]\n"
"ds_read_b64_tr_b16 a[124:125], v221 offset:40960\n"
"ds_read_b64_tr_b16 a[126:127], v221 offset:41984\n"
"v_mfma_f32_16x16x32_bf16 v[28:31], a[24:27], a[32:35], v[28:31]\n"
"ds_read_b64_tr_b16 a[128:129], v222 offset:32768\n"
"ds_read_b64_tr_b16 a[130:131], v222 offset:33792\n"
"v_mfma_f32_16x16x32_bf16 v[28:31], a[28:31], a[36:39], v[28:31]\n"
"ds_read_b64_tr_b16 a[132:133], v222 offset:40960\n"
"ds_read_b64_tr_b16 a[134:135], v222 offset:41984\n"
"v_mfma_f32_16x16x32_bf16 v[60:63], a[28:31], a[44:47], v[60:63]\n"
"ds_read_b64_tr_b16 a[136:137], v223 offset:32768\n"
"ds_read_b64_tr_b16 a[138:139], v223 offset:33792\n"
"v_mfma_f32_16x16x32_bf16 v[60:63], a[24:27], a[40:43], v[60:63]\n"
"ds_read_b64_tr_b16 a[140:141], v223 offset:40960\n"
"ds_read_b64_tr_b16 a[142:143], v223 offset:41984\n"
"v_mfma_f32_16x16x32_bf16 v[92:95], a[24:27], a[48:51], v[92:95]\n"
"s_cbranch_vccnz L_stats_retry\n"
"L_stats_back:\n"
"v_cmp_gt_u32 vcc, 16, v242\n"
"v_xor_b32 v216, 0, v207\n"
"v_mfma_f32_16x16x32_bf16 v[92:95], a[28:31], a[52:55], v[92:95]\n"
"s_mov_b32 s97, 0x01000100\n"
"s_mov_b32 exec_lo, 0x00010001\n"
"s_mov_b32 exec_hi, 0x00010001\n"
"ds_write_b128 v175, v[196:199]\n"
"ds_write_b128 v175, v[200:203] offset:64\n"
"s_mov_b64 exec, -1\n"
"v_and_b32 v244, 0xffff0000, v160\n"
"v_rcp_f32 v244, v244\n"
"v_perm_b32 v160, v160, v160, s97\n"
"v_xor_b32 v160, 0x80008000, v160\n"
"v_cndmask_b32 v160, 0, v160, vcc\n"
"v_mul_f32 v244, 0x3e0293ee, v244\n"
"v_accvgpr_write_b32 a96, v160\n"
"v_and_b32 v245, 0xffff0000, v162\n"
"v_rcp_f32 v245, v245\n"
"v_mfma_f32_16x16x32_bf16 v[124:127], a[28:31], a[60:63], v[124:127]\n"
"buffer_load_dwordx4 v241, s[48:51], s25 offen offset:1024 lds\n"
"v_mfma_f32_16x16x32_bf16 v[124:127], a[24:27], a[56:59], v[124:127]\n"
"s_add_u32 m0, s30, 32768\n"
"s_xor_b32 s25, s19, 16384\n"
"v_mfma_f32_16x16x32_bf16 v[0:3], a[0:3], a[64:67], v[0:3]\n"
"ds_read_b64_tr_b16 a[24:25], v219 offset:32768\n"
"ds_read_b64_tr_b16 a[26:27], v219 offset:33792\n"
"v_mfma_f32_16x16x32_bf16 v[0:3], a[4:7], a[68:71], v[0:3]\n"
"ds_read_b64_tr_b16 a[28:29], v219 offset:40960\n"
"ds_read_b64_tr_b16 a[30:31], v219 offset:41984\n"
"v_mfma_f32_16x16x32_bf16 v[32:35], a[0:3], a[72:75], v[32:35]\n"
"s_waitcnt lgkmcnt(4)\n"
"v_perm_b32 v162, v162, v162, s97\n"
"v_xor_b32 v162, 0x80008000, v162\n"
"v_cndmask_b32 v162, 0, v162, vcc\n"
"v_mul_f32 v245, 0x3e0293ee, v245\n"
"v_accvgpr_write_b32 a100, v162\n"
"v_and_b32 v246, 0xffff0000, v164\n"
"v_rcp_f32 v246, v246\n"
"v_perm_b32 v164, v164, v164, s97\n"
"v_xor_b32 v164, 0x80008000, v164\n"
"v_cndmask_b32 v164, 0, v164, vcc\n"
"v_mul_f32 v246, 0x3e0293ee, v246\n"
"v_accvgpr_write_b32 a104, v164\n"
"v_mfma_f32_16x16x32_bf16 v[32:35], a[4:7], a[76:79], v[32:35]\n"
"s_barrier\n"
"v_mfma_f32_16x16x32_bf16 v[4:7], a[8:11], a[64:67], v[4:7]\n"
"ds_read2_b32 v[152:153], v171 offset0:0 offset1:16\n"
"v_and_b32 v247, 0xffff0000, v166\n"
"v_mfma_f32_16x16x32_bf16 v[4:7], a[12:15], a[68:71], v[4:7]\n"
"ds_read2_b32 v[154:155], v172 offset0:0 offset1:16\n"
"v_rcp_f32 v247, v247\n"
"v_mfma_f32_16x16x32_bf16 v[36:39], a[8:11], a[72:75], v[36:39]\n"
"ds_read2_b32 v[156:157], v173 offset0:0 offset1:16\n"
"ds_read2_b32 v[158:159], v174 offset0:0 offset1:16\n"
"v_perm_b32 v166, v166, v166, s97\n"
"v_xor_b32 v166, 0x80008000, v166\n"
"v_mfma_f32_16x16x32_bf16 v[36:39], a[12:15], a[76:79], v[36:39]\n"
"v_cndmask_b32 v166, 0, v166, vcc\n"
"v_mul_f32 v247, 0x3e0293ee, v247\n"
"v_accvgpr_write_b32 a108, v166\n"
"v_mfma_f32_16x16x32_bf16 v[8:11], a[16:19], a[64:67], v[8:11]\n"
"buffer_load_dwordx4 v238, s[44:47], s25 offen offset:0 lds\n"
"v_mfma_f32_16x16x32_bf16 v[8:11], a[20:23], a[68:71], v[8:11]\n"
"ds_read_b128 v[128:131], v151\n"
"ds_read_b128 v[132:135], v151 offset:16\n"
"v_mfma_f32_16x16x32_bf16 v[40:43], a[16:19], a[72:75], v[40:43]\n"
"s_waitcnt lgkmcnt(2)\n"
"v_cvt_pk_bf16_f32 v176, v152, v152\n"
"v_cvt_pk_bf16_f32 v178, v153, v153\n"
"v_mfma_f32_16x16x32_bf16 v[40:43], a[20:23], a[76:79], v[40:43]\n"
"v_lshlrev_b32 v176, 16, v176\n"
"v_lshlrev_b32 v178, 16, v178\n"
"v_mfma_f32_16x16x32_bf16 v[12:15], a[24:27], a[64:67], v[12:15]\n"
"v_sub_f32 v176, v152, v176\n"
"v_sub_f32 v178, v153, v178\n"
"v_mfma_f32_16x16x32_bf16 v[12:15], a[28:31], a[68:71], v[12:15]\n"
"v_cvt_pk_bf16_f32 v176, v152, v176\n"
"v_cvt_pk_bf16_f32 v178, v153, v178\n"
"v_mfma_f32_16x16x32_bf16 v[44:47], a[24:27], a[72:75], v[44:47]\n"
"v_cvt_pk_bf16_f32 v180, v154, v154\n"
"v_cvt_pk_bf16_f32 v182, v155, v155\n"
"v_mfma_f32_16x16x32_bf16 v[44:47], a[28:31], a[76:79], v[44:47]\n"
"v_lshlrev_b32 v180, 16, v180\n"
"v_lshlrev_b32 v182, 16, v182\n"
"v_mfma_f32_16x16x32_bf16 v[16:19], a[112:115], a[64:67], v[16:19]\n"
"s_waitcnt lgkmcnt(0)\n"
"v_sub_f32 v180, v154, v180\n"
"v_sub_f32 v182, v155, v182\n"
"v_max_u32 v146, v128, v129\n"
"v_min_u32 v147, v128, v129\n"
"v_mfma_f32_16x16x32_bf16 v[16:19], a[116:119], a[68:71], v[16:19]\n"
"v_cvt_pk_bf16_f32 v180, v154, v180\n"
"v_cvt_pk_bf16_f32 v182, v155, v182\n"
"v_mfma_f32_16x16x32_bf16 v[48:51], a[112:115], a[72:75], v[48:51]\n"
"buffer_load_dwordx4 v239, s[44:47], s25 offen offset:1024 lds\n"
"v_mfma_f32_16x16x32_bf16 v[48:51], a[116:119], a[76:79], v[48:51]\n"
"s_add_u32 m0, s30, 40960\n"
"v_cvt_pk_bf16_f32 v184, v156, v156\n"
"v_mfma_f32_16x16x32_bf16 v[20:23], a[120:123], a[64:67], v[20:23]\n"
"v_cvt_pk_bf16_f32 v186, v157, v157\n"
"v_lshlrev_b32 v184, 16, v184\n"
"v_mfma_f32_16x16x32_bf16 v[20:23], a[124:127], a[68:71], v[20:23]\n"
"v_lshlrev_b32 v186, 16, v186\n"
"v_sub_f32 v184, v156, v184\n"
"v_mfma_f32_16x16x32_bf16 v[52:55], a[120:123], a[72:75], v[52:55]\n"
"v_sub_f32 v186, v157, v186\n"
"v_cvt_pk_bf16_f32 v184, v156, v184\n"
"v_mfma_f32_16x16x32_bf16 v[52:55], a[124:127], a[76:79], v[52:55]\n"
"v_cvt_pk_bf16_f32 v186, v157, v186\n"
"v_cvt_pk_bf16_f32 v188, v158, v158\n"
"v_mfma_f32_16x16x32_bf16 v[24:27], a[128:131], a[64:67], v[24:27]\n"
"v_cvt_pk_bf16_f32 v190, v159, v159\n"
"v_lshlrev_b32 v188, 16, v188\n"
"v_mfma_f32_16x16x32_bf16 v[24:27], a[132:135], a[68:71], v[24:27]\n"
"v_lshlrev_b32 v190, 16, v190\n"
"v_sub_f32 v188, v158, v188\n"
"v_mfma_f32_16x16x32_bf16 v[56:59], a[128:131], a[72:75], v[56:59]\n"
"v_sub_f32 v190, v159, v190\n"
"v_cvt_pk_bf16_f32 v188, v158, v188\n"
"v_mfma_f32_16x16x32_bf16 v[56:59], a[132:135], a[76:79], v[56:59]\n"
"v_cvt_pk_bf16_f32 v190, v159, v190\n"
"v_med3_u32 v147, v146, v147, v130\n"
"v_max_u32 v146, v146, v130\n"
"v_med3_u32 v147, v146, v147, v131\n"
"v_max_u32 v146, v146, v131\n"
"v_mfma_f32_16x16x32_bf16 v[28:31], a[136:139], a[64:67], v[28:31]\n"
"buffer_load_dwordx4 v240, s[48:51], s25 offen offset:0 lds\n"
"v_mfma_f32_16x16x32_bf16 v[28:31], a[140:143], a[68:71], v[28:31]\n"
"v_med3_u32 v147, v146, v147, v132\n"
"v_max_u32 v146, v146, v132\n"
"v_mfma_f32_16x16x32_bf16 v[60:63], a[136:139], a[72:75], v[60:63]\n"
"v_med3_u32 v147, v146, v147, v133\n"
"v_max_u32 v146, v146, v133\n"
"v_mfma_f32_16x16x32_bf16 v[60:63], a[140:143], a[76:79], v[60:63]\n"
"v_med3_u32 v147, v146, v147, v134\n"
"v_max_u32 v146, v146, v134\n"
"v_mfma_f32_16x16x32_bf16 v[0:3], v[176:179], a[96:99], v[0:3]\n"
"v_med3_u32 v147, v146, v147, v135\n"
"L_ic2:\n"
"s_cbranch_execz L_ic6\n"
"v_max_u32 v146, v146, v135\n"
"v_mfma_f32_16x16x32_bf16 v[4:7], v[178:181], a[96:99], v[4:7]\n"
"v_readfirstlane_b32 s80, v146\n"
"v_readfirstlane_b32 s81, v147\n"
"v_mfma_f32_16x16x32_bf16 v[8:11], v[180:183], a[96:99], v[8:11]\n"
"v_xor_b32 v217, 32, v207\n"
"v_xor_b32 v218, 64, v207\n"
"v_mfma_f32_16x16x32_bf16 v[12:15], v[182:185], a[96:99], v[12:15]\n"
"s_and_b32 s82, s80, 0x7ff\n"
"s_sub_u32 s82, 0x7ff, s82\n"
"s_and_b32 s83, s81, 0x7ff\n"
"v_mfma_f32_16x16x32_bf16 v[16:19], v[184:187], a[96:99], v[16:19]\n"
"s_sub_u32 s83, 0x7ff, s83\n"
"s_mov_b32 s76, 0xcccccccc\n"
"s_mov_b32 s77, 0xcccccccc\n"
"v_mfma_f32_16x16x32_bf16 v[20:23], v[186:189], a[96:99], v[20:23]\n"
"s_mov_b32 s78, 0xf0f0f0f0\n"
"s_mov_b32 s79, 0xf0f0f0f0\n"
"s_or_b32 s99, s11, s10\n"
"s_cmp_eq_u32 s99, 0\n"
"s_cselect_b32 s99, 0, 2130706432\n"
"v_mfma_f32_16x16x32_bf16 v[24:27], v[188:191], a[96:99], v[24:27]\n"
"buffer_load_dwordx4 v241, s[48:51], s25 offen offset:1024 lds\n"
"v_mfma_f32_16x16x32_bf16 v[28:31], v[190:193], a[96:99], v[28:31]\n"
"v_mov_b32 v250, s14\n"
"v_mov_b32 v251, s99\n"
"v_mfma_f32_16x16x32_bf16 v[32:35], v[176:179], a[100:103], v[32:35]\n"
"s_mov_b64 exec, 1\n"
"buffer_store_dword v250, v251, s[56:59], 0 offen sc1\n"
"s_mov_b64 exec, -1\n"
"v_mfma_f32_16x16x32_bf16 v[36:39], v[178:181], a[100:103], v[36:39]\n"
"v_xor_b32 v219, 96, v207\n"
"v_xor_b32 v220, 128, v207\n"
"v_mul_f32 v0, v0, v244\n"
"v_mul_f32 v1, v1, v244\n"
"v_mul_f32 v2, v2, v244\n"
"v_mul_f32 v3, v3, v244\n"
"v_mul_f32 v4, v4, v244\n"
"v_mul_f32 v5, v5, v244\n"
"v_mul_f32 v6, v6, v244\n"
"v_mfma_f32_16x16x32_bf16 v[40:43], v[180:183], a[100:103], v[40:43]\n"
"v_xor_b32 v221, 160, v207\n"
"v_xor_b32 v222, 192, v207\n"
"v_mfma_f32_16x16x32_bf16 v[44:47], v[182:185], a[100:103], v[44:47]\n"
"v_xor_b32 v223, 224, v207\n"
"v_mul_f32 v7, v7, v244\n"
"v_mfma_f32_16x16x32_bf16 v[48:51], v[184:187], a[100:103], v[48:51]\n"
"v_cvt_pk_bf16_f32 v128, v0, v1\n"
"v_cvt_pk_bf16_f32 v129, v2, v3\n"
"v_mfma_f32_16x16x32_bf16 v[52:55], v[186:189], a[100:103], v[52:55]\n"
"v_cvt_pk_bf16_f32 v130, v4, v5\n"
"v_cvt_pk_bf16_f32 v131, v6, v7\n"
"v_mfma_f32_16x16x32_bf16 v[56:59], v[188:191], a[100:103], v[56:59]\n"
"v_mul_f32 v8, v8, v244\n"
"v_mul_f32 v9, v9, v244\n"
"v_mfma_f32_16x16x32_bf16 v[60:63], v[190:193], a[100:103], v[60:63]\n"
"v_mul_f32 v10, v10, v244\n"
"v_mfma_f32_16x16x32_bf16 v[64:67], a[0:3], a[80:83], v[64:67]\n"
"v_mul_f32 v11, v11, v244\n"
"v_mul_f32 v12, v12, v244\n"
"v_mfma_f32_16x16x32_bf16 v[64:67], a[4:7], a[84:87], v[64:67]\n"
"s_add_u32 m0, s30, 49152\n"
"s_xor_b32 s25, s19, 24576\n"
"v_mul_f32 v13, v13, v244\n"
"v_mul_f32 v14, v14, v244\n"
"v_mul_f32 v15, v15, v244\n"
"v_cvt_pk_bf16_f32 v132, v8, v9\n"
"v_cvt_pk_bf16_f32 v133, v10, v11\n"
"v_cvt_pk_bf16_f32 v134, v12, v13\n"
"v_mfma_f32_16x16x32_bf16 v[64:67], v[176:179], a[104:107], v[64:67]\n"
"buffer_load_dwordx4 v238, s[44:47], s25 offen offset:0 lds\n"
"v_mfma_f32_16x16x32_bf16 v[68:71], a[8:11], a[80:83], v[68:71]\n"
"v_cvt_pk_bf16_f32 v135, v14, v15\n"
"v_permlane16_swap_b32 v128, v130\n"
"v_mfma_f32_16x16x32_bf16 v[68:71], a[12:15], a[84:87], v[68:71]\n"
"v_permlane16_swap_b32 v129, v131\n"
"v_mul_f32 v16, v16, v244\n"
"v_mfma_f32_16x16x32_bf16 v[68:71], v[178:181], a[104:107], v[68:71]\n"
"v_mul_f32 v17, v17, v244\n"
"v_mul_f32 v18, v18, v244\n"
"v_mfma_f32_16x16x32_bf16 v[72:75], a[16:19], a[80:83], v[72:75]\n"
"v_mul_f32 v19, v19, v244\n"
"v_mul_f32 v20, v20, v244\n"
"v_mfma_f32_16x16x32_bf16 v[72:75], a[20:23], a[84:87], v[72:75]\n"
"v_mul_f32 v21, v21, v244\n"
"v_mul_f32 v22, v22, v244\n"
"v_mfma_f32_16x16x32_bf16 v[72:75], v[180:183], a[104:107], v[72:75]\n"
"v_mul_f32 v23, v23, v244\n"
"v_cvt_pk_bf16_f32 v136, v16, v17\n"
"v_mfma_f32_16x16x32_bf16 v[76:79], a[24:27], a[80:83], v[76:79]\n"
"v_cvt_pk_bf16_f32 v137, v18, v19\n"
"v_cvt_pk_bf16_f32 v138, v20, v21\n"
"v_mfma_f32_16x16x32_bf16 v[76:79], a[28:31], a[84:87], v[76:79]\n"
"v_cvt_pk_bf16_f32 v139, v22, v23\n"
"v_permlane16_swap_b32 v132, v134\n"
"v_mfma_f32_16x16x32_bf16 v[76:79], v[182:185], a[104:107], v[76:79]\n"
"s_waitcnt vmcnt(10)\n"
"v_permlane16_swap_b32 v133, v135\n"
"v_mul_f32 v24, v24, v244\n"
"v_mul_f32 v25, v25, v244\n"
"v_mul_f32 v26, v26, v244\n"
"v_mul_f32 v27, v27, v244\n"
"v_mul_f32 v28, v28, v244\n"
"v_mul_f32 v29, v29, v244\n"
"v_mul_f32 v30, v30, v244\n"
"v_mul_f32 v31, v31, v244\n"
"v_cvt_pk_bf16_f32 v140, v24, v25\n"
"v_cvt_pk_bf16_f32 v141, v26, v27\n"
"v_cvt_pk_bf16_f32 v142, v28, v29\n"
"v_cvt_pk_bf16_f32 v143, v30, v31\n"
"v_permlane16_swap_b32 v136, v138\n"
"v_permlane16_swap_b32 v137, v139\n"
"v_mul_f32 v32, v32, v245\n"
"v_mfma_f32_16x16x32_bf16 v[80:83], a[112:115], a[80:83], v[80:83]\n"
"s_barrier\n"
"v_mfma_f32_16x16x32_bf16 v[80:83], a[116:119], a[84:87], v[80:83]\n"
"buffer_load_dwordx4 v239, s[44:47], s25 offen offset:1024 lds\n"
"v_mfma_f32_16x16x32_bf16 v[80:83], v[184:187], a[104:107], v[80:83]\n"
"s_add_u32 m0, s30, 57344\n"
"ds_read_b128 a[172:175], v224 offset:0\n"
"v_mfma_f32_16x16x32_bf16 v[84:87], a[120:123], a[80:83], v[84:87]\n"
"ds_read_b128 a[176:179], v225 offset:0\n"
"ds_read_b128 a[180:183], v226 offset:0\n"
"v_mfma_f32_16x16x32_bf16 v[84:87], a[124:127], a[84:87], v[84:87]\n"
"ds_read_b128 a[184:187], v227 offset:0\n"
"ds_read_b128 a[188:191], v224 offset:4096\n"
"v_mfma_f32_16x16x32_bf16 v[84:87], v[186:189], a[104:107], v[84:87]\n"
"ds_read_b128 a[192:195], v225 offset:4096\n"
"ds_read_b128 a[196:199], v226 offset:4096\n"
"v_mfma_f32_16x16x32_bf16 v[88:91], a[128:131], a[80:83], v[88:91]\n"
"ds_read_b128 a[200:203], v227 offset:4096\n"
"v_mul_f32 v33, v33, v245\n"
"v_mfma_f32_16x16x32_bf16 v[88:91], a[132:135], a[84:87], v[88:91]\n"
"v_mul_f32 v34, v34, v245\n"
"v_mul_f32 v35, v35, v245\n"
"v_mfma_f32_16x16x32_bf16 v[88:91], v[188:191], a[104:107], v[88:91]\n"
"v_mul_f32 v36, v36, v245\n"
"v_mul_f32 v37, v37, v245\n"
"v_mfma_f32_16x16x32_bf16 v[92:95], a[136:139], a[80:83], v[92:95]\n"
"v_mul_f32 v38, v38, v245\n"
"v_mul_f32 v39, v39, v245\n"
"v_mfma_f32_16x16x32_bf16 v[92:95], a[140:143], a[84:87], v[92:95]\n"
"v_permlane16_swap_b32 v140, v142\n"
"v_permlane16_swap_b32 v141, v143\n"
"v_cvt_pk_bf16_f32 v144, v32, v33\n"
"v_cvt_pk_bf16_f32 v145, v34, v35\n"
"v_cvt_pk_bf16_f32 v146, v36, v37\n"
"v_cvt_pk_bf16_f32 v147, v38, v39\n"
"v_mul_f32 v40, v40, v245\n"
"v_mul_f32 v41, v41, v245\n"
"v_mfma_f32_16x16x32_bf16 v[92:95], v[190:193], a[104:107], v[92:95]\n"
"buffer_load_dwordx4 v240, s[48:51], s25 offen offset:0 lds\n"
"v_mfma_f32_16x16x32_bf16 v[96:99], a[0:3], a[88:91], v[96:99]\n"
"ds_read_b64_tr_b16 a[204:205], v216 offset:8192\n"
"ds_read_b64_tr_b16 a[206:207], v216 offset:12288\n"
"v_mfma_f32_16x16x32_bf16 v[96:99], a[4:7], a[92:95], v[96:99]\n"
"ds_read_b64_tr_b16 a[208:209], v217 offset:8192\n"
"ds_read_b64_tr_b16 a[210:211], v217 offset:12288\n"
"v_mfma_f32_16x16x32_bf16 v[96:99], v[176:179], a[108:111], v[96:99]\n"
"ds_read_b64_tr_b16 a[212:213], v218 offset:8192\n"
"ds_read_b64_tr_b16 a[214:215], v218 offset:12288\n"
"v_mfma_f32_16x16x32_bf16 v[100:103], a[8:11], a[88:91], v[100:103]\n"
"ds_read_b64_tr_b16 a[216:217], v219 offset:8192\n"
"ds_read_b64_tr_b16 a[218:219], v219 offset:12288\n"
"v_mfma_f32_16x16x32_bf16 v[100:103], a[12:15], a[92:95], v[100:103]\n"
"ds_read_b64_tr_b16 a[220:221], v220 offset:8192\n"
"ds_read_b64_tr_b16 a[222:223], v220 offset:12288\n"
"v_mfma_f32_16x16x32_bf16 v[100:103], v[178:181], a[108:111], v[100:103]\n"
"ds_read_b64_tr_b16 a[224:225], v221 offset:8192\n"
"ds_read_b64_tr_b16 a[226:227], v221 offset:12288\n"
"v_mfma_f32_16x16x32_bf16 v[104:107], a[16:19], a[88:91], v[104:107]\n"
"ds_read_b64_tr_b16 a[228:229], v222 offset:8192\n"
"ds_read_b64_tr_b16 a[230:231], v222 offset:12288\n"
"v_mfma_f32_16x16x32_bf16 v[104:107], a[20:23], a[92:95], v[104:107]\n"
"ds_read_b64_tr_b16 a[232:233], v223 offset:8192\n"
"ds_read_b64_tr_b16 a[234:235], v223 offset:12288\n"
"v_mfma_f32_16x16x32_bf16 v[104:107], v[180:183], a[108:111], v[104:107]\n"
"v_mul_f32 v42, v42, v245\n"
"v_mul_f32 v43, v43, v245\n"
"v_mul_f32 v44, v44, v245\n"
"v_mul_f32 v45, v45, v245\n"
"v_mul_f32 v46, v46, v245\n"
"v_mul_f32 v47, v47, v245\n"
"v_cvt_pk_bf16_f32 v148, v40, v41\n"
"v_cvt_pk_bf16_f32 v149, v42, v43\n"
"v_cvt_pk_bf16_f32 v150, v44, v45\n"
"v_cvt_pk_bf16_f32 v151, v46, v47\n"
"v_permlane16_swap_b32 v144, v146\n"
"v_permlane16_swap_b32 v145, v147\n"
"v_mul_f32 v48, v48, v245\n"
"v_mul_f32 v49, v49, v245\n"
"v_mfma_f32_16x16x32_bf16 v[108:111], a[24:27], a[88:91], v[108:111]\n"
"buffer_load_dwordx4 v241, s[48:51], s25 offen offset:1024 lds\n"
"v_mfma_f32_16x16x32_bf16 v[108:111], a[28:31], a[92:95], v[108:111]\n"
"v_mul_f32 v50, v50, v245\n"
"v_mul_f32 v51, v51, v245\n"
"v_mfma_f32_16x16x32_bf16 v[108:111], v[182:185], a[108:111], v[108:111]\n"
"v_mul_f32 v52, v52, v245\n"
"v_mul_f32 v53, v53, v245\n"
"v_mfma_f32_16x16x32_bf16 v[112:115], a[112:115], a[88:91], v[112:115]\n"
"v_mul_f32 v54, v54, v245\n"
"v_mul_f32 v55, v55, v245\n"
"v_mfma_f32_16x16x32_bf16 v[112:115], a[116:119], a[92:95], v[112:115]\n"
"v_cvt_pk_bf16_f32 v152, v48, v49\n"
"v_cvt_pk_bf16_f32 v153, v50, v51\n"
"v_mfma_f32_16x16x32_bf16 v[112:115], v[184:187], a[108:111], v[112:115]\n"
"v_cvt_pk_bf16_f32 v154, v52, v53\n"
"v_cvt_pk_bf16_f32 v155, v54, v55\n"
"v_mfma_f32_16x16x32_bf16 v[116:119], a[120:123], a[88:91], v[116:119]\n"
"v_permlane16_swap_b32 v148, v150\n"
"v_permlane16_swap_b32 v149, v151\n"
"v_mfma_f32_16x16x32_bf16 v[116:119], a[124:127], a[92:95], v[116:119]\n"
"v_mul_f32 v56, v56, v245\n"
"v_mul_f32 v57, v57, v245\n"
"v_mfma_f32_16x16x32_bf16 v[116:119], v[186:189], a[108:111], v[116:119]\n"
"v_mul_f32 v58, v58, v245\n"
"v_mul_f32 v59, v59, v245\n"
"v_mfma_f32_16x16x32_bf16 v[120:123], a[128:131], a[88:91], v[120:123]\n"
"v_mul_f32 v60, v60, v245\n"
"v_mul_f32 v61, v61, v245\n"
"v_mfma_f32_16x16x32_bf16 v[120:123], a[132:135], a[92:95], v[120:123]\n"
"v_mul_f32 v62, v62, v245\n"
"v_mul_f32 v63, v63, v245\n"
"v_mfma_f32_16x16x32_bf16 v[120:123], v[188:191], a[108:111], v[120:123]\n"
"v_cvt_pk_bf16_f32 v156, v56, v57\n"
"v_cvt_pk_bf16_f32 v157, v58, v59\n"
"v_mfma_f32_16x16x32_bf16 v[124:127], a[136:139], a[88:91], v[124:127]\n"
"v_cvt_pk_bf16_f32 v158, v60, v61\n"
"v_cvt_pk_bf16_f32 v159, v62, v63\n"
"v_mfma_f32_16x16x32_bf16 v[124:127], a[140:143], a[92:95], v[124:127]\n"
"v_permlane16_swap_b32 v152, v154\n"
"v_permlane16_swap_b32 v153, v155\n"
"v_mfma_f32_16x16x32_bf16 v[124:127], v[190:193], a[108:111], v[124:127]\n"
"s_waitcnt lgkmcnt(15)\n"
"v_mul_f32 v64, v64, v246\n"
"v_mul_f32 v65, v65, v246\n"
"v_mfma_f32_16x16x32_bf16 v[192:195], a[172:175], v[128:131], 0\n"
"v_mul_f32 v66, v66, v246\n"
"v_mul_f32 v67, v67, v246\n"
"v_mfma_f32_16x16x32_bf16 v[192:195], a[176:179], v[132:135], v[192:195]\n"
"v_mul_f32 v68, v68, v246\n"
"v_mul_f32 v69, v69, v246\n"
"v_mfma_f32_16x16x32_bf16 v[192:195], a[180:183], v[136:139], v[192:195]\n"
"v_mul_f32 v70, v70, v246\n"
"v_mul_f32 v71, v71, v246\n"
"v_mfma_f32_16x16x32_bf16 v[192:195], a[184:187], v[140:143], v[192:195]\n"
"v_permlane16_swap_b32 v156, v158\n"
"v_permlane16_swap_b32 v157, v159\n"
"v_mfma_f32_16x16x32_bf16 v[196:199], a[188:191], v[128:131], 0\n"
"v_cvt_pk_bf16_f32 v160, v64, v65\n"
"v_cvt_pk_bf16_f32 v161, v66, v67\n"
"v_mfma_f32_16x16x32_bf16 v[196:199], a[192:195], v[132:135], v[196:199]\n"
"v_cvt_pk_bf16_f32 v162, v68, v69\n"
"v_cvt_pk_bf16_f32 v163, v70, v71\n"
"v_mfma_f32_16x16x32_bf16 v[196:199], a[196:199], v[136:139], v[196:199]\n"
"v_mul_f32 v72, v72, v246\n"
"v_mul_f32 v73, v73, v246\n"
"v_mul_f32 v74, v74, v246\n"
"v_mul_f32 v75, v75, v246\n"
"v_mul_f32 v76, v76, v246\n"
"v_mfma_f32_16x16x32_bf16 v[196:199], a[200:203], v[140:143], v[196:199]\n"
"v_exp_f32 v192, v192\n"
"v_mfma_f32_16x16x32_bf16 v[200:203], a[172:175], v[144:147], 0\n"
"v_exp_f32 v193, v193\n"
"v_mfma_f32_16x16x32_bf16 v[200:203], a[176:179], v[148:151], v[200:203]\n"
"v_exp_f32 v194, v194\n"
"v_mfma_f32_16x16x32_bf16 v[200:203], a[180:183], v[152:155], v[200:203]\n"
"v_exp_f32 v195, v195\n"
"v_mfma_f32_16x16x32_bf16 v[200:203], a[184:187], v[156:159], v[200:203]\n"
"v_exp_f32 v196, v196\n"
"v_mfma_f32_16x16x32_bf16 v[204:207], a[188:191], v[144:147], 0\n"
"v_exp_f32 v197, v197\n"
"v_mfma_f32_16x16x32_bf16 v[204:207], a[192:195], v[148:151], v[204:207]\n"
"v_exp_f32 v198, v198\n"
"v_mfma_f32_16x16x32_bf16 v[204:207], a[196:199], v[152:155], v[204:207]\n"
"v_exp_f32 v199, v199\n"
"v_mfma_f32_16x16x32_bf16 v[204:207], a[200:203], v[156:159], v[204:207]\n"
"s_waitcnt lgkmcnt(12)\n"
"v_cvt_pk_bf16_f32 v208, v192, v193\n"
"v_cvt_pk_bf16_f32 v209, v194, v195\n"
"v_cvt_pk_bf16_f32 v210, v196, v197\n"
"v_cvt_pk_bf16_f32 v211, v198, v199\n"
"v_exp_f32 v200, v200\n"
"s_nop 0\n"
"v_mfma_f32_16x16x32_bf16 v[0:3], a[204:207], v[208:211], 0\n"
"v_mul_f32 v77, v77, v246\n"
"v_mul_f32 v78, v78, v246\n"
"v_mul_f32 v79, v79, v246\n"
"v_cvt_pk_bf16_f32 v164, v72, v73\n"
"v_cvt_pk_bf16_f32 v165, v74, v75\n"
"v_cvt_pk_bf16_f32 v166, v76, v77\n"
"v_cvt_pk_bf16_f32 v167, v78, v79\n"
"v_permlane16_swap_b32 v160, v162\n"
"v_permlane16_swap_b32 v161, v163\n"
"v_mul_f32 v80, v80, v246\n"
"v_mul_f32 v81, v81, v246\n"
"v_mul_f32 v82, v82, v246\n"
"v_mul_f32 v83, v83, v246\n"
"v_mul_f32 v84, v84, v246\n"
"v_mul_f32 v85, v85, v246\n"
"v_mul_f32 v86, v86, v246\n"
"v_mul_f32 v87, v87, v246\n"
"v_cvt_pk_bf16_f32 v168, v80, v81\n"
"v_mfma_f32_16x16x32_bf16 v[4:7], a[208:211], v[208:211], 0\n"
"s_waitcnt lgkmcnt(8)\n"
"v_exp_f32 v201, v201\n"
"v_mfma_f32_16x16x32_bf16 v[8:11], a[212:215], v[208:211], 0\n"
"v_cvt_pk_bf16_f32 v212, v200, v201\n"
"v_cvt_pk_bf16_f32 v169, v82, v83\n"
"v_cvt_pk_bf16_f32 v170, v84, v85\n"
"v_cvt_pk_bf16_f32 v171, v86, v87\n"
"v_permlane16_swap_b32 v164, v166\n"
"v_permlane16_swap_b32 v165, v167\n"
"v_mul_f32 v88, v88, v246\n"
"v_mul_f32 v89, v89, v246\n"
"v_mul_f32 v90, v90, v246\n"
"v_mul_f32 v91, v91, v246\n"
"v_mul_f32 v92, v92, v246\n"
"v_mul_f32 v93, v93, v246\n"
"v_mul_f32 v94, v94, v246\n"
"v_mul_f32 v95, v95, v246\n"
"v_cvt_pk_bf16_f32 v172, v88, v89\n"
"v_cvt_pk_bf16_f32 v173, v90, v91\n"
"v_cvt_pk_bf16_f32 v174, v92, v93\n"
"v_cvt_pk_bf16_f32 v175, v94, v95\n"
"v_permlane16_swap_b32 v168, v170\n"
"v_permlane16_swap_b32 v169, v171\n"
"v_mfma_f32_16x16x32_bf16 v[12:15], a[216:219], v[208:211], 0\n"
"s_waitcnt lgkmcnt(4)\n"
"v_exp_f32 v202, v202\n"
"v_mfma_f32_16x16x32_bf16 v[16:19], a[220:223], v[208:211], 0\n"
"v_mul_f32 v96, v96, v247\n"
"v_mul_f32 v97, v97, v247\n"
"v_mul_f32 v98, v98, v247\n"
"v_mul_f32 v99, v99, v247\n"
"v_mul_f32 v100, v100, v247\n"
"v_mul_f32 v101, v101, v247\n"
"v_mul_f32 v102, v102, v247\n"
"v_mul_f32 v103, v103, v247\n"
"v_permlane16_swap_b32 v172, v174\n"
"v_permlane16_swap_b32 v173, v175\n"
"v_cvt_pk_bf16_f32 v176, v96, v97\n"
"v_cvt_pk_bf16_f32 v177, v98, v99\n"
"v_mfma_f32_16x16x32_bf16 v[20:23], a[224:227], v[208:211], 0\n"
"s_waitcnt lgkmcnt(0)\n"
"v_exp_f32 v203, v203\n"
"v_mfma_f32_16x16x32_bf16 v[24:27], a[228:231], v[208:211], 0\n"
"v_cvt_pk_bf16_f32 v213, v202, v203\n"
"v_cvt_pk_bf16_f32 v178, v100, v101\n"
"v_cvt_pk_bf16_f32 v179, v102, v103\n"
"v_mul_f32 v104, v104, v247\n"
"v_mul_f32 v105, v105, v247\n"
"v_mul_f32 v106, v106, v247\n"
"v_mul_f32 v107, v107, v247\n"
"v_mfma_f32_16x16x32_bf16 v[28:31], a[232:235], v[208:211], 0\n"
"v_exp_f32 v204, v204\n"
"v_mfma_f32_16x16x32_bf16 a[236:239], a[252:255], v[208:211], 0\n"
"v_mul_f32 v108, v108, v247\n"
"v_mul_f32 v109, v109, v247\n"
"v_mul_f32 v110, v110, v247\n"
"v_mul_f32 v111, v111, v247\n"
"v_cvt_pk_bf16_f32 v180, v104, v105\n"
"v_cvt_pk_bf16_f32 v181, v106, v107\n"
"v_mfma_f32_16x16x32_bf16 v[192:195], a[172:175], v[160:163], 0\n"
"v_exp_f32 v205, v205\n"
"v_mfma_f32_16x16x32_bf16 v[192:195], a[176:179], v[164:167], v[192:195]\n"
"v_cvt_pk_bf16_f32 v214, v204, v205\n"
"v_cvt_pk_bf16_f32 v182, v108, v109\n"
"v_cvt_pk_bf16_f32 v183, v110, v111\n"
"v_permlane16_swap_b32 v176, v178\n"
"v_permlane16_swap_b32 v177, v179\n"
"v_mul_f32 v112, v112, v247\n"
"v_mul_f32 v113, v113, v247\n"
"v_mfma_f32_16x16x32_bf16 v[192:195], a[180:183], v[168:171], v[192:195]\n"
"v_exp_f32 v206, v206\n"
"v_mfma_f32_16x16x32_bf16 v[192:195], a[184:187], v[172:175], v[192:195]\n"
"v_mul_f32 v114, v114, v247\n"
"v_mul_f32 v115, v115, v247\n"
"v_mul_f32 v116, v116, v247\n"
"v_mul_f32 v117, v117, v247\n"
"v_mul_f32 v118, v118, v247\n"
"v_mul_f32 v119, v119, v247\n"
"v_mfma_f32_16x16x32_bf16 v[196:199], a[188:191], v[160:163], 0\n"
"v_exp_f32 v207, v207\n"
"v_mfma_f32_16x16x32_bf16 v[196:199], a[192:195], v[164:167], v[196:199]\n"
"s_waitcnt vmcnt(9)\n"
"v_cvt_pk_bf16_f32 v215, v206, v207\n"
"v_cvt_pk_bf16_f32 v184, v112, v113\n"
"v_cvt_pk_bf16_f32 v185, v114, v115\n"
"v_cvt_pk_bf16_f32 v186, v116, v117\n"
"v_cvt_pk_bf16_f32 v187, v118, v119\n"
"v_permlane16_swap_b32 v180, v182\n"
"v_permlane16_swap_b32 v181, v183\n"
"v_mfma_f32_16x16x32_bf16 v[196:199], a[196:199], v[168:171], v[196:199]\n"
"s_barrier\n"
"v_mfma_f32_16x16x32_bf16 v[196:199], a[200:203], v[172:175], v[196:199]\n"
"s_add_u32 m0, s30, 0\n"
"v_exp_f32 v192, v192\n"
"v_mfma_f32_16x16x32_bf16 v[32:35], a[204:207], v[212:215], 0\n"
"s_xor_b32 s25, s19, 32768\n"
"v_mul_f32 v120, v120, v247\n"
"v_mul_f32 v121, v121, v247\n"
"v_mul_f32 v122, v122, v247\n"
"v_mul_f32 v123, v123, v247\n"
"v_mul_f32 v124, v124, v247\n"
"v_mul_f32 v125, v125, v247\n"
"v_mfma_f32_16x16x32_bf16 v[36:39], a[208:211], v[212:215], 0\n"
"buffer_load_dwordx4 v238, s[44:47], s25 offen offset:0 lds\n"
"v_exp_f32 v193, v193\n"
"v_mfma_f32_16x16x32_bf16 v[40:43], a[212:215], v[212:215], 0\n"
"v_cvt_pk_bf16_f32 v208, v192, v193\n"
"v_mul_f32 v126, v126, v247\n"
"v_mul_f32 v127, v127, v247\n"
"v_cvt_pk_bf16_f32 v188, v120, v121\n"
"v_cvt_pk_bf16_f32 v189, v122, v123\n"
"v_cvt_pk_bf16_f32 v190, v124, v125\n"
"v_cvt_pk_bf16_f32 v191, v126, v127\n"
"v_mfma_f32_16x16x32_bf16 v[44:47], a[216:219], v[212:215], 0\n"
"v_exp_f32 v194, v194\n"
"v_mfma_f32_16x16x32_bf16 v[48:51], a[220:223], v[212:215], 0\n"
"v_permlane16_swap_b32 v184, v186\n"
"v_permlane16_swap_b32 v185, v187\n"
"v_permlane16_swap_b32 v188, v190\n"
"v_permlane16_swap_b32 v189, v191\n"
"v_mfma_f32_16x16x32_bf16 v[52:55], a[224:227], v[212:215], 0\n"
"v_exp_f32 v195, v195\n"
"v_mfma_f32_16x16x32_bf16 v[56:59], a[228:231], v[212:215], 0\n"
"v_cvt_pk_bf16_f32 v209, v194, v195\n"
"v_mfma_f32_16x16x32_bf16 v[60:63], a[232:235], v[212:215], 0\n"
"v_exp_f32 v196, v196\n"
"v_mfma_f32_16x16x32_bf16 a[240:243], a[252:255], v[212:215], 0\n"
"buffer_load_dwordx4 v239, s[44:47], s25 offen offset:1024 lds\n"
"v_mfma_f32_16x16x32_bf16 v[200:203], a[172:175], v[176:179], 0\n"
"s_add_u32 m0, s30, 8192\n"
"v_exp_f32 v197, v197\n"
"v_mfma_f32_16x16x32_bf16 v[200:203], a[176:179], v[180:183], v[200:203]\n"
"ds_read_b128 a[172:175], v224 offset:16384\n"
"v_cvt_pk_bf16_f32 v210, v196, v197\n"
"v_mfma_f32_16x16x32_bf16 v[200:203], a[180:183], v[184:187], v[200:203]\n"
"v_exp_f32 v198, v198\n"
"v_mfma_f32_16x16x32_bf16 v[200:203], a[184:187], v[188:191], v[200:203]\n"
"ds_read_b128 a[176:179], v225 offset:16384\n"
"ds_read_b128 a[180:183], v226 offset:16384\n"
"v_mfma_f32_16x16x32_bf16 v[204:207], a[188:191], v[176:179], 0\n"
"buffer_load_dwordx4 v240, s[48:51], s25 offen offset:0 lds\n"
"v_exp_f32 v199, v199\n"
"v_mfma_f32_16x16x32_bf16 v[204:207], a[192:195], v[180:183], v[204:207]\n"
"ds_read_b128 a[184:187], v227 offset:16384\n"
"v_cvt_pk_bf16_f32 v211, v198, v199\n"
"v_mfma_f32_16x16x32_bf16 v[204:207], a[196:199], v[184:187], v[204:207]\n"
"ds_read_b128 a[188:191], v224 offset:20480\n"
"ds_read_b128 a[192:195], v225 offset:20480\n"
"v_mfma_f32_16x16x32_bf16 v[204:207], a[200:203], v[188:191], v[204:207]\n"
"v_exp_f32 v200, v200\n"
"v_mfma_f32_16x16x32_bf16 v[64:67], a[204:207], v[208:211], 0\n"
"ds_read_b128 a[196:199], v226 offset:20480\n"
"ds_read_b128 a[200:203], v227 offset:20480\n"
"buffer_load_dwordx4 v241, s[48:51], s25 offen offset:1024 lds\n"
"v_mfma_f32_16x16x32_bf16 v[68:71], a[208:211], v[208:211], 0\n"
"s_add_u32 m0, s30, 147456\n"
"v_exp_f32 v201, v201\n"
"v_mfma_f32_16x16x32_bf16 v[72:75], a[212:215], v[208:211], 0\n"
"v_cvt_pk_bf16_f32 v212, v200, v201\n"
"v_add_u32 v244, 65536, v234\n"
"v_add_u32 v245, 65536, v235\n"
"v_mfma_f32_16x16x32_bf16 v[76:79], a[216:219], v[208:211], 0\n"
"v_exp_f32 v202, v202\n"
"v_mfma_f32_16x16x32_bf16 v[80:83], a[220:223], v[208:211], 0\n"
"v_add_u32 v246, 65536, v236\n"
"v_add_u32 v247, 65536, v237\n"
"v_mfma_f32_16x16x32_bf16 v[84:87], a[224:227], v[208:211], 0\n"
"v_exp_f32 v203, v203\n"
"v_mfma_f32_16x16x32_bf16 v[88:91], a[228:231], v[208:211], 0\n"
"buffer_load_dwordx4 v248, s[64:67], s2 offen offset:0 lds\n"
"v_cvt_pk_bf16_f32 v213, v202, v203\n"
"v_mfma_f32_16x16x32_bf16 v[92:95], a[232:235], v[208:211], 0\n"
"s_add_u32 m0, s90, 65536\n"
"v_exp_f32 v204, v204\n"
"v_mfma_f32_16x16x32_bf16 a[244:247], a[252:255], v[208:211], 0\n"
"s_waitcnt lgkmcnt(6)\n"
"s_xor_b32 s24, s89, 0\n"
"v_mfma_f32_16x16x32_bf16 v[192:195], a[172:175], v[128:131], 0\n"
"v_exp_f32 v205, v205\n"
"v_mfma_f32_16x16x32_bf16 v[192:195], a[176:179], v[132:135], v[192:195]\n"
"s_waitcnt lgkmcnt(4)\n"
"v_cvt_pk_bf16_f32 v214, v204, v205\n"
"v_mfma_f32_16x16x32_bf16 v[192:195], a[180:183], v[136:139], v[192:195]\n"
"v_exp_f32 v206, v206\n"
"v_mfma_f32_16x16x32_bf16 v[192:195], a[184:187], v[140:143], v[192:195]\n"
"s_waitcnt lgkmcnt(2)\n"
"v_mfma_f32_16x16x32_bf16 v[196:199], a[188:191], v[128:131], 0\n"
"v_exp_f32 v207, v207\n"
"v_mfma_f32_16x16x32_bf16 v[196:199], a[192:195], v[132:135], v[196:199]\n"
"s_waitcnt lgkmcnt(0)\n"
"v_cvt_pk_bf16_f32 v215, v206, v207\n"
"v_mfma_f32_16x16x32_bf16 v[196:199], a[196:199], v[136:139], v[196:199]\n"
"buffer_load_dwordx4 v230, s[52:55], s24 offen offset:0 lds\n"
"v_mfma_f32_16x16x32_bf16 v[196:199], a[200:203], v[140:143], v[196:199]\n"
"v_exp_f32 v192, v192\n"
"v_mfma_f32_16x16x32_bf16 v[96:99], a[204:207], v[212:215], 0\n"
"ds_read_b64_tr_b16 a[204:205], v216 offset:24576\n"
"ds_read_b64_tr_b16 a[206:207], v216 offset:28672\n"
"v_mfma_f32_16x16x32_bf16 v[100:103], a[208:211], v[212:215], 0\n"
"v_exp_f32 v193, v193\n"
"v_mfma_f32_16x16x32_bf16 v[104:107], a[212:215], v[212:215], 0\n"
"ds_read_b64_tr_b16 a[208:209], v217 offset:24576\n"
"v_cvt_pk_bf16_f32 v208, v192, v193\n"
"v_mfma_f32_16x16x32_bf16 v[108:111], a[216:219], v[212:215], 0\n"
"v_exp_f32 v194, v194\n"
"v_mfma_f32_16x16x32_bf16 v[112:115], a[220:223], v[212:215], 0\n"
"ds_read_b64_tr_b16 a[210:211], v217 offset:28672\n"
"ds_read_b64_tr_b16 a[212:213], v218 offset:24576\n"
"v_mfma_f32_16x16x32_bf16 v[116:119], a[224:227], v[212:215], 0\n"
"v_exp_f32 v195, v195\n"
"v_mfma_f32_16x16x32_bf16 v[120:123], a[228:231], v[212:215], 0\n"
"ds_read_b64_tr_b16 a[214:215], v218 offset:28672\n"
"v_cvt_pk_bf16_f32 v209, v194, v195\n"
"v_mfma_f32_16x16x32_bf16 v[124:127], a[232:235], v[212:215], 0\n"
"v_exp_f32 v196, v196\n"
"v_mfma_f32_16x16x32_bf16 a[248:251], a[252:255], v[212:215], 0\n"
"ds_read_b64_tr_b16 a[216:217], v219 offset:24576\n"
"ds_read_b64_tr_b16 a[218:219], v219 offset:28672\n"
"v_mfma_f32_16x16x32_bf16 v[200:203], a[172:175], v[144:147], 0\n"
"v_exp_f32 v197, v197\n"
"v_mfma_f32_16x16x32_bf16 v[200:203], a[176:179], v[148:151], v[200:203]\n"
"ds_read_b64_tr_b16 a[220:221], v220 offset:24576\n"
"v_cvt_pk_bf16_f32 v210, v196, v197\n"
"v_mfma_f32_16x16x32_bf16 v[200:203], a[180:183], v[152:155], v[200:203]\n"
"v_exp_f32 v198, v198\n"
"v_mfma_f32_16x16x32_bf16 v[200:203], a[184:187], v[156:159], v[200:203]\n"
"ds_read_b64_tr_b16 a[222:223], v220 offset:28672\n"
"ds_read_b64_tr_b16 a[224:225], v221 offset:24576\n"
"v_mfma_f32_16x16x32_bf16 v[204:207], a[188:191], v[144:147], 0\n"
"v_exp_f32 v199, v199\n"
"v_mfma_f32_16x16x32_bf16 v[204:207], a[192:195], v[148:151], v[204:207]\n"
"ds_read_b64_tr_b16 a[226:227], v221 offset:28672\n"
"v_cvt_pk_bf16_f32 v211, v198, v199\n"
"v_mfma_f32_16x16x32_bf16 v[204:207], a[196:199], v[152:155], v[204:207]\n"
"ds_read_b64_tr_b16 a[228:229], v222 offset:24576\n"
"ds_read_b64_tr_b16 a[230:231], v222 offset:28672\n"
"v_mfma_f32_16x16x32_bf16 v[204:207], a[200:203], v[156:159], v[204:207]\n"
"s_waitcnt lgkmcnt(10)\n"
"v_exp_f32 v200, v200\n"
"v_mfma_f32_16x16x32_bf16 v[0:3], a[204:207], v[208:211], v[0:3]\n"
"ds_read_b64_tr_b16 a[232:233], v223 offset:24576\n"
"ds_read_b64_tr_b16 a[234:235], v223 offset:28672\n"
"v_mfma_f32_16x16x32_bf16 v[4:7], a[208:211], v[208:211], v[4:7]\n"
"s_waitcnt lgkmcnt(8)\n"
"v_exp_f32 v201, v201\n"
"v_mfma_f32_16x16x32_bf16 v[8:11], a[212:215], v[208:211], v[8:11]\n"
"v_cvt_pk_bf16_f32 v212, v200, v201\n"
"v_mfma_f32_16x16x32_bf16 v[12:15], a[216:219], v[208:211], v[12:15]\n"
"s_waitcnt lgkmcnt(4)\n"
"v_exp_f32 v202, v202\n"
"v_mfma_f32_16x16x32_bf16 v[16:19], a[220:223], v[208:211], v[16:19]\n"
"buffer_load_dwordx4 v231, s[52:55], s24 offen offset:1024 lds\n"
"v_mfma_f32_16x16x32_bf16 v[20:23], a[224:227], v[208:211], v[20:23]\n"
"s_waitcnt lgkmcnt(0)\n"
"v_exp_f32 v203, v203\n"
"v_mfma_f32_16x16x32_bf16 v[24:27], a[228:231], v[208:211], v[24:27]\n"
"v_cvt_pk_bf16_f32 v213, v202, v203\n"
"v_mfma_f32_16x16x32_bf16 v[28:31], a[232:235], v[208:211], v[28:31]\n"
"v_exp_f32 v204, v204\n"
"v_mfma_f32_16x16x32_bf16 a[236:239], a[252:255], v[208:211], a[236:239]\n"
"buffer_load_dwordx4 v232, s[52:55], s24 offen offset:2048 lds\n"
"v_mfma_f32_16x16x32_bf16 v[192:195], a[172:175], v[160:163], 0\n"
"v_exp_f32 v205, v205\n"
"v_mfma_f32_16x16x32_bf16 v[192:195], a[176:179], v[164:167], v[192:195]\n"
"v_cvt_pk_bf16_f32 v214, v204, v205\n"
"v_mfma_f32_16x16x32_bf16 v[192:195], a[180:183], v[168:171], v[192:195]\n"
"v_exp_f32 v206, v206\n"
"v_mfma_f32_16x16x32_bf16 v[192:195], a[184:187], v[172:175], v[192:195]\n"
"buffer_load_dwordx4 v233, s[52:55], s24 offen offset:3072 lds\n"
"v_mfma_f32_16x16x32_bf16 v[196:199], a[188:191], v[160:163], 0\n"
"v_exp_f32 v207, v207\n"
"v_mfma_f32_16x16x32_bf16 v[196:199], a[192:195], v[164:167], v[196:199]\n"
"s_waitcnt vmcnt(14)\n"
"v_cvt_pk_bf16_f32 v215, v206, v207\n"
"v_mfma_f32_16x16x32_bf16 v[196:199], a[196:199], v[168:171], v[196:199]\n"
"s_barrier\n"
"v_mfma_f32_16x16x32_bf16 v[196:199], a[200:203], v[172:175], v[196:199]\n"
"s_add_u32 m0, s30, 16384\n"
"v_exp_f32 v192, v192\n"
"v_mfma_f32_16x16x32_bf16 v[32:35], a[204:207], v[212:215], v[32:35]\n"
"s_xor_b32 s25, s19, 40960\n"
"v_mfma_f32_16x16x32_bf16 v[36:39], a[208:211], v[212:215], v[36:39]\n"
"v_exp_f32 v193, v193\n"
"v_mfma_f32_16x16x32_bf16 v[40:43], a[212:215], v[212:215], v[40:43]\n"
"v_cvt_pk_bf16_f32 v208, v192, v193\n"
"v_mfma_f32_16x16x32_bf16 v[44:47], a[216:219], v[212:215], v[44:47]\n"
"v_exp_f32 v194, v194\n"
"v_mfma_f32_16x16x32_bf16 v[48:51], a[220:223], v[212:215], v[48:51]\n"
"buffer_load_dwordx4 v238, s[44:47], s25 offen offset:0 lds\n"
"v_mfma_f32_16x16x32_bf16 v[52:55], a[224:227], v[212:215], v[52:55]\n"
"v_exp_f32 v195, v195\n"
"v_mfma_f32_16x16x32_bf16 v[56:59], a[228:231], v[212:215], v[56:59]\n"
"v_cvt_pk_bf16_f32 v209, v194, v195\n"
"v_mfma_f32_16x16x32_bf16 v[60:63], a[232:235], v[212:215], v[60:63]\n"
"v_exp_f32 v196, v196\n"
"v_mfma_f32_16x16x32_bf16 a[240:243], a[252:255], v[212:215], a[240:243]\n"
"buffer_load_dwordx4 v239, s[44:47], s25 offen offset:1024 lds\n"
"v_mfma_f32_16x16x32_bf16 v[200:203], a[172:175], v[176:179], 0\n"
"s_add_u32 m0, s30, 24576\n"
"v_exp_f32 v197, v197\n"
"v_mfma_f32_16x16x32_bf16 v[200:203], a[176:179], v[180:183], v[200:203]\n"
"ds_read_b128 a[172:175], v224 offset:32768\n"
"v_cvt_pk_bf16_f32 v210, v196, v197\n"
"v_mfma_f32_16x16x32_bf16 v[200:203], a[180:183], v[184:187], v[200:203]\n"
"v_exp_f32 v198, v198\n"
"v_mfma_f32_16x16x32_bf16 v[200:203], a[184:187], v[188:191], v[200:203]\n"
"ds_read_b128 a[176:179], v225 offset:32768\n"
"ds_read_b128 a[180:183], v226 offset:32768\n"
"v_mfma_f32_16x16x32_bf16 v[204:207], a[188:191], v[176:179], 0\n"
"buffer_load_dwordx4 v240, s[48:51], s25 offen offset:0 lds\n"
"v_exp_f32 v199, v199\n"
"v_mfma_f32_16x16x32_bf16 v[204:207], a[192:195], v[180:183], v[204:207]\n"
"ds_read_b128 a[184:187], v227 offset:32768\n"
"v_cvt_pk_bf16_f32 v211, v198, v199\n"
"v_mfma_f32_16x16x32_bf16 v[204:207], a[196:199], v[184:187], v[204:207]\n"
"ds_read_b128 a[188:191], v224 offset:36864\n"
"ds_read_b128 a[192:195], v225 offset:36864\n"
"v_mfma_f32_16x16x32_bf16 v[204:207], a[200:203], v[188:191], v[204:207]\n"
"v_exp_f32 v200, v200\n"
"v_mfma_f32_16x16x32_bf16 v[64:67], a[204:207], v[208:211], v[64:67]\n"
"ds_read_b128 a[196:199], v226 offset:36864\n"
"ds_read_b128 a[200:203], v227 offset:36864\n"
"buffer_load_dwordx4 v241, s[48:51], s25 offen offset:1024 lds\n"
"v_mfma_f32_16x16x32_bf16 v[68:71], a[208:211], v[208:211], v[68:71]\n"
"s_add_u32 m0, s30, 147456\n"
"v_exp_f32 v201, v201\n"
"v_mfma_f32_16x16x32_bf16 v[72:75], a[212:215], v[208:211], v[72:75]\n"
"v_cvt_pk_bf16_f32 v212, v200, v201\n"
"v_mfma_f32_16x16x32_bf16 v[76:79], a[216:219], v[208:211], v[76:79]\n"
"v_exp_f32 v202, v202\n"
"v_mfma_f32_16x16x32_bf16 v[80:83], a[220:223], v[208:211], v[80:83]\n"
"buffer_load_dwordx4 v249, s[64:67], s2 offen offset:1024 lds\n"
"v_mfma_f32_16x16x32_bf16 v[84:87], a[224:227], v[208:211], v[84:87]\n"
"L_ic3:\n"
"s_cbranch_execz L_ic7\n"
"s_add_u32 m0, s90, 81920\n"
"v_exp_f32 v203, v203\n"
"v_mfma_f32_16x16x32_bf16 v[88:91], a[228:231], v[208:211], v[88:91]\n"
"s_xor_b32 s24, s89, 32768\n"
"v_cvt_pk_bf16_f32 v213, v202, v203\n"
"v_mfma_f32_16x16x32_bf16 v[92:95], a[232:235], v[208:211], v[92:95]\n"
"v_exp_f32 v204, v204\n"
"v_mfma_f32_16x16x32_bf16 a[244:247], a[252:255], v[208:211], a[244:247]\n"
"s_waitcnt lgkmcnt(6)\n"
"v_mfma_f32_16x16x32_bf16 v[192:195], a[172:175], v[128:131], 0\n"
"v_exp_f32 v205, v205\n"
"v_mfma_f32_16x16x32_bf16 v[192:195], a[176:179], v[132:135], v[192:195]\n"
"s_waitcnt lgkmcnt(4)\n"
"v_cvt_pk_bf16_f32 v214, v204, v205\n"
"v_mfma_f32_16x16x32_bf16 v[192:195], a[180:183], v[136:139], v[192:195]\n"
"v_exp_f32 v206, v206\n"
"v_mfma_f32_16x16x32_bf16 v[192:195], a[184:187], v[140:143], v[192:195]\n"
"s_waitcnt lgkmcnt(2)\n"
"v_mfma_f32_16x16x32_bf16 v[196:199], a[188:191], v[128:131], 0\n"
"v_exp_f32 v207, v207\n"
"v_mfma_f32_16x16x32_bf16 v[196:199], a[192:195], v[132:135], v[196:199]\n"
"s_waitcnt lgkmcnt(0)\n"
"v_cvt_pk_bf16_f32 v215, v206, v207\n"
"v_mfma_f32_16x16x32_bf16 v[196:199], a[196:199], v[136:139], v[196:199]\n"
"buffer_load_dwordx4 v230, s[52:55], s24 offen offset:0 lds\n"
"v_mfma_f32_16x16x32_bf16 v[196:199], a[200:203], v[140:143], v[196:199]\n"
"v_exp_f32 v192, v192\n"
"v_mfma_f32_16x16x32_bf16 v[96:99], a[204:207], v[212:215], v[96:99]\n"
"ds_read_b64_tr_b16 a[204:205], v216 offset:40960\n"
"ds_read_b64_tr_b16 a[206:207], v216 offset:45056\n"
"v_mfma_f32_16x16x32_bf16 v[100:103], a[208:211], v[212:215], v[100:103]\n"
"v_exp_f32 v193, v193\n"
"v_mfma_f32_16x16x32_bf16 v[104:107], a[212:215], v[212:215], v[104:107]\n"
"ds_read_b64_tr_b16 a[208:209], v217 offset:40960\n"
"v_cvt_pk_bf16_f32 v208, v192, v193\n"
"v_mfma_f32_16x16x32_bf16 v[108:111], a[216:219], v[212:215], v[108:111]\n"
"v_exp_f32 v194, v194\n"
"v_mfma_f32_16x16x32_bf16 v[112:115], a[220:223], v[212:215], v[112:115]\n"
"ds_read_b64_tr_b16 a[210:211], v217 offset:45056\n"
"ds_read_b64_tr_b16 a[212:213], v218 offset:40960\n"
"v_mfma_f32_16x16x32_bf16 v[116:119], a[224:227], v[212:215], v[116:119]\n"
"v_exp_f32 v195, v195\n"
"v_mfma_f32_16x16x32_bf16 v[120:123], a[228:231], v[212:215], v[120:123]\n"
"ds_read_b64_tr_b16 a[214:215], v218 offset:45056\n"
"v_cvt_pk_bf16_f32 v209, v194, v195\n"
"v_mfma_f32_16x16x32_bf16 v[124:127], a[232:235], v[212:215], v[124:127]\n"
"v_exp_f32 v196, v196\n"
"v_mfma_f32_16x16x32_bf16 a[248:251], a[252:255], v[212:215], a[248:251]\n"
"ds_read_b64_tr_b16 a[216:217], v219 offset:40960\n"
"ds_read_b64_tr_b16 a[218:219], v219 offset:45056\n"
"v_mfma_f32_16x16x32_bf16 v[200:203], a[172:175], v[144:147], 0\n"
"v_exp_f32 v197, v197\n"
"v_mfma_f32_16x16x32_bf16 v[200:203], a[176:179], v[148:151], v[200:203]\n"
"ds_read_b64_tr_b16 a[220:221], v220 offset:40960\n"
"v_cvt_pk_bf16_f32 v210, v196, v197\n"
"v_mfma_f32_16x16x32_bf16 v[200:203], a[180:183], v[152:155], v[200:203]\n"
"v_exp_f32 v198, v198\n"
"v_mfma_f32_16x16x32_bf16 v[200:203], a[184:187], v[156:159], v[200:203]\n"
"ds_read_b64_tr_b16 a[222:223], v220 offset:45056\n"
"ds_read_b64_tr_b16 a[224:225], v221 offset:40960\n"
"v_mfma_f32_16x16x32_bf16 v[204:207], a[188:191], v[144:147], 0\n"
"v_exp_f32 v199, v199\n"
"v_mfma_f32_16x16x32_bf16 v[204:207], a[192:195], v[148:151], v[204:207]\n"
"ds_read_b64_tr_b16 a[226:227], v221 offset:45056\n"
"v_cvt_pk_bf16_f32 v211, v198, v199\n"
"v_mfma_f32_16x16x32_bf16 v[204:207], a[196:199], v[152:155], v[204:207]\n"
"ds_read_b64_tr_b16 a[228:229], v222 offset:40960\n"
"ds_read_b64_tr_b16 a[230:231], v222 offset:45056\n"
"v_mfma_f32_16x16x32_bf16 v[204:207], a[200:203], v[156:159], v[204:207]\n"
"s_waitcnt lgkmcnt(10)\n"
"v_exp_f32 v200, v200\n"
"v_mfma_f32_16x16x32_bf16 v[0:3], a[204:207], v[208:211], v[0:3]\n"
"ds_read_b64_tr_b16 a[232:233], v223 offset:40960\n"
"ds_read_b64_tr_b16 a[234:235], v223 offset:45056\n"
"v_mfma_f32_16x16x32_bf16 v[4:7], a[208:211], v[208:211], v[4:7]\n"
"s_waitcnt lgkmcnt(8)\n"
"v_exp_f32 v201, v201\n"
"v_mfma_f32_16x16x32_bf16 v[8:11], a[212:215], v[208:211], v[8:11]\n"
"v_cvt_pk_bf16_f32 v212, v200, v201\n"
"v_mfma_f32_16x16x32_bf16 v[12:15], a[216:219], v[208:211], v[12:15]\n"
"s_waitcnt lgkmcnt(4)\n"
"v_exp_f32 v202, v202\n"
"v_mfma_f32_16x16x32_bf16 v[16:19], a[220:223], v[208:211], v[16:19]\n"
"buffer_load_dwordx4 v231, s[52:55], s24 offen offset:1024 lds\n"
"v_mfma_f32_16x16x32_bf16 v[20:23], a[224:227], v[208:211], v[20:23]\n"
"s_waitcnt lgkmcnt(0)\n"
"v_exp_f32 v203, v203\n"
"v_mfma_f32_16x16x32_bf16 v[24:27], a[228:231], v[208:211], v[24:27]\n"
"v_cvt_pk_bf16_f32 v213, v202, v203\n"
"v_mfma_f32_16x16x32_bf16 v[28:31], a[232:235], v[208:211], v[28:31]\n"
"v_exp_f32 v204, v204\n"
"v_mfma_f32_16x16x32_bf16 a[236:239], a[252:255], v[208:211], a[236:239]\n"
"buffer_load_dwordx4 v232, s[52:55], s24 offen offset:2048 lds\n"
"v_mfma_f32_16x16x32_bf16 v[192:195], a[172:175], v[160:163], 0\n"
"v_exp_f32 v205, v205\n"
"v_mfma_f32_16x16x32_bf16 v[192:195], a[176:179], v[164:167], v[192:195]\n"
"v_cvt_pk_bf16_f32 v214, v204, v205\n"
"v_mfma_f32_16x16x32_bf16 v[192:195], a[180:183], v[168:171], v[192:195]\n"
"v_exp_f32 v206, v206\n"
"v_mfma_f32_16x16x32_bf16 v[192:195], a[184:187], v[172:175], v[192:195]\n"
"buffer_load_dwordx4 v233, s[52:55], s24 offen offset:3072 lds\n"
"v_mfma_f32_16x16x32_bf16 v[196:199], a[188:191], v[160:163], 0\n"
"v_exp_f32 v207, v207\n"
"v_mfma_f32_16x16x32_bf16 v[196:199], a[192:195], v[164:167], v[196:199]\n"
"s_waitcnt vmcnt(18)\n"
"v_cvt_pk_bf16_f32 v215, v206, v207\n"
"v_mfma_f32_16x16x32_bf16 v[196:199], a[196:199], v[168:171], v[196:199]\n"
"s_barrier\n"
"v_mfma_f32_16x16x32_bf16 v[196:199], a[200:203], v[172:175], v[196:199]\n"
"s_add_u32 m0, s30, 32768\n"
"v_exp_f32 v192, v192\n"
"v_mfma_f32_16x16x32_bf16 v[32:35], a[204:207], v[212:215], v[32:35]\n"
"s_xor_b32 s25, s19, 49152\n"
"s_waitcnt vmcnt(9)\n"
"ds_read_b64_tr_b16 a[0:1], v234 offset:0\n"
"v_mfma_f32_16x16x32_bf16 v[36:39], a[208:211], v[212:215], v[36:39]\n"
"v_exp_f32 v193, v193\n"
"v_mfma_f32_16x16x32_bf16 v[40:43], a[212:215], v[212:215], v[40:43]\n"
"ds_read_b64_tr_b16 a[2:3], v234 offset:2048\n"
"v_cvt_pk_bf16_f32 v208, v192, v193\n"
"v_mfma_f32_16x16x32_bf16 v[44:47], a[216:219], v[212:215], v[44:47]\n"
"v_exp_f32 v194, v194\n"
"v_mfma_f32_16x16x32_bf16 v[48:51], a[220:223], v[212:215], v[48:51]\n"
"buffer_load_dwordx4 v238, s[44:47], s25 offen offset:0 lds\n"
"v_mfma_f32_16x16x32_bf16 v[52:55], a[224:227], v[212:215], v[52:55]\n"
"v_exp_f32 v195, v195\n"
"v_mfma_f32_16x16x32_bf16 v[56:59], a[228:231], v[212:215], v[56:59]\n"
"ds_read_b64_tr_b16 a[4:5], v235 offset:0\n"
"v_cvt_pk_bf16_f32 v209, v194, v195\n"
"v_mfma_f32_16x16x32_bf16 v[60:63], a[232:235], v[212:215], v[60:63]\n"
"v_exp_f32 v196, v196\n"
"v_mfma_f32_16x16x32_bf16 a[240:243], a[252:255], v[212:215], a[240:243]\n"
"buffer_load_dwordx4 v239, s[44:47], s25 offen offset:1024 lds\n"
"v_mfma_f32_16x16x32_bf16 v[200:203], a[172:175], v[176:179], 0\n"
"s_add_u32 m0, s30, 40960\n"
"v_exp_f32 v197, v197\n"
"v_mfma_f32_16x16x32_bf16 v[200:203], a[176:179], v[180:183], v[200:203]\n"
"ds_read_b128 a[172:175], v224 offset:49152\n"
"v_cvt_pk_bf16_f32 v210, v196, v197\n"
"v_mfma_f32_16x16x32_bf16 v[200:203], a[180:183], v[184:187], v[200:203]\n"
"v_exp_f32 v198, v198\n"
"v_mfma_f32_16x16x32_bf16 v[200:203], a[184:187], v[188:191], v[200:203]\n"
"ds_read_b128 a[176:179], v225 offset:49152\n"
"ds_read_b128 a[180:183], v226 offset:49152\n"
"v_mfma_f32_16x16x32_bf16 v[204:207], a[188:191], v[176:179], 0\n"
"buffer_load_dwordx4 v240, s[48:51], s25 offen offset:0 lds\n"
"v_exp_f32 v199, v199\n"
"v_mfma_f32_16x16x32_bf16 v[204:207], a[192:195], v[180:183], v[204:207]\n"
"ds_read_b128 a[184:187], v227 offset:49152\n"
"v_cvt_pk_bf16_f32 v211, v198, v199\n"
"v_mfma_f32_16x16x32_bf16 v[204:207], a[196:199], v[184:187], v[204:207]\n"
"ds_read_b128 a[188:191], v224 offset:53248\n"
"ds_read_b128 a[192:195], v225 offset:53248\n"
"v_mfma_f32_16x16x32_bf16 v[204:207], a[200:203], v[188:191], v[204:207]\n"
"v_exp_f32 v200, v200\n"
"v_mfma_f32_16x16x32_bf16 v[64:67], a[204:207], v[208:211], v[64:67]\n"
"ds_read_b128 a[196:199], v226 offset:53248\n"
"ds_read_b128 a[200:203], v227 offset:53248\n"
"buffer_load_dwordx4 v241, s[48:51], s25 offen offset:1024 lds\n"
"v_mfma_f32_16x16x32_bf16 v[68:71], a[208:211], v[208:211], v[68:71]\n"
"s_add_u32 m0, s30, 155648\n"
"v_exp_f32 v201, v201\n"
"v_mfma_f32_16x16x32_bf16 v[72:75], a[212:215], v[208:211], v[72:75]\n"
"ds_read_b64_tr_b16 a[6:7], v235 offset:2048\n"
"v_cvt_pk_bf16_f32 v212, v200, v201\n"
"v_mfma_f32_16x16x32_bf16 v[76:79], a[216:219], v[208:211], v[76:79]\n"
"v_exp_f32 v202, v202\n"
"v_mfma_f32_16x16x32_bf16 v[80:83], a[220:223], v[208:211], v[80:83]\n"
"buffer_load_dwordx4 v248, s[68:71], s2 offen offset:0 lds\n"
"v_mfma_f32_16x16x32_bf16 v[84:87], a[224:227], v[208:211], v[84:87]\n"
"s_add_u32 m0, s90, 98304\n"
"v_exp_f32 v203, v203\n"
"v_mfma_f32_16x16x32_bf16 v[88:91], a[228:231], v[208:211], v[88:91]\n"
"ds_read_b64_tr_b16 a[8:9], v236 offset:0\n"
"v_cvt_pk_bf16_f32 v213, v202, v203\n"
"v_mfma_f32_16x16x32_bf16 v[92:95], a[232:235], v[208:211], v[92:95]\n"
"s_xor_b32 s24, s89, 65536\n"
"v_exp_f32 v204, v204\n"
"v_mfma_f32_16x16x32_bf16 a[244:247], a[252:255], v[208:211], a[244:247]\n"
"s_waitcnt lgkmcnt(8)\n"
"ds_read_b64_tr_b16 a[10:11], v236 offset:2048\n"
"v_mfma_f32_16x16x32_bf16 v[192:195], a[172:175], v[128:131], 0\n"
"buffer_load_dwordx4 v230, s[52:55], s24 offen offset:0 lds\n"
"v_exp_f32 v205, v205\n"
"v_mfma_f32_16x16x32_bf16 v[192:195], a[176:179], v[132:135], v[192:195]\n"
"s_waitcnt lgkmcnt(7)\n"
"v_cvt_pk_bf16_f32 v214, v204, v205\n"
"v_mfma_f32_16x16x32_bf16 v[192:195], a[180:183], v[136:139], v[192:195]\n"
"v_exp_f32 v206, v206\n"
"v_mfma_f32_16x16x32_bf16 v[192:195], a[184:187], v[140:143], v[192:195]\n"
"s_waitcnt lgkmcnt(5)\n"
"ds_read_b64_tr_b16 a[12:13], v237 offset:0\n"
"v_mfma_f32_16x16x32_bf16 v[196:199], a[188:191], v[128:131], 0\n"
"buffer_load_dwordx4 v231, s[52:55], s24 offen offset:1024 lds\n"
"v_exp_f32 v207, v207\n"
"v_mfma_f32_16x16x32_bf16 v[196:199], a[192:195], v[132:135], v[196:199]\n"
"s_waitcnt lgkmcnt(4)\n"
"v_cvt_pk_bf16_f32 v215, v206, v207\n"
"v_mfma_f32_16x16x32_bf16 v[196:199], a[196:199], v[136:139], v[196:199]\n"
"ds_read_b64_tr_b16 a[14:15], v237 offset:2048\n"
"v_mfma_f32_16x16x32_bf16 v[196:199], a[200:203], v[140:143], v[196:199]\n"
"v_exp_f32 v192, v192\n"
"v_mfma_f32_16x16x32_bf16 v[96:99], a[204:207], v[212:215], v[96:99]\n"
"ds_read_b64_tr_b16 a[204:205], v216 offset:57344\n"
"ds_read_b64_tr_b16 a[206:207], v216 offset:61440\n"
"v_mfma_f32_16x16x32_bf16 v[100:103], a[208:211], v[212:215], v[100:103]\n"
"v_exp_f32 v193, v193\n"
"v_mfma_f32_16x16x32_bf16 v[104:107], a[212:215], v[212:215], v[104:107]\n"
"ds_read_b64_tr_b16 a[208:209], v217 offset:57344\n"
"v_cvt_pk_bf16_f32 v208, v192, v193\n"
"v_mfma_f32_16x16x32_bf16 v[108:111], a[216:219], v[212:215], v[108:111]\n"
"v_exp_f32 v194, v194\n"
"v_mfma_f32_16x16x32_bf16 v[112:115], a[220:223], v[212:215], v[112:115]\n"
"ds_read_b64_tr_b16 a[210:211], v217 offset:61440\n"
"ds_read_b64_tr_b16 a[212:213], v218 offset:57344\n"
"v_mfma_f32_16x16x32_bf16 v[116:119], a[224:227], v[212:215], v[116:119]\n"
"v_exp_f32 v195, v195\n"
"v_mfma_f32_16x16x32_bf16 v[120:123], a[228:231], v[212:215], v[120:123]\n"
"ds_read_b64_tr_b16 a[214:215], v218 offset:61440\n"
"v_cvt_pk_bf16_f32 v209, v194, v195\n"
"v_mfma_f32_16x16x32_bf16 v[124:127], a[232:235], v[212:215], v[124:127]\n"
"v_exp_f32 v196, v196\n"
"v_mfma_f32_16x16x32_bf16 a[248:251], a[252:255], v[212:215], a[248:251]\n"
"ds_read_b64_tr_b16 a[216:217], v219 offset:57344\n"
"ds_read_b64_tr_b16 a[218:219], v219 offset:61440\n"
"v_mfma_f32_16x16x32_bf16 v[200:203], a[172:175], v[144:147], 0\n"
"v_exp_f32 v197, v197\n"
"v_mfma_f32_16x16x32_bf16 v[200:203], a[176:179], v[148:151], v[200:203]\n"
"ds_read_b64_tr_b16 a[220:221], v220 offset:57344\n"
"v_cvt_pk_bf16_f32 v210, v196, v197\n"
"v_mfma_f32_16x16x32_bf16 v[200:203], a[180:183], v[152:155], v[200:203]\n"
"v_exp_f32 v198, v198\n"
"v_mfma_f32_16x16x32_bf16 v[200:203], a[184:187], v[156:159], v[200:203]\n"
"ds_read_b64_tr_b16 a[222:223], v220 offset:61440\n"
"ds_read_b64_tr_b16 a[224:225], v221 offset:57344\n"
"v_mfma_f32_16x16x32_bf16 v[204:207], a[188:191], v[144:147], 0\n"
"v_exp_f32 v199, v199\n"
"v_mfma_f32_16x16x32_bf16 v[204:207], a[192:195], v[148:151], v[204:207]\n"
"ds_read_b64_tr_b16 a[226:227], v221 offset:61440\n"
"v_cvt_pk_bf16_f32 v211, v198, v199\n"
"v_mfma_f32_16x16x32_bf16 v[204:207], a[196:199], v[152:155], v[204:207]\n"
"ds_read_b64_tr_b16 a[228:229], v222 offset:57344\n"
"ds_read_b64_tr_b16 a[230:231], v222 offset:61440\n"
"v_mfma_f32_16x16x32_bf16 v[204:207], a[200:203], v[156:159], v[204:207]\n"
"s_waitcnt lgkmcnt(10)\n"
"v_exp_f32 v200, v200\n"
"v_mfma_f32_16x16x32_bf16 v[0:3], a[204:207], v[208:211], v[0:3]\n"
"ds_read_b64_tr_b16 a[232:233], v223 offset:57344\n"
"ds_read_b64_tr_b16 a[234:235], v223 offset:61440\n"
"v_mfma_f32_16x16x32_bf16 v[4:7], a[208:211], v[208:211], v[4:7]\n"
"s_waitcnt lgkmcnt(8)\n"
"v_exp_f32 v201, v201\n"
"v_mfma_f32_16x16x32_bf16 v[8:11], a[212:215], v[208:211], v[8:11]\n"
"v_cvt_pk_bf16_f32 v212, v200, v201\n"
"v_mfma_f32_16x16x32_bf16 v[12:15], a[216:219], v[208:211], v[12:15]\n"
"s_waitcnt lgkmcnt(4)\n"
"v_exp_f32 v202, v202\n"
"v_mfma_f32_16x16x32_bf16 v[16:19], a[220:223], v[208:211], v[16:19]\n"
"buffer_load_dwordx4 v232, s[52:55], s24 offen offset:2048 lds\n"
"v_mfma_f32_16x16x32_bf16 v[20:23], a[224:227], v[208:211], v[20:23]\n"
"s_waitcnt lgkmcnt(0)\n"
"v_exp_f32 v203, v203\n"
"v_mfma_f32_16x16x32_bf16 v[24:27], a[228:231], v[208:211], v[24:27]\n"
"v_cvt_pk_bf16_f32 v213, v202, v203\n"
"v_mfma_f32_16x16x32_bf16 v[28:31], a[232:235], v[208:211], v[28:31]\n"
"v_exp_f32 v204, v204\n"
"v_mfma_f32_16x16x32_bf16 a[236:239], a[252:255], v[208:211], a[236:239]\n"
"buffer_load_dwordx4 v233, s[52:55], s24 offen offset:3072 lds\n"
"v_mfma_f32_16x16x32_bf16 v[192:195], a[172:175], v[160:163], 0\n"
"v_exp_f32 v205, v205\n"
"v_mfma_f32_16x16x32_bf16 v[192:195], a[176:179], v[164:167], v[192:195]\n"
"v_cvt_pk_bf16_f32 v214, v204, v205\n"
"v_mfma_f32_16x16x32_bf16 v[192:195], a[180:183], v[168:171], v[192:195]\n"
"v_exp_f32 v206, v206\n"
"v_mfma_f32_16x16x32_bf16 v[192:195], a[184:187], v[172:175], v[192:195]\n"
"v_mfma_f32_16x16x32_bf16 v[196:199], a[188:191], v[160:163], 0\n"
"v_exp_f32 v207, v207\n"
"v_mfma_f32_16x16x32_bf16 v[196:199], a[192:195], v[164:167], v[196:199]\n"
"v_cvt_pk_bf16_f32 v215, v206, v207\n"
"v_mfma_f32_16x16x32_bf16 v[196:199], a[196:199], v[168:171], v[196:199]\n"
"s_barrier\n"
"v_mfma_f32_16x16x32_bf16 v[196:199], a[200:203], v[172:175], v[196:199]\n"
"s_add_u32 m0, s30, 49152\n"
"v_exp_f32 v192, v192\n"
"v_mfma_f32_16x16x32_bf16 v[32:35], a[204:207], v[212:215], v[32:35]\n"
"s_xor_b32 s25, s19, 57344\n"
"s_waitcnt vmcnt(9)\n"
"ds_read_b64_tr_b16 a[16:17], v234 offset:16384\n"
"v_mfma_f32_16x16x32_bf16 v[36:39], a[208:211], v[212:215], v[36:39]\n"
"ds_read_b64_tr_b16 a[18:19], v234 offset:18432\n"
"v_exp_f32 v193, v193\n"
"v_mfma_f32_16x16x32_bf16 v[40:43], a[212:215], v[212:215], v[40:43]\n"
"ds_read_b64_tr_b16 a[20:21], v235 offset:16384\n"
"v_cvt_pk_bf16_f32 v208, v192, v193\n"
"v_mfma_f32_16x16x32_bf16 v[44:47], a[216:219], v[212:215], v[44:47]\n"
"ds_read_b64_tr_b16 a[22:23], v235 offset:18432\n"
"v_exp_f32 v194, v194\n"
"v_mfma_f32_16x16x32_bf16 v[48:51], a[220:223], v[212:215], v[48:51]\n"
"buffer_load_dwordx4 v238, s[44:47], s25 offen offset:0 lds\n"
"v_mfma_f32_16x16x32_bf16 v[52:55], a[224:227], v[212:215], v[52:55]\n"
"v_exp_f32 v195, v195\n"
"v_mfma_f32_16x16x32_bf16 v[56:59], a[228:231], v[212:215], v[56:59]\n"
"ds_read_b64_tr_b16 a[24:25], v236 offset:16384\n"
"v_cvt_pk_bf16_f32 v209, v194, v195\n"
"v_mfma_f32_16x16x32_bf16 v[60:63], a[232:235], v[212:215], v[60:63]\n"
"ds_read_b64_tr_b16 a[26:27], v236 offset:18432\n"
"v_exp_f32 v196, v196\n"
"v_mfma_f32_16x16x32_bf16 a[240:243], a[252:255], v[212:215], a[240:243]\n"
"buffer_load_dwordx4 v239, s[44:47], s25 offen offset:1024 lds\n"
"v_mfma_f32_16x16x32_bf16 v[200:203], a[172:175], v[176:179], 0\n"
"s_add_u32 m0, s30, 57344\n"
"v_exp_f32 v197, v197\n"
"v_mfma_f32_16x16x32_bf16 v[200:203], a[176:179], v[180:183], v[200:203]\n"
"ds_read_b128 a[172:175], v224 offset:0\n"
"v_cvt_pk_bf16_f32 v210, v196, v197\n"
"v_mfma_f32_16x16x32_bf16 v[200:203], a[180:183], v[184:187], v[200:203]\n"
"v_exp_f32 v198, v198\n"
"v_mfma_f32_16x16x32_bf16 v[200:203], a[184:187], v[188:191], v[200:203]\n"
"ds_read_b128 a[176:179], v225 offset:0\n"
"ds_read_b128 a[180:183], v226 offset:0\n"
"v_mfma_f32_16x16x32_bf16 v[204:207], a[188:191], v[176:179], 0\n"
"buffer_load_dwordx4 v240, s[48:51], s25 offen offset:0 lds\n"
"v_exp_f32 v199, v199\n"
"v_mfma_f32_16x16x32_bf16 v[204:207], a[192:195], v[180:183], v[204:207]\n"
"ds_read_b128 a[184:187], v227 offset:0\n"
"v_cvt_pk_bf16_f32 v211, v198, v199\n"
"v_mfma_f32_16x16x32_bf16 v[204:207], a[196:199], v[184:187], v[204:207]\n"
"ds_read_b128 a[188:191], v224 offset:4096\n"
"ds_read_b128 a[192:195], v225 offset:4096\n"
"v_mfma_f32_16x16x32_bf16 v[204:207], a[200:203], v[188:191], v[204:207]\n"
"v_exp_f32 v200, v200\n"
"v_mfma_f32_16x16x32_bf16 v[64:67], a[204:207], v[208:211], v[64:67]\n"
"ds_read_b128 a[196:199], v226 offset:4096\n"
"ds_read_b128 a[200:203], v227 offset:4096\n"
"buffer_load_dwordx4 v241, s[48:51], s25 offen offset:1024 lds\n"
"v_mfma_f32_16x16x32_bf16 v[68:71], a[208:211], v[208:211], v[68:71]\n"
"s_add_u32 m0, s30, 155648\n"
"v_exp_f32 v201, v201\n"
"v_mfma_f32_16x16x32_bf16 v[72:75], a[212:215], v[208:211], v[72:75]\n"
"ds_read_b64_tr_b16 a[28:29], v237 offset:16384\n"
"v_cvt_pk_bf16_f32 v212, v200, v201\n"
"v_mfma_f32_16x16x32_bf16 v[76:79], a[216:219], v[208:211], v[76:79]\n"
"ds_read_b64_tr_b16 a[30:31], v237 offset:18432\n"
"v_exp_f32 v202, v202\n"
"v_mfma_f32_16x16x32_bf16 v[80:83], a[220:223], v[208:211], v[80:83]\n"
"buffer_load_dwordx4 v249, s[68:71], s2 offen offset:1024 lds\n"
"v_cmp_gt_u32 vcc, 32, v242\n"
"v_mfma_f32_16x16x32_bf16 v[84:87], a[224:227], v[208:211], v[84:87]\n"
"v_exp_f32 v203, v203\n"
"v_mfma_f32_16x16x32_bf16 v[88:91], a[228:231], v[208:211], v[88:91]\n"
"v_cvt_pk_bf16_f32 v213, v202, v203\n"
"v_mov_b32 v254, s83\n"
"v_mov_b32 v252, s82\n"
"v_mfma_f32_16x16x32_bf16 v[92:95], a[232:235], v[208:211], v[92:95]\n"
"v_exp_f32 v204, v204\n"
"v_mfma_f32_16x16x32_bf16 a[244:247], a[252:255], v[208:211], a[244:247]\n"
"s_waitcnt lgkmcnt(8)\n"
"v_cndmask_b32 v254, v254, v252, vcc\n"
"v_and_b32 v252, 31, v242\n"
"v_mfma_f32_16x16x32_bf16 v[192:195], a[172:175], v[128:131], 0\n"
"v_exp_f32 v205, v205\n"
"v_mfma_f32_16x16x32_bf16 v[192:195], a[176:179], v[132:135], v[192:195]\n"
"s_waitcnt lgkmcnt(6)\n"
"v_cvt_pk_bf16_f32 v214, v204, v205\n"
"v_lshlrev_b32 v252, 3, v252\n"
"v_lshl_add_u32 v252, v254, 10, v252\n"
"v_mfma_f32_16x16x32_bf16 v[192:195], a[180:183], v[136:139], v[192:195]\n"
"v_exp_f32 v206, v206\n"
"v_mfma_f32_16x16x32_bf16 v[192:195], a[184:187], v[140:143], v[192:195]\n"
"s_waitcnt lgkmcnt(4)\n"
"v_lshlrev_b32 v254, 3, v254\n"
"v_add_u32 v254, 16384, v254\n"
"v_mfma_f32_16x16x32_bf16 v[196:199], a[188:191], v[128:131], 0\n"
"v_exp_f32 v207, v207\n"
"v_mfma_f32_16x16x32_bf16 v[196:199], a[192:195], v[132:135], v[196:199]\n"
"s_waitcnt lgkmcnt(2)\n"
"v_cvt_pk_bf16_f32 v215, v206, v207\n"
"v_mfma_f32_16x16x32_bf16 v[196:199], a[196:199], v[136:139], v[196:199]\n"
"buffer_load_dwordx2 v[250:251], v252, s[36:39], s4 offen\n"
"v_mfma_f32_16x16x32_bf16 v[196:199], a[200:203], v[140:143], v[196:199]\n"
"v_exp_f32 v192, v192\n"
"v_mfma_f32_16x16x32_bf16 v[96:99], a[204:207], v[212:215], v[96:99]\n"
"ds_read_b64_tr_b16 a[204:205], v216 offset:8192\n"
"ds_read_b64_tr_b16 a[206:207], v216 offset:12288\n"
"v_mfma_f32_16x16x32_bf16 v[100:103], a[208:211], v[212:215], v[100:103]\n"
"v_exp_f32 v193, v193\n"
"v_mfma_f32_16x16x32_bf16 v[104:107], a[212:215], v[212:215], v[104:107]\n"
"ds_read_b64_tr_b16 a[208:209], v217 offset:8192\n"
"buffer_load_dwordx2 v[252:253], v254, s[56:59], 0 offen sc1\n"
"v_cvt_pk_bf16_f32 v208, v192, v193\n"
"v_mfma_f32_16x16x32_bf16 v[108:111], a[216:219], v[212:215], v[108:111]\n"
"s_add_u32 m0, s90, 114688\n"
"v_exp_f32 v194, v194\n"
"v_mfma_f32_16x16x32_bf16 v[112:115], a[220:223], v[212:215], v[112:115]\n"
"ds_read_b64_tr_b16 a[210:211], v217 offset:12288\n"
"ds_read_b64_tr_b16 a[212:213], v218 offset:8192\n"
"v_mfma_f32_16x16x32_bf16 v[116:119], a[224:227], v[212:215], v[116:119]\n"
"s_xor_b32 s24, s89, 98304\n"
"v_exp_f32 v195, v195\n"
"v_mfma_f32_16x16x32_bf16 v[120:123], a[228:231], v[212:215], v[120:123]\n"
"ds_read_b64_tr_b16 a[214:215], v218 offset:12288\n"
"v_cvt_pk_bf16_f32 v209, v194, v195\n"
"v_mfma_f32_16x16x32_bf16 v[124:127], a[232:235], v[212:215], v[124:127]\n"
"buffer_load_dwordx4 v230, s[52:55], s24 offen offset:0 lds\n"
"v_exp_f32 v196, v196\n"
"v_mfma_f32_16x16x32_bf16 a[248:251], a[252:255], v[212:215], a[248:251]\n"
"ds_read_b64_tr_b16 a[216:217], v219 offset:8192\n"
"ds_read_b64_tr_b16 a[218:219], v219 offset:12288\n"
"v_mfma_f32_16x16x32_bf16 v[200:203], a[172:175], v[144:147], 0\n"
"v_exp_f32 v197, v197\n"
"v_mfma_f32_16x16x32_bf16 v[200:203], a[176:179], v[148:151], v[200:203]\n"
"ds_read_b64_tr_b16 a[220:221], v220 offset:8192\n"
"v_cvt_pk_bf16_f32 v210, v196, v197\n"
"v_mfma_f32_16x16x32_bf16 v[200:203], a[180:183], v[152:155], v[200:203]\n"
"buffer_load_dwordx4 v231, s[52:55], s24 offen offset:1024 lds\n"
"v_exp_f32 v198, v198\n"
"v_mfma_f32_16x16x32_bf16 v[200:203], a[184:187], v[156:159], v[200:203]\n"
"ds_read_b64_tr_b16 a[222:223], v220 offset:12288\n"
"ds_read_b64_tr_b16 a[224:225], v221 offset:8192\n"
"v_mfma_f32_16x16x32_bf16 v[204:207], a[188:191], v[144:147], 0\n"
"v_exp_f32 v199, v199\n"
"v_mfma_f32_16x16x32_bf16 v[204:207], a[192:195], v[148:151], v[204:207]\n"
"ds_read_b64_tr_b16 a[226:227], v221 offset:12288\n"
"v_cvt_pk_bf16_f32 v211, v198, v199\n"
"v_mfma_f32_16x16x32_bf16 v[204:207], a[196:199], v[152:155], v[204:207]\n"
"ds_read_b64_tr_b16 a[228:229], v222 offset:8192\n"
"ds_read_b64_tr_b16 a[230:231], v222 offset:12288\n"
"v_mfma_f32_16x16x32_bf16 v[204:207], a[200:203], v[156:159], v[204:207]\n"
"s_waitcnt lgkmcnt(10)\n"
"v_exp_f32 v200, v200\n"
"v_mfma_f32_16x16x32_bf16 v[0:3], a[204:207], v[208:211], v[0:3]\n"
"ds_read_b64_tr_b16 a[232:233], v223 offset:8192\n"
"ds_read_b64_tr_b16 a[234:235], v223 offset:12288\n"
"v_mfma_f32_16x16x32_bf16 v[4:7], a[208:211], v[208:211], v[4:7]\n"
"s_waitcnt lgkmcnt(8)\n"
"v_exp_f32 v201, v201\n"
"v_mfma_f32_16x16x32_bf16 v[8:11], a[212:215], v[208:211], v[8:11]\n"
"v_cvt_pk_bf16_f32 v212, v200, v201\n"
"v_mfma_f32_16x16x32_bf16 v[12:15], a[216:219], v[208:211], v[12:15]\n"
"s_waitcnt lgkmcnt(4)\n"
"v_exp_f32 v202, v202\n"
"v_mfma_f32_16x16x32_bf16 v[16:19], a[220:223], v[208:211], v[16:19]\n"
"buffer_load_dwordx4 v232, s[52:55], s24 offen offset:2048 lds\n"
"v_mfma_f32_16x16x32_bf16 v[20:23], a[224:227], v[208:211], v[20:23]\n"
"s_waitcnt lgkmcnt(0)\n"
"v_exp_f32 v203, v203\n"
"v_mfma_f32_16x16x32_bf16 v[24:27], a[228:231], v[208:211], v[24:27]\n"
"v_cvt_pk_bf16_f32 v213, v202, v203\n"
"v_mfma_f32_16x16x32_bf16 v[28:31], a[232:235], v[208:211], v[28:31]\n"
"v_exp_f32 v204, v204\n"
"v_mfma_f32_16x16x32_bf16 a[236:239], a[252:255], v[208:211], a[236:239]\n"
"buffer_load_dwordx4 v233, s[52:55], s24 offen offset:3072 lds\n"
"v_mfma_f32_16x16x32_bf16 v[192:195], a[172:175], v[160:163], 0\n"
"v_exp_f32 v205, v205\n"
"v_mfma_f32_16x16x32_bf16 v[192:195], a[176:179], v[164:167], v[192:195]\n"
"v_cvt_pk_bf16_f32 v214, v204, v205\n"
"v_mfma_f32_16x16x32_bf16 v[192:195], a[180:183], v[168:171], v[192:195]\n"
"v_exp_f32 v206, v206\n"
"v_mfma_f32_16x16x32_bf16 v[192:195], a[184:187], v[172:175], v[192:195]\n"
"v_mfma_f32_16x16x32_bf16 v[196:199], a[188:191], v[160:163], 0\n"
"v_exp_f32 v207, v207\n"
"v_mfma_f32_16x16x32_bf16 v[196:199], a[192:195], v[164:167], v[196:199]\n"
"v_cvt_pk_bf16_f32 v215, v206, v207\n"
"v_mfma_f32_16x16x32_bf16 v[196:199], a[196:199], v[168:171], v[196:199]\n"
"s_barrier\n"
"v_mfma_f32_16x16x32_bf16 v[196:199], a[200:203], v[172:175], v[196:199]\n"
"s_add_u32 m0, s30, 0\n"
"v_exp_f32 v192, v192\n"
"v_mfma_f32_16x16x32_bf16 v[32:35], a[204:207], v[212:215], v[32:35]\n"
"s_xor_b32 s25, s19, 65536\n"
"s_waitcnt vmcnt(11)\n"
"ds_read_b64_tr_b16 a[32:33], v234 offset:32768\n"
"v_mfma_f32_16x16x32_bf16 v[36:39], a[208:211], v[212:215], v[36:39]\n"
"v_exp_f32 v193, v193\n"
"v_mfma_f32_16x16x32_bf16 v[40:43], a[212:215], v[212:215], v[40:43]\n"
"ds_read_b64_tr_b16 a[34:35], v234 offset:34816\n"
"v_cvt_pk_bf16_f32 v208, v192, v193\n"
"v_mfma_f32_16x16x32_bf16 v[44:47], a[216:219], v[212:215], v[44:47]\n"
"v_exp_f32 v194, v194\n"
"v_mfma_f32_16x16x32_bf16 v[48:51], a[220:223], v[212:215], v[48:51]\n"
"buffer_load_dwordx4 v238, s[44:47], s25 offen offset:0 lds\n"
"v_mfma_f32_16x16x32_bf16 v[52:55], a[224:227], v[212:215], v[52:55]\n"
"v_exp_f32 v195, v195\n"
"v_mfma_f32_16x16x32_bf16 v[56:59], a[228:231], v[212:215], v[56:59]\n"
"ds_read_b64_tr_b16 a[36:37], v235 offset:32768\n"
"v_cvt_pk_bf16_f32 v209, v194, v195\n"
"v_mfma_f32_16x16x32_bf16 v[60:63], a[232:235], v[212:215], v[60:63]\n"
"v_exp_f32 v196, v196\n"
"v_mfma_f32_16x16x32_bf16 a[240:243], a[252:255], v[212:215], a[240:243]\n"
"buffer_load_dwordx4 v239, s[44:47], s25 offen offset:1024 lds\n"
"v_mfma_f32_16x16x32_bf16 v[200:203], a[172:175], v[176:179], 0\n"
"s_add_u32 m0, s30, 8192\n"
"v_exp_f32 v197, v197\n"
"v_mfma_f32_16x16x32_bf16 v[200:203], a[176:179], v[180:183], v[200:203]\n"
"ds_read_b128 a[172:175], v224 offset:16384\n"
"v_cvt_pk_bf16_f32 v210, v196, v197\n"
"v_mfma_f32_16x16x32_bf16 v[200:203], a[180:183], v[184:187], v[200:203]\n"
"v_exp_f32 v198, v198\n"
"v_mfma_f32_16x16x32_bf16 v[200:203], a[184:187], v[188:191], v[200:203]\n"
"ds_read_b128 a[176:179], v225 offset:16384\n"
"ds_read_b128 a[180:183], v226 offset:16384\n"
"v_mfma_f32_16x16x32_bf16 v[204:207], a[188:191], v[176:179], 0\n"
"buffer_load_dwordx4 v240, s[48:51], s25 offen offset:0 lds\n"
"v_exp_f32 v199, v199\n"
"v_mfma_f32_16x16x32_bf16 v[204:207], a[192:195], v[180:183], v[204:207]\n"
"ds_read_b128 a[184:187], v227 offset:16384\n"
"v_cvt_pk_bf16_f32 v211, v198, v199\n"
"v_mfma_f32_16x16x32_bf16 v[204:207], a[196:199], v[184:187], v[204:207]\n"
"ds_read_b128 a[188:191], v224 offset:20480\n"
"ds_read_b128 a[192:195], v225 offset:20480\n"
"v_mfma_f32_16x16x32_bf16 v[204:207], a[200:203], v[188:191], v[204:207]\n"
"v_exp_f32 v200, v200\n"
"v_mfma_f32_16x16x32_bf16 v[64:67], a[204:207], v[208:211], v[64:67]\n"
"ds_read_b128 a[196:199], v226 offset:20480\n"
"ds_read_b128 a[200:203], v227 offset:20480\n"
"buffer_load_dwordx4 v241, s[48:51], s25 offen offset:1024 lds\n"
"v_mfma_f32_16x16x32_bf16 v[68:71], a[208:211], v[208:211], v[68:71]\n"
"s_add_u32 m0, s90, 131072\n"
"v_exp_f32 v201, v201\n"
"v_mfma_f32_16x16x32_bf16 v[72:75], a[212:215], v[208:211], v[72:75]\n"
"ds_read_b64_tr_b16 a[38:39], v235 offset:34816\n"
"v_cvt_pk_bf16_f32 v212, v200, v201\n"
"v_mfma_f32_16x16x32_bf16 v[76:79], a[216:219], v[208:211], v[76:79]\n"
"s_xor_b32 s24, s89, 131072\n"
"v_exp_f32 v202, v202\n"
"v_mfma_f32_16x16x32_bf16 v[80:83], a[220:223], v[208:211], v[80:83]\n"
"ds_read_b64_tr_b16 a[40:41], v236 offset:32768\n"
"ds_read_b64_tr_b16 a[42:43], v236 offset:34816\n"
"v_mfma_f32_16x16x32_bf16 v[84:87], a[224:227], v[208:211], v[84:87]\n"
"v_exp_f32 v203, v203\n"
"v_mfma_f32_16x16x32_bf16 v[88:91], a[228:231], v[208:211], v[88:91]\n"
"ds_read_b64_tr_b16 a[44:45], v237 offset:32768\n"
"v_cvt_pk_bf16_f32 v213, v202, v203\n"
"v_mfma_f32_16x16x32_bf16 v[92:95], a[232:235], v[208:211], v[92:95]\n"
"v_exp_f32 v204, v204\n"
"v_mfma_f32_16x16x32_bf16 a[244:247], a[252:255], v[208:211], a[244:247]\n"
"s_waitcnt lgkmcnt(10)\n"
"ds_read_b64_tr_b16 a[46:47], v237 offset:34816\n"
"v_mfma_f32_16x16x32_bf16 v[192:195], a[172:175], v[128:131], 0\n"
"v_exp_f32 v205, v205\n"
"v_mfma_f32_16x16x32_bf16 v[192:195], a[176:179], v[132:135], v[192:195]\n"
"s_waitcnt lgkmcnt(9)\n"
"v_cvt_pk_bf16_f32 v214, v204, v205\n"
"v_mfma_f32_16x16x32_bf16 v[192:195], a[180:183], v[136:139], v[192:195]\n"
"v_exp_f32 v206, v206\n"
"v_mfma_f32_16x16x32_bf16 v[192:195], a[184:187], v[140:143], v[192:195]\n"
"s_waitcnt lgkmcnt(7)\n"
"v_mfma_f32_16x16x32_bf16 v[196:199], a[188:191], v[128:131], 0\n"
"v_exp_f32 v207, v207\n"
"v_mfma_f32_16x16x32_bf16 v[196:199], a[192:195], v[132:135], v[196:199]\n"
"s_waitcnt lgkmcnt(5)\n"
"v_cvt_pk_bf16_f32 v215, v206, v207\n"
"v_mfma_f32_16x16x32_bf16 v[196:199], a[196:199], v[136:139], v[196:199]\n"
"buffer_load_dwordx4 v230, s[52:55], s24 offen offset:0 lds\n"
"v_mfma_f32_16x16x32_bf16 v[196:199], a[200:203], v[140:143], v[196:199]\n"
"v_exp_f32 v192, v192\n"
"v_mfma_f32_16x16x32_bf16 v[96:99], a[204:207], v[212:215], v[96:99]\n"
"ds_read_b64_tr_b16 a[204:205], v216 offset:24576\n"
"ds_read_b64_tr_b16 a[206:207], v216 offset:28672\n"
"v_mfma_f32_16x16x32_bf16 v[100:103], a[208:211], v[212:215], v[100:103]\n"
"v_exp_f32 v193, v193\n"
"v_mfma_f32_16x16x32_bf16 v[104:107], a[212:215], v[212:215], v[104:107]\n"
"ds_read_b64_tr_b16 a[208:209], v217 offset:24576\n"
"v_cvt_pk_bf16_f32 v208, v192, v193\n"
"v_mfma_f32_16x16x32_bf16 v[108:111], a[216:219], v[212:215], v[108:111]\n"
"v_exp_f32 v194, v194\n"
"v_mfma_f32_16x16x32_bf16 v[112:115], a[220:223], v[212:215], v[112:115]\n"
"ds_read_b64_tr_b16 a[210:211], v217 offset:28672\n"
"ds_read_b64_tr_b16 a[212:213], v218 offset:24576\n"
"v_mfma_f32_16x16x32_bf16 v[116:119], a[224:227], v[212:215], v[116:119]\n"
"v_exp_f32 v195, v195\n"
"v_mfma_f32_16x16x32_bf16 v[120:123], a[228:231], v[212:215], v[120:123]\n"
"ds_read_b64_tr_b16 a[214:215], v218 offset:28672\n"
"v_cvt_pk_bf16_f32 v209, v194, v195\n"
"v_mfma_f32_16x16x32_bf16 v[124:127], a[232:235], v[212:215], v[124:127]\n"
"v_exp_f32 v196, v196\n"
"v_mfma_f32_16x16x32_bf16 a[248:251], a[252:255], v[212:215], a[248:251]\n"
"ds_read_b64_tr_b16 a[216:217], v219 offset:24576\n"
"ds_read_b64_tr_b16 a[218:219], v219 offset:28672\n"
"v_mfma_f32_16x16x32_bf16 v[200:203], a[172:175], v[144:147], 0\n"
"v_exp_f32 v197, v197\n"
"v_mfma_f32_16x16x32_bf16 v[200:203], a[176:179], v[148:151], v[200:203]\n"
"ds_read_b64_tr_b16 a[220:221], v220 offset:24576\n"
"v_cvt_pk_bf16_f32 v210, v196, v197\n"
"v_mfma_f32_16x16x32_bf16 v[200:203], a[180:183], v[152:155], v[200:203]\n"
"v_exp_f32 v198, v198\n"
"v_mfma_f32_16x16x32_bf16 v[200:203], a[184:187], v[156:159], v[200:203]\n"
"L_ic4:\n"
"s_cbranch_execz L_ic8\n"
"ds_read_b64_tr_b16 a[222:223], v220 offset:28672\n"
"ds_read_b64_tr_b16 a[224:225], v221 offset:24576\n"
"v_mfma_f32_16x16x32_bf16 v[204:207], a[188:191], v[144:147], 0\n"
"v_exp_f32 v199, v199\n"
"v_mfma_f32_16x16x32_bf16 v[204:207], a[192:195], v[148:151], v[204:207]\n"
"ds_read_b64_tr_b16 a[226:227], v221 offset:28672\n"
"v_cvt_pk_bf16_f32 v211, v198, v199\n"
"v_mfma_f32_16x16x32_bf16 v[204:207], a[196:199], v[152:155], v[204:207]\n"
"ds_read_b64_tr_b16 a[228:229], v222 offset:24576\n"
"ds_read_b64_tr_b16 a[230:231], v222 offset:28672\n"
"v_mfma_f32_16x16x32_bf16 v[204:207], a[200:203], v[156:159], v[204:207]\n"
"s_waitcnt lgkmcnt(10)\n"
"v_exp_f32 v200, v200\n"
"v_mfma_f32_16x16x32_bf16 v[0:3], a[204:207], v[208:211], v[0:3]\n"
"ds_read_b64_tr_b16 a[232:233], v223 offset:24576\n"
"ds_read_b64_tr_b16 a[234:235], v223 offset:28672\n"
"v_mfma_f32_16x16x32_bf16 v[4:7], a[208:211], v[208:211], v[4:7]\n"
"s_waitcnt lgkmcnt(8)\n"
"v_exp_f32 v201, v201\n"
"v_mfma_f32_16x16x32_bf16 v[8:11], a[212:215], v[208:211], v[8:11]\n"
"v_cvt_pk_bf16_f32 v212, v200, v201\n"
"v_mfma_f32_16x16x32_bf16 v[12:15], a[216:219], v[208:211], v[12:15]\n"
"s_waitcnt lgkmcnt(4)\n"
"v_exp_f32 v202, v202\n"
"v_mfma_f32_16x16x32_bf16 v[16:19], a[220:223], v[208:211], v[16:19]\n"
"buffer_load_dwordx4 v231, s[52:55], s24 offen offset:1024 lds\n"
"v_mfma_f32_16x16x32_bf16 v[20:23], a[224:227], v[208:211], v[20:23]\n"
"s_waitcnt lgkmcnt(0)\n"
"v_exp_f32 v203, v203\n"
"v_mfma_f32_16x16x32_bf16 v[24:27], a[228:231], v[208:211], v[24:27]\n"
"v_cvt_pk_bf16_f32 v213, v202, v203\n"
"v_mfma_f32_16x16x32_bf16 v[28:31], a[232:235], v[208:211], v[28:31]\n"
"v_exp_f32 v204, v204\n"
"v_mfma_f32_16x16x32_bf16 a[236:239], a[252:255], v[208:211], a[236:239]\n"
"buffer_load_dwordx4 v232, s[52:55], s24 offen offset:2048 lds\n"
"v_mfma_f32_16x16x32_bf16 v[192:195], a[172:175], v[160:163], 0\n"
"v_exp_f32 v205, v205\n"
"v_mfma_f32_16x16x32_bf16 v[192:195], a[176:179], v[164:167], v[192:195]\n"
"v_cvt_pk_bf16_f32 v214, v204, v205\n"
"v_mfma_f32_16x16x32_bf16 v[192:195], a[180:183], v[168:171], v[192:195]\n"
"v_exp_f32 v206, v206\n"
"v_mfma_f32_16x16x32_bf16 v[192:195], a[184:187], v[172:175], v[192:195]\n"
"buffer_load_dwordx4 v233, s[52:55], s24 offen offset:3072 lds\n"
"v_mfma_f32_16x16x32_bf16 v[196:199], a[188:191], v[160:163], 0\n"
"v_exp_f32 v207, v207\n"
"v_mfma_f32_16x16x32_bf16 v[196:199], a[192:195], v[164:167], v[196:199]\n"
"s_waitcnt vmcnt(12)\n"
"v_cvt_pk_bf16_f32 v215, v206, v207\n"
"v_mfma_f32_16x16x32_bf16 v[196:199], a[196:199], v[168:171], v[196:199]\n"
"s_barrier\n"
"v_mfma_f32_16x16x32_bf16 v[196:199], a[200:203], v[172:175], v[196:199]\n"
"s_add_u32 m0, s30, 16384\n"
"v_exp_f32 v192, v192\n"
"v_cmp_ne_u32 vcc, s14, v253\n"
"v_mfma_f32_16x16x32_bf16 v[32:35], a[204:207], v[212:215], v[32:35]\n"
"s_xor_b32 s25, s19, 73728\n"
"s_waitcnt vmcnt(8)\n"
"ds_read_b64_tr_b16 a[48:49], v234 offset:49152\n"
"v_mfma_f32_16x16x32_bf16 v[36:39], a[208:211], v[212:215], v[36:39]\n"
"v_exp_f32 v193, v193\n"
"v_mfma_f32_16x16x32_bf16 v[40:43], a[212:215], v[212:215], v[40:43]\n"
"ds_read_b64_tr_b16 a[50:51], v234 offset:51200\n"
"s_cbranch_vccnz L_xsel_retry\n"
"L_xsel_back:\n"
"v_cvt_pk_bf16_f32 v208, v192, v193\n"
"v_mfma_f32_16x16x32_bf16 v[44:47], a[216:219], v[212:215], v[44:47]\n"
"v_exp_f32 v194, v194\n"
"v_mfma_f32_16x16x32_bf16 v[48:51], a[220:223], v[212:215], v[48:51]\n"
"buffer_load_dwordx4 v238, s[44:47], s25 offen offset:0 lds\n"
"v_mfma_f32_16x16x32_bf16 v[52:55], a[224:227], v[212:215], v[52:55]\n"
"v_exp_f32 v195, v195\n"
"v_mfma_f32_16x16x32_bf16 v[56:59], a[228:231], v[212:215], v[56:59]\n"
"ds_read_b64_tr_b16 a[52:53], v235 offset:49152\n"
"v_cvt_pk_bf16_f32 v209, v194, v195\n"
"v_mfma_f32_16x16x32_bf16 v[60:63], a[232:235], v[212:215], v[60:63]\n"
"v_exp_f32 v196, v196\n"
"v_mfma_f32_16x16x32_bf16 a[240:243], a[252:255], v[212:215], a[240:243]\n"
"buffer_load_dwordx4 v239, s[44:47], s25 offen offset:1024 lds\n"
"v_mfma_f32_16x16x32_bf16 v[200:203], a[172:175], v[176:179], 0\n"
"s_add_u32 m0, s30, 24576\n"
"v_exp_f32 v197, v197\n"
"v_mfma_f32_16x16x32_bf16 v[200:203], a[176:179], v[180:183], v[200:203]\n"
"ds_read_b128 a[172:175], v224 offset:32768\n"
"v_cvt_pk_bf16_f32 v210, v196, v197\n"
"v_mfma_f32_16x16x32_bf16 v[200:203], a[180:183], v[184:187], v[200:203]\n"
"v_exp_f32 v198, v198\n"
"v_mfma_f32_16x16x32_bf16 v[200:203], a[184:187], v[188:191], v[200:203]\n"
"ds_read_b128 a[176:179], v225 offset:32768\n"
"ds_read_b128 a[180:183], v226 offset:32768\n"
"v_mfma_f32_16x16x32_bf16 v[204:207], a[188:191], v[176:179], 0\n"
"buffer_load_dwordx4 v240, s[48:51], s25 offen offset:0 lds\n"
"v_exp_f32 v199, v199\n"
"v_mfma_f32_16x16x32_bf16 v[204:207], a[192:195], v[180:183], v[204:207]\n"
"ds_read_b128 a[184:187], v227 offset:32768\n"
"v_cvt_pk_bf16_f32 v211, v198, v199\n"
"v_mfma_f32_16x16x32_bf16 v[204:207], a[196:199], v[184:187], v[204:207]\n"
"ds_read_b128 a[188:191], v224 offset:36864\n"
"ds_read_b128 a[192:195], v225 offset:36864\n"
"v_mfma_f32_16x16x32_bf16 v[204:207], a[200:203], v[188:191], v[204:207]\n"
"v_exp_f32 v200, v200\n"
"v_mfma_f32_16x16x32_bf16 v[64:67], a[204:207], v[208:211], v[64:67]\n"
"ds_read_b128 a[196:199], v226 offset:36864\n"
"ds_read_b128 a[200:203], v227 offset:36864\n"
"buffer_load_dwordx4 v241, s[48:51], s25 offen offset:1024 lds\n"
"v_mfma_f32_16x16x32_bf16 v[68:71], a[208:211], v[208:211], v[68:71]\n"
"v_exp_f32 v201, v201\n"
"v_mfma_f32_16x16x32_bf16 v[72:75], a[212:215], v[208:211], v[72:75]\n"
"ds_read_b64_tr_b16 a[54:55], v235 offset:51200\n"
"v_cvt_pk_bf16_f32 v212, v200, v201\n"
"v_mfma_f32_16x16x32_bf16 v[76:79], a[216:219], v[208:211], v[76:79]\n"
"s_add_u32 m0, s90, 65536\n"
"s_xor_b32 s24, s89, 163840\n"
"v_exp_f32 v202, v202\n"
"v_mfma_f32_16x16x32_bf16 v[80:83], a[220:223], v[208:211], v[80:83]\n"
"ds_read_b64_tr_b16 a[56:57], v236 offset:49152\n"
"ds_read_b64_tr_b16 a[58:59], v236 offset:51200\n"
"v_mfma_f32_16x16x32_bf16 v[84:87], a[224:227], v[208:211], v[84:87]\n"
"buffer_load_dwordx4 v230, s[52:55], s24 offen offset:0 lds\n"
"v_exp_f32 v203, v203\n"
"v_mfma_f32_16x16x32_bf16 v[88:91], a[228:231], v[208:211], v[88:91]\n"
"ds_read_b64_tr_b16 a[60:61], v237 offset:49152\n"
"v_cvt_pk_bf16_f32 v213, v202, v203\n"
"v_mfma_f32_16x16x32_bf16 v[92:95], a[232:235], v[208:211], v[92:95]\n"
"v_exp_f32 v204, v204\n"
"v_mfma_f32_16x16x32_bf16 a[244:247], a[252:255], v[208:211], a[244:247]\n"
"s_waitcnt lgkmcnt(10)\n"
"ds_read_b64_tr_b16 a[62:63], v237 offset:51200\n"
"v_mfma_f32_16x16x32_bf16 v[192:195], a[172:175], v[128:131], 0\n"
"buffer_load_dwordx4 v231, s[52:55], s24 offen offset:1024 lds\n"
"v_exp_f32 v205, v205\n"
"v_mfma_f32_16x16x32_bf16 v[192:195], a[176:179], v[132:135], v[192:195]\n"
"s_waitcnt lgkmcnt(9)\n"
"v_cvt_pk_bf16_f32 v214, v204, v205\n"
"v_and_b32 v253, 0xffff0000, v252\n"
"v_rcp_f32 v253, v253\n"
"v_lshlrev_b32 v252, 16, v252\n"
"v_lshlrev_b32 v248, 16, v250\n"
"v_mfma_f32_16x16x32_bf16 v[192:195], a[180:183], v[136:139], v[192:195]\n"
"v_exp_f32 v206, v206\n"
"v_mfma_f32_16x16x32_bf16 v[192:195], a[184:187], v[140:143], v[192:195]\n"
"s_waitcnt lgkmcnt(7)\n"
"v_and_b32 v249, 0xffff0000, v250\n"
"v_lshlrev_b32 v250, 16, v251\n"
"v_and_b32 v251, 0xffff0000, v251\n"
"v_sub_f32 v248, v248, v252\n"
"v_mfma_f32_16x16x32_bf16 v[196:199], a[188:191], v[128:131], 0\n"
"v_exp_f32 v207, v207\n"
"v_mfma_f32_16x16x32_bf16 v[196:199], a[192:195], v[132:135], v[196:199]\n"
"s_waitcnt lgkmcnt(5)\n"
"v_cvt_pk_bf16_f32 v215, v206, v207\n"
"v_sub_f32 v249, v249, v252\n"
"v_sub_f32 v250, v250, v252\n"
"v_sub_f32 v251, v251, v252\n"
"v_mul_f32 v248, v248, v253\n"
"v_mfma_f32_16x16x32_bf16 v[196:199], a[196:199], v[136:139], v[196:199]\n"
"buffer_load_dwordx4 v232, s[52:55], s24 offen offset:2048 lds\n"
"v_mfma_f32_16x16x32_bf16 v[196:199], a[200:203], v[140:143], v[196:199]\n"
"v_exp_f32 v192, v192\n"
"v_mfma_f32_16x16x32_bf16 v[96:99], a[204:207], v[212:215], v[96:99]\n"
"ds_read_b64_tr_b16 a[204:205], v216 offset:40960\n"
"ds_read_b64_tr_b16 a[206:207], v216 offset:45056\n"
"v_mfma_f32_16x16x32_bf16 v[100:103], a[208:211], v[212:215], v[100:103]\n"
"v_exp_f32 v193, v193\n"
"v_mfma_f32_16x16x32_bf16 v[104:107], a[212:215], v[212:215], v[104:107]\n"
"ds_read_b64_tr_b16 a[208:209], v217 offset:40960\n"
"buffer_load_dwordx4 v233, s[52:55], s24 offen offset:3072 lds\n"
"v_cvt_pk_bf16_f32 v208, v192, v193\n"
"v_mfma_f32_16x16x32_bf16 v[108:111], a[216:219], v[212:215], v[108:111]\n"
"v_exp_f32 v194, v194\n"
"v_mfma_f32_16x16x32_bf16 v[112:115], a[220:223], v[212:215], v[112:115]\n"
"ds_read_b64_tr_b16 a[210:211], v217 offset:45056\n"
"ds_read_b64_tr_b16 a[212:213], v218 offset:40960\n"
"v_mfma_f32_16x16x32_bf16 v[116:119], a[224:227], v[212:215], v[116:119]\n"
"s_add_u32 m0, s90, 81920\n"
"v_exp_f32 v195, v195\n"
"v_mfma_f32_16x16x32_bf16 v[120:123], a[228:231], v[212:215], v[120:123]\n"
"ds_read_b64_tr_b16 a[214:215], v218 offset:45056\n"
"v_cvt_pk_bf16_f32 v209, v194, v195\n"
"v_mfma_f32_16x16x32_bf16 v[124:127], a[232:235], v[212:215], v[124:127]\n"
"s_xor_b32 s24, s89, 196608\n"
"v_exp_f32 v196, v196\n"
"v_mfma_f32_16x16x32_bf16 a[248:251], a[252:255], v[212:215], a[248:251]\n"
"ds_read_b64_tr_b16 a[216:217], v219 offset:40960\n"
"ds_read_b64_tr_b16 a[218:219], v219 offset:45056\n"
"v_mfma_f32_16x16x32_bf16 v[200:203], a[172:175], v[144:147], 0\n"
"buffer_load_dwordx4 v230, s[52:55], s24 offen offset:0 lds\n"
"v_exp_f32 v197, v197\n"
"v_mfma_f32_16x16x32_bf16 v[200:203], a[176:179], v[148:151], v[200:203]\n"
"ds_read_b64_tr_b16 a[220:221], v220 offset:40960\n"
"v_cvt_pk_bf16_f32 v210, v196, v197\n"
"v_mfma_f32_16x16x32_bf16 v[200:203], a[180:183], v[152:155], v[200:203]\n"
"v_exp_f32 v198, v198\n"
"v_mfma_f32_16x16x32_bf16 v[200:203], a[184:187], v[156:159], v[200:203]\n"
"ds_read_b64_tr_b16 a[222:223], v220 offset:45056\n"
"ds_read_b64_tr_b16 a[224:225], v221 offset:40960\n"
"v_mfma_f32_16x16x32_bf16 v[204:207], a[188:191], v[144:147], 0\n"
"v_exp_f32 v199, v199\n"
"v_mfma_f32_16x16x32_bf16 v[204:207], a[192:195], v[148:151], v[204:207]\n"
"ds_read_b64_tr_b16 a[226:227], v221 offset:45056\n"
"v_cvt_pk_bf16_f32 v211, v198, v199\n"
"v_mfma_f32_16x16x32_bf16 v[204:207], a[196:199], v[152:155], v[204:207]\n"
"ds_read_b64_tr_b16 a[228:229], v222 offset:40960\n"
"ds_read_b64_tr_b16 a[230:231], v222 offset:45056\n"
"v_mfma_f32_16x16x32_bf16 v[204:207], a[200:203], v[156:159], v[204:207]\n"
"s_waitcnt lgkmcnt(10)\n"
"v_exp_f32 v200, v200\n"
"v_mfma_f32_16x16x32_bf16 v[0:3], a[204:207], v[208:211], v[0:3]\n"
"ds_read_b64_tr_b16 a[232:233], v223 offset:40960\n"
"ds_read_b64_tr_b16 a[234:235], v223 offset:45056\n"
"v_mfma_f32_16x16x32_bf16 v[4:7], a[208:211], v[208:211], v[4:7]\n"
"s_waitcnt lgkmcnt(8)\n"
"v_exp_f32 v201, v201\n"
"v_mfma_f32_16x16x32_bf16 v[8:11], a[212:215], v[208:211], v[8:11]\n"
"v_cvt_pk_bf16_f32 v212, v200, v201\n"
"v_mul_f32 v249, v249, v253\n"
"v_mul_f32 v250, v250, v253\n"
"v_mul_f32 v251, v251, v253\n"
"v_cvt_pk_bf16_f32 v248, v248, v249\n"
"v_cvt_pk_bf16_f32 v249, v250, v251\n"
"v_mfma_f32_16x16x32_bf16 v[12:15], a[216:219], v[208:211], v[12:15]\n"
"s_waitcnt lgkmcnt(4)\n"
"v_exp_f32 v202, v202\n"
"v_mfma_f32_16x16x32_bf16 v[16:19], a[220:223], v[208:211], v[16:19]\n"
"buffer_load_dwordx4 v231, s[52:55], s24 offen offset:1024 lds\n"
"v_mfma_f32_16x16x32_bf16 v[20:23], a[224:227], v[208:211], v[20:23]\n"
"s_waitcnt lgkmcnt(0)\n"
"v_exp_f32 v203, v203\n"
"v_mfma_f32_16x16x32_bf16 v[24:27], a[228:231], v[208:211], v[24:27]\n"
"v_cvt_pk_bf16_f32 v213, v202, v203\n"
"v_and_b32 v250, 1, v242\n"
"v_and_b32 v251, 48, v242\n"
"v_lshrrev_b32 v252, 1, v251\n"
"v_lshl_add_u32 v250, v250, 7, v252\n"
"v_mfma_f32_16x16x32_bf16 v[28:31], a[232:235], v[208:211], v[28:31]\n"
"v_exp_f32 v204, v204\n"
"v_mfma_f32_16x16x32_bf16 a[236:239], a[252:255], v[208:211], a[236:239]\n"
"buffer_load_dwordx4 v232, s[52:55], s24 offen offset:2048 lds\n"
"v_mfma_f32_16x16x32_bf16 v[192:195], a[172:175], v[160:163], 0\n"
"v_exp_f32 v205, v205\n"
"v_mfma_f32_16x16x32_bf16 v[192:195], a[176:179], v[164:167], v[192:195]\n"
"v_cvt_pk_bf16_f32 v214, v204, v205\n"
"v_lshl_add_u32 v251, v251, 5, s5\n"
"v_bfe_u32 v252, v242, 2, 2\n"
"v_lshl_add_u32 v251, v252, 6, v251\n"
"v_and_b32 v252, 3, v242\n"
"v_mfma_f32_16x16x32_bf16 v[192:195], a[180:183], v[168:171], v[192:195]\n"
"v_exp_f32 v206, v206\n"
"v_mfma_f32_16x16x32_bf16 v[192:195], a[184:187], v[172:175], v[192:195]\n"
"buffer_load_dwordx4 v233, s[52:55], s24 offen offset:3072 lds\n"
"v_mfma_f32_16x16x32_bf16 v[196:199], a[188:191], v[160:163], 0\n"
"v_exp_f32 v207, v207\n"
"v_mfma_f32_16x16x32_bf16 v[196:199], a[192:195], v[164:167], v[196:199]\n"
"v_cvt_pk_bf16_f32 v215, v206, v207\n"
"v_lshl_add_u32 v251, v252, 3, v251\n"
"v_accvgpr_write_b32 a168, v248\n"
"v_accvgpr_write_b32 a169, v249\n"
"v_mfma_f32_16x16x32_bf16 v[196:199], a[196:199], v[168:171], v[196:199]\n"
"s_barrier\n"
"v_mfma_f32_16x16x32_bf16 v[196:199], a[200:203], v[172:175], v[196:199]\n"
"s_add_u32 m0, s30, 32768\n"
"ds_bpermute_b32 a144, v250, a168 offset:0\n"
"v_exp_f32 v192, v192\n"
"v_mfma_f32_16x16x32_bf16 v[32:35], a[204:207], v[212:215], v[32:35]\n"
"s_xor_b32 s25, s19, 81920\n"
"s_waitcnt vmcnt(12)\n"
"ds_read_b64_tr_b16 a[64:65], v244 offset:0\n"
"v_mfma_f32_16x16x32_bf16 v[36:39], a[208:211], v[212:215], v[36:39]\n"
"v_exp_f32 v193, v193\n"
"v_mfma_f32_16x16x32_bf16 v[40:43], a[212:215], v[212:215], v[40:43]\n"
"ds_read_b64_tr_b16 a[66:67], v244 offset:2048\n"
"v_cvt_pk_bf16_f32 v208, v192, v193\n"
"v_mfma_f32_16x16x32_bf16 v[44:47], a[216:219], v[212:215], v[44:47]\n"
"v_exp_f32 v194, v194\n"
"v_mfma_f32_16x16x32_bf16 v[48:51], a[220:223], v[212:215], v[48:51]\n"
"buffer_load_dwordx4 v238, s[44:47], s25 offen offset:0 lds\n"
"v_mfma_f32_16x16x32_bf16 v[52:55], a[224:227], v[212:215], v[52:55]\n"
"v_exp_f32 v195, v195\n"
"v_mfma_f32_16x16x32_bf16 v[56:59], a[228:231], v[212:215], v[56:59]\n"
"ds_read_b64_tr_b16 a[68:69], v245 offset:0\n"
"v_cvt_pk_bf16_f32 v209, v194, v195\n"
"v_mfma_f32_16x16x32_bf16 v[60:63], a[232:235], v[212:215], v[60:63]\n"
"v_exp_f32 v196, v196\n"
"v_mfma_f32_16x16x32_bf16 a[240:243], a[252:255], v[212:215], a[240:243]\n"
"buffer_load_dwordx4 v239, s[44:47], s25 offen offset:1024 lds\n"
"v_mfma_f32_16x16x32_bf16 v[200:203], a[172:175], v[176:179], 0\n"
"s_add_u32 m0, s30, 40960\n"
"v_exp_f32 v197, v197\n"
"v_mfma_f32_16x16x32_bf16 v[200:203], a[176:179], v[180:183], v[200:203]\n"
"ds_read_b128 a[172:175], v224 offset:49152\n"
"v_cvt_pk_bf16_f32 v210, v196, v197\n"
"v_mfma_f32_16x16x32_bf16 v[200:203], a[180:183], v[184:187], v[200:203]\n"
"v_exp_f32 v198, v198\n"
"v_mfma_f32_16x16x32_bf16 v[200:203], a[184:187], v[188:191], v[200:203]\n"
"ds_read_b128 a[176:179], v225 offset:49152\n"
"ds_read_b128 a[180:183], v226 offset:49152\n"
"v_mfma_f32_16x16x32_bf16 v[204:207], a[188:191], v[176:179], 0\n"
"buffer_load_dwordx4 v240, s[48:51], s25 offen offset:0 lds\n"
"v_exp_f32 v199, v199\n"
"v_mfma_f32_16x16x32_bf16 v[204:207], a[192:195], v[180:183], v[204:207]\n"
"ds_read_b128 a[184:187], v227 offset:49152\n"
"v_cvt_pk_bf16_f32 v211, v198, v199\n"
"v_mfma_f32_16x16x32_bf16 v[204:207], a[196:199], v[184:187], v[204:207]\n"
"ds_read_b128 a[188:191], v224 offset:53248\n"
"ds_read_b128 a[192:195], v225 offset:53248\n"
"v_mfma_f32_16x16x32_bf16 v[204:207], a[200:203], v[188:191], v[204:207]\n"
"v_exp_f32 v200, v200\n"
"v_mfma_f32_16x16x32_bf16 v[64:67], a[204:207], v[208:211], v[64:67]\n"
"ds_read_b128 a[196:199], v226 offset:53248\n"
"ds_read_b128 a[200:203], v227 offset:53248\n"
"buffer_load_dwordx4 v241, s[48:51], s25 offen offset:1024 lds\n"
"v_mfma_f32_16x16x32_bf16 v[68:71], a[208:211], v[208:211], v[68:71]\n"
"v_exp_f32 v201, v201\n"
"v_mfma_f32_16x16x32_bf16 v[72:75], a[212:215], v[208:211], v[72:75]\n"
"ds_read_b64_tr_b16 a[70:71], v245 offset:2048\n"
"v_cvt_pk_bf16_f32 v212, v200, v201\n"
"v_mfma_f32_16x16x32_bf16 v[76:79], a[216:219], v[208:211], v[76:79]\n"
"s_add_u32 m0, s90, 98304\n"
"v_exp_f32 v202, v202\n"
"v_mfma_f32_16x16x32_bf16 v[80:83], a[220:223], v[208:211], v[80:83]\n"
"ds_read_b64_tr_b16 a[72:73], v246 offset:0\n"
"ds_read_b64_tr_b16 a[74:75], v246 offset:2048\n"
"v_mfma_f32_16x16x32_bf16 v[84:87], a[224:227], v[208:211], v[84:87]\n"
"s_xor_b32 s24, s89, 229376\n"
"v_exp_f32 v203, v203\n"
"v_mfma_f32_16x16x32_bf16 v[88:91], a[228:231], v[208:211], v[88:91]\n"
"ds_read_b64_tr_b16 a[76:77], v247 offset:0\n"
"v_cvt_pk_bf16_f32 v213, v202, v203\n"
"v_mfma_f32_16x16x32_bf16 v[92:95], a[232:235], v[208:211], v[92:95]\n"
"v_exp_f32 v204, v204\n"
"v_mfma_f32_16x16x32_bf16 a[244:247], a[252:255], v[208:211], a[244:247]\n"
"s_waitcnt lgkmcnt(10)\n"
"ds_read_b64_tr_b16 a[78:79], v247 offset:2048\n"
"v_mfma_f32_16x16x32_bf16 v[192:195], a[172:175], v[128:131], 0\n"
"v_exp_f32 v205, v205\n"
"v_mfma_f32_16x16x32_bf16 v[192:195], a[176:179], v[132:135], v[192:195]\n"
"s_waitcnt lgkmcnt(9)\n"
"v_cvt_pk_bf16_f32 v214, v204, v205\n"
"v_mfma_f32_16x16x32_bf16 v[192:195], a[180:183], v[136:139], v[192:195]\n"
"v_exp_f32 v206, v206\n"
"v_mfma_f32_16x16x32_bf16 v[192:195], a[184:187], v[140:143], v[192:195]\n"
"s_waitcnt lgkmcnt(7)\n"
"ds_bpermute_b32 a145, v250, a169 offset:0\n"
"ds_bpermute_b32 a146, v250, a168 offset:4\n"
"v_mfma_f32_16x16x32_bf16 v[196:199], a[188:191], v[128:131], 0\n"
"ds_bpermute_b32 a147, v250, a169 offset:4\n"
"v_exp_f32 v207, v207\n"
"v_mfma_f32_16x16x32_bf16 v[196:199], a[192:195], v[132:135], v[196:199]\n"
"s_waitcnt lgkmcnt(8)\n"
"ds_bpermute_b32 a148, v250, a168 offset:32\n"
"v_cvt_pk_bf16_f32 v215, v206, v207\n"
"v_mfma_f32_16x16x32_bf16 v[196:199], a[196:199], v[136:139], v[196:199]\n"
"buffer_load_dwordx4 v230, s[52:55], s24 offen offset:0 lds\n"
"v_mfma_f32_16x16x32_bf16 v[196:199], a[200:203], v[140:143], v[196:199]\n"
"v_exp_f32 v192, v192\n"
"v_mfma_f32_16x16x32_bf16 v[96:99], a[204:207], v[212:215], v[96:99]\n"
"ds_read_b64_tr_b16 a[204:205], v216 offset:57344\n"
"ds_read_b64_tr_b16 a[206:207], v216 offset:61440\n"
"v_mfma_f32_16x16x32_bf16 v[100:103], a[208:211], v[212:215], v[100:103]\n"
"v_exp_f32 v193, v193\n"
"v_mfma_f32_16x16x32_bf16 v[104:107], a[212:215], v[212:215], v[104:107]\n"
"ds_read_b64_tr_b16 a[208:209], v217 offset:57344\n"
"v_cvt_pk_bf16_f32 v208, v192, v193\n"
"v_mfma_f32_16x16x32_bf16 v[108:111], a[216:219], v[212:215], v[108:111]\n"
"v_exp_f32 v194, v194\n"
"v_mfma_f32_16x16x32_bf16 v[112:115], a[220:223], v[212:215], v[112:115]\n"
"ds_read_b64_tr_b16 a[210:211], v217 offset:61440\n"
"ds_read_b64_tr_b16 a[212:213], v218 offset:57344\n"
"v_mfma_f32_16x16x32_bf16 v[116:119], a[224:227], v[212:215], v[116:119]\n"
"v_exp_f32 v195, v195\n"
"v_mfma_f32_16x16x32_bf16 v[120:123], a[228:231], v[212:215], v[120:123]\n"
"ds_read_b64_tr_b16 a[214:215], v218 offset:61440\n"
"v_cvt_pk_bf16_f32 v209, v194, v195\n"
"v_mfma_f32_16x16x32_bf16 v[124:127], a[232:235], v[212:215], v[124:127]\n"
"v_exp_f32 v196, v196\n"
"v_mfma_f32_16x16x32_bf16 a[248:251], a[252:255], v[212:215], a[248:251]\n"
"ds_read_b64_tr_b16 a[216:217], v219 offset:57344\n"
"ds_read_b64_tr_b16 a[218:219], v219 offset:61440\n"
"v_mfma_f32_16x16x32_bf16 v[200:203], a[172:175], v[144:147], 0\n"
"v_exp_f32 v197, v197\n"
"v_mfma_f32_16x16x32_bf16 v[200:203], a[176:179], v[148:151], v[200:203]\n"
"ds_read_b64_tr_b16 a[220:221], v220 offset:57344\n"
"v_cvt_pk_bf16_f32 v210, v196, v197\n"
"v_mfma_f32_16x16x32_bf16 v[200:203], a[180:183], v[152:155], v[200:203]\n"
"v_exp_f32 v198, v198\n"
"v_mfma_f32_16x16x32_bf16 v[200:203], a[184:187], v[156:159], v[200:203]\n"
"ds_read_b64_tr_b16 a[222:223], v220 offset:61440\n"
"ds_read_b64_tr_b16 a[224:225], v221 offset:57344\n"
"v_mfma_f32_16x16x32_bf16 v[204:207], a[188:191], v[144:147], 0\n"
"v_exp_f32 v199, v199\n"
"v_mfma_f32_16x16x32_bf16 v[204:207], a[192:195], v[148:151], v[204:207]\n"
"ds_read_b64_tr_b16 a[226:227], v221 offset:61440\n"
"v_cvt_pk_bf16_f32 v211, v198, v199\n"
"v_mfma_f32_16x16x32_bf16 v[204:207], a[196:199], v[152:155], v[204:207]\n"
"ds_read_b64_tr_b16 a[228:229], v222 offset:57344\n"
"ds_read_b64_tr_b16 a[230:231], v222 offset:61440\n"
"v_mfma_f32_16x16x32_bf16 v[204:207], a[200:203], v[156:159], v[204:207]\n"
"s_waitcnt lgkmcnt(10)\n"
"v_exp_f32 v200, v200\n"
"v_mfma_f32_16x16x32_bf16 v[0:3], a[204:207], v[208:211], v[0:3]\n"
"ds_read_b64_tr_b16 a[232:233], v223 offset:57344\n"
"ds_read_b64_tr_b16 a[234:235], v223 offset:61440\n"
"v_mfma_f32_16x16x32_bf16 v[4:7], a[208:211], v[208:211], v[4:7]\n"
"s_waitcnt lgkmcnt(8)\n"
"v_exp_f32 v201, v201\n"
"v_mfma_f32_16x16x32_bf16 v[8:11], a[212:215], v[208:211], v[8:11]\n"
"ds_bpermute_b32 a149, v250, a169 offset:32\n"
"ds_bpermute_b32 a150, v250, a168 offset:36\n"
"ds_bpermute_b32 a151, v250, a169 offset:36\n"
"ds_bpermute_b32 a152, v250, a168 offset:64\n"
"ds_bpermute_b32 a153, v250, a169 offset:64\n"
"v_cvt_pk_bf16_f32 v212, v200, v201\n"
"v_mfma_f32_16x16x32_bf16 v[12:15], a[216:219], v[208:211], v[12:15]\n"
"s_waitcnt lgkmcnt(9)\n"
"v_exp_f32 v202, v202\n"
"v_mfma_f32_16x16x32_bf16 v[16:19], a[220:223], v[208:211], v[16:19]\n"
"buffer_load_dwordx4 v231, s[52:55], s24 offen offset:1024 lds\n"
"v_mfma_f32_16x16x32_bf16 v[20:23], a[224:227], v[208:211], v[20:23]\n"
"s_waitcnt lgkmcnt(5)\n"
"v_exp_f32 v203, v203\n"
"v_mfma_f32_16x16x32_bf16 v[24:27], a[228:231], v[208:211], v[24:27]\n"
"ds_bpermute_b32 a154, v250, a168 offset:68\n"
"v_cvt_pk_bf16_f32 v213, v202, v203\n"
"v_mfma_f32_16x16x32_bf16 v[28:31], a[232:235], v[208:211], v[28:31]\n"
"ds_bpermute_b32 a155, v250, a169 offset:68\n"
"v_exp_f32 v204, v204\n"
"v_mfma_f32_16x16x32_bf16 a[236:239], a[252:255], v[208:211], a[236:239]\n"
"buffer_load_dwordx4 v232, s[52:55], s24 offen offset:2048 lds\n"
"ds_bpermute_b32 a156, v250, a168 offset:96\n"
"v_mfma_f32_16x16x32_bf16 v[192:195], a[172:175], v[160:163], 0\n"
"ds_bpermute_b32 a157, v250, a169 offset:96\n"
"v_exp_f32 v205, v205\n"
"v_mfma_f32_16x16x32_bf16 v[192:195], a[176:179], v[164:167], v[192:195]\n"
"ds_bpermute_b32 a158, v250, a168 offset:100\n"
"v_cvt_pk_bf16_f32 v214, v204, v205\n"
"v_mfma_f32_16x16x32_bf16 v[192:195], a[180:183], v[168:171], v[192:195]\n"
"ds_bpermute_b32 a159, v250, a169 offset:100\n"
"v_exp_f32 v206, v206\n"
"v_mfma_f32_16x16x32_bf16 v[192:195], a[184:187], v[172:175], v[192:195]\n"
"buffer_load_dwordx4 v233, s[52:55], s24 offen offset:3072 lds\n"
"ds_read_b64_tr_b16 a[128:129], v251 offset:0\n"
"v_mfma_f32_16x16x32_bf16 v[196:199], a[188:191], v[160:163], 0\n"
"ds_read_b64_tr_b16 a[130:131], v251 offset:256\n"
"v_exp_f32 v207, v207\n"
"v_mfma_f32_16x16x32_bf16 v[196:199], a[192:195], v[164:167], v[196:199]\n"
"v_cvt_pk_bf16_f32 v215, v206, v207\n"
"v_mfma_f32_16x16x32_bf16 v[196:199], a[196:199], v[168:171], v[196:199]\n"
"s_barrier\n"
"v_mfma_f32_16x16x32_bf16 v[196:199], a[200:203], v[172:175], v[196:199]\n"
"s_add_u32 m0, s30, 49152\n"
"v_exp_f32 v192, v192\n"
"v_mfma_f32_16x16x32_bf16 v[32:35], a[204:207], v[212:215], v[32:35]\n"
"s_xor_b32 s25, s19, 90112\n"
"s_waitcnt vmcnt(12)\n"
"ds_read_b64_tr_b16 a[80:81], v234 offset:0\n"
"v_mfma_f32_16x16x32_bf16 v[36:39], a[208:211], v[212:215], v[36:39]\n"
"ds_read_b64_tr_b16 a[82:83], v234 offset:2048\n"
"v_exp_f32 v193, v193\n"
"v_mfma_f32_16x16x32_bf16 v[40:43], a[212:215], v[212:215], v[40:43]\n"
"ds_read_b64_tr_b16 a[84:85], v235 offset:0\n"
"v_cvt_pk_bf16_f32 v208, v192, v193\n"
"v_mfma_f32_16x16x32_bf16 v[44:47], a[216:219], v[212:215], v[44:47]\n"
"v_exp_f32 v194, v194\n"
"v_mfma_f32_16x16x32_bf16 v[48:51], a[220:223], v[212:215], v[48:51]\n"
"buffer_load_dwordx4 v238, s[44:47], s25 offen offset:0 lds\n"
"v_mfma_f32_16x16x32_bf16 v[52:55], a[224:227], v[212:215], v[52:55]\n"
"v_exp_f32 v195, v195\n"
"v_mfma_f32_16x16x32_bf16 v[56:59], a[228:231], v[212:215], v[56:59]\n"
"ds_read_b64_tr_b16 a[86:87], v235 offset:2048\n"
"v_cvt_pk_bf16_f32 v209, v194, v195\n"
"v_mfma_f32_16x16x32_bf16 v[60:63], a[232:235], v[212:215], v[60:63]\n"
"v_exp_f32 v196, v196\n"
"v_mfma_f32_16x16x32_bf16 a[240:243], a[252:255], v[212:215], a[240:243]\n"
"buffer_load_dwordx4 v239, s[44:47], s25 offen offset:1024 lds\n"
"v_mfma_f32_16x16x32_bf16 v[200:203], a[172:175], v[176:179], 0\n"
"s_add_u32 m0, s30, 57344\n"
"v_exp_f32 v197, v197\n"
"v_mfma_f32_16x16x32_bf16 v[200:203], a[176:179], v[180:183], v[200:203]\n"
"ds_read_b128 a[172:175], v224 offset:0\n"
"v_cvt_pk_bf16_f32 v210, v196, v197\n"
"v_mfma_f32_16x16x32_bf16 v[200:203], a[180:183], v[184:187], v[200:203]\n"
"v_exp_f32 v198, v198\n"
"v_mfma_f32_16x16x32_bf16 v[200:203], a[184:187], v[188:191], v[200:203]\n"
"ds_read_b128 a[176:179], v225 offset:0\n"
"ds_read_b128 a[180:183], v226 offset:0\n"
"v_mfma_f32_16x16x32_bf16 v[204:207], a[188:191], v[176:179], 0\n"
"buffer_load_dwordx4 v240, s[48:51], s25 offen offset:0 lds\n"
"v_exp_f32 v199, v199\n"
"v_mfma_f32_16x16x32_bf16 v[204:207], a[192:195], v[180:183], v[204:207]\n"
"ds_read_b128 a[184:187], v227 offset:0\n"
"v_cvt_pk_bf16_f32 v211, v198, v199\n"
"v_mfma_f32_16x16x32_bf16 v[204:207], a[196:199], v[184:187], v[204:207]\n"
"ds_read_b128 a[188:191], v224 offset:4096\n"
"ds_read_b128 a[192:195], v225 offset:4096\n"
"v_mfma_f32_16x16x32_bf16 v[204:207], a[200:203], v[188:191], v[204:207]\n"
"v_exp_f32 v200, v200\n"
"v_mfma_f32_16x16x32_bf16 v[64:67], a[204:207], v[208:211], v[64:67]\n"
"ds_read_b128 a[196:199], v226 offset:4096\n"
"ds_read_b128 a[200:203], v227 offset:4096\n"
"buffer_load_dwordx4 v241, s[48:51], s25 offen offset:1024 lds\n"
"v_mfma_f32_16x16x32_bf16 v[68:71], a[208:211], v[208:211], v[68:71]\n"
"s_add_u32 m0, s90, 114688\n"
"s_xor_b32 s24, s89, 262144\n"
"v_exp_f32 v201, v201\n"
"v_mfma_f32_16x16x32_bf16 v[72:75], a[212:215], v[208:211], v[72:75]\n"
"ds_read_b64_tr_b16 a[88:89], v236 offset:0\n"
"v_cvt_pk_bf16_f32 v212, v200, v201\n"
"v_mfma_f32_16x16x32_bf16 v[76:79], a[216:219], v[208:211], v[76:79]\n"
"ds_read_b64_tr_b16 a[90:91], v236 offset:2048\n"
"v_exp_f32 v202, v202\n"
"v_mfma_f32_16x16x32_bf16 v[80:83], a[220:223], v[208:211], v[80:83]\n"
"ds_read_b64_tr_b16 a[92:93], v237 offset:0\n"
"ds_read_b64_tr_b16 a[94:95], v237 offset:2048\n"
"v_mfma_f32_16x16x32_bf16 v[84:87], a[224:227], v[208:211], v[84:87]\n"
"s_waitcnt vmcnt(12)\n"
"buffer_load_dwordx4 v230, s[52:55], s24 offen offset:0 lds\n"
"v_exp_f32 v203, v203\n"
"v_mfma_f32_16x16x32_bf16 v[88:91], a[228:231], v[208:211], v[88:91]\n"
"ds_read_b64_tr_b16 a[96:97], v234 offset:16384\n"
"v_cvt_pk_bf16_f32 v213, v202, v203\n"
"v_mfma_f32_16x16x32_bf16 v[92:95], a[232:235], v[208:211], v[92:95]\n"
"ds_read_b64_tr_b16 a[98:99], v234 offset:18432\n"
"v_exp_f32 v204, v204\n"
"v_mfma_f32_16x16x32_bf16 a[244:247], a[252:255], v[208:211], a[244:247]\n"
"s_waitcnt lgkmcnt(12)\n"
"ds_read_b64_tr_b16 a[100:101], v235 offset:16384\n"
"ds_read_b64_tr_b16 a[102:103], v235 offset:18432\n"
"v_mfma_f32_16x16x32_bf16 v[192:195], a[172:175], v[128:131], 0\n"
"buffer_load_dwordx4 v231, s[52:55], s24 offen offset:1024 lds\n"
"v_exp_f32 v205, v205\n"
"v_mfma_f32_16x16x32_bf16 v[192:195], a[176:179], v[132:135], v[192:195]\n"
"s_waitcnt lgkmcnt(12)\n"
"v_cvt_pk_bf16_f32 v214, v204, v205\n"
"v_mfma_f32_16x16x32_bf16 v[192:195], a[180:183], v[136:139], v[192:195]\n"
"v_exp_f32 v206, v206\n"
"v_mfma_f32_16x16x32_bf16 v[192:195], a[184:187], v[140:143], v[192:195]\n"
"s_waitcnt lgkmcnt(10)\n"
"ds_read_b64_tr_b16 a[104:105], v236 offset:16384\n"
"ds_read_b64_tr_b16 a[106:107], v236 offset:18432\n"
"v_mfma_f32_16x16x32_bf16 v[196:199], a[188:191], v[128:131], 0\n"
"buffer_load_dwordx4 v232, s[52:55], s24 offen offset:2048 lds\n"
"v_exp_f32 v207, v207\n"
"v_mfma_f32_16x16x32_bf16 v[196:199], a[192:195], v[132:135], v[196:199]\n"
"s_waitcnt lgkmcnt(10)\n"
"v_cvt_pk_bf16_f32 v215, v206, v207\n"
"v_mfma_f32_16x16x32_bf16 v[196:199], a[196:199], v[136:139], v[196:199]\n"
"ds_read_b64_tr_b16 a[108:109], v237 offset:16384\n"
"ds_read_b64_tr_b16 a[110:111], v237 offset:18432\n"
"v_mfma_f32_16x16x32_bf16 v[196:199], a[200:203], v[140:143], v[196:199]\n"
"v_exp_f32 v192, v192\n"
"v_mfma_f32_16x16x32_bf16 v[96:99], a[204:207], v[212:215], v[96:99]\n"
"ds_read_b64_tr_b16 a[204:205], v216 offset:8192\n"
"ds_read_b64_tr_b16 a[206:207], v216 offset:12288\n"
"buffer_load_dwordx4 v233, s[52:55], s24 offen offset:3072 lds\n"
"v_mfma_f32_16x16x32_bf16 v[100:103], a[208:211], v[212:215], v[100:103]\n"
"v_exp_f32 v193, v193\n"
"v_mfma_f32_16x16x32_bf16 v[104:107], a[212:215], v[212:215], v[104:107]\n"
"ds_read_b64_tr_b16 a[208:209], v217 offset:8192\n"
"v_cvt_pk_bf16_f32 v208, v192, v193\n"
"v_mfma_f32_16x16x32_bf16 v[108:111], a[216:219], v[212:215], v[108:111]\n"
"s_add_u32 m0, s90, 131072\n"
"v_exp_f32 v194, v194\n"
"v_mfma_f32_16x16x32_bf16 v[112:115], a[220:223], v[212:215], v[112:115]\n"
"ds_read_b64_tr_b16 a[210:211], v217 offset:12288\n"
"ds_read_b64_tr_b16 a[212:213], v218 offset:8192\n"
"v_mfma_f32_16x16x32_bf16 v[116:119], a[224:227], v[212:215], v[116:119]\n"
"s_xor_b32 s24, s89, 294912\n"
"v_exp_f32 v195, v195\n"
"v_mfma_f32_16x16x32_bf16 v[120:123], a[228:231], v[212:215], v[120:123]\n"
"ds_read_b64_tr_b16 a[214:215], v218 offset:12288\n"
"v_cvt_pk_bf16_f32 v209, v194, v195\n"
"v_mfma_f32_16x16x32_bf16 v[124:127], a[232:235], v[212:215], v[124:127]\n"
"buffer_load_dwordx4 v230, s[52:55], s24 offen offset:0 lds\n"
"v_exp_f32 v196, v196\n"
"v_mfma_f32_16x16x32_bf16 a[248:251], a[252:255], v[212:215], a[248:251]\n"
"ds_read_b64_tr_b16 a[216:217], v219 offset:8192\n"
"ds_read_b64_tr_b16 a[218:219], v219 offset:12288\n"
"v_mfma_f32_16x16x32_bf16 v[200:203], a[172:175], v[144:147], 0\n"
"v_exp_f32 v197, v197\n"
"v_mfma_f32_16x16x32_bf16 v[200:203], a[176:179], v[148:151], v[200:203]\n"
"ds_read_b64_tr_b16 a[220:221], v220 offset:8192\n"
"v_cvt_pk_bf16_f32 v210, v196, v197\n"
"v_mfma_f32_16x16x32_bf16 v[200:203], a[180:183], v[152:155], v[200:203]\n"
"v_exp_f32 v198, v198\n"
"v_mfma_f32_16x16x32_bf16 v[200:203], a[184:187], v[156:159], v[200:203]\n"
"ds_read_b64_tr_b16 a[222:223], v220 offset:12288\n"
"ds_read_b64_tr_b16 a[224:225], v221 offset:8192\n"
"L_ic5:\n"
"s_cbranch_execz L_ic9\n"
"v_mfma_f32_16x16x32_bf16 v[204:207], a[188:191], v[144:147], 0\n"
"v_exp_f32 v199, v199\n"
"v_mfma_f32_16x16x32_bf16 v[204:207], a[192:195], v[148:151], v[204:207]\n"
"ds_read_b64_tr_b16 a[226:227], v221 offset:12288\n"
"v_cvt_pk_bf16_f32 v211, v198, v199\n"
"v_mfma_f32_16x16x32_bf16 v[204:207], a[196:199], v[152:155], v[204:207]\n"
"ds_read_b64_tr_b16 a[228:229], v222 offset:8192\n"
"ds_read_b64_tr_b16 a[230:231], v222 offset:12288\n"
"v_mfma_f32_16x16x32_bf16 v[204:207], a[200:203], v[156:159], v[204:207]\n"
"s_waitcnt lgkmcnt(10)\n"
"v_exp_f32 v200, v200\n"
"v_mfma_f32_16x16x32_bf16 v[0:3], a[204:207], v[208:211], v[0:3]\n"
"ds_read_b64_tr_b16 a[232:233], v223 offset:8192\n"
"ds_read_b64_tr_b16 a[234:235], v223 offset:12288\n"
"v_mfma_f32_16x16x32_bf16 v[4:7], a[208:211], v[208:211], v[4:7]\n"
"s_waitcnt lgkmcnt(8)\n"
"v_exp_f32 v201, v201\n"
"v_mfma_f32_16x16x32_bf16 v[8:11], a[212:215], v[208:211], v[8:11]\n"
"ds_read_b64_tr_b16 a[132:133], v251 offset:2048\n"
"ds_read_b64_tr_b16 a[134:135], v251 offset:2304\n"
"ds_read_b64_tr_b16 a[136:137], v251 offset:4096\n"
"ds_read_b64_tr_b16 a[138:139], v251 offset:4352\n"
"v_cvt_pk_bf16_f32 v212, v200, v201\n"
"v_mfma_f32_16x16x32_bf16 v[12:15], a[216:219], v[208:211], v[12:15]\n"
"s_waitcnt lgkmcnt(8)\n"
"v_exp_f32 v202, v202\n"
"v_mfma_f32_16x16x32_bf16 v[16:19], a[220:223], v[208:211], v[16:19]\n"
"buffer_load_dwordx4 v231, s[52:55], s24 offen offset:1024 lds\n"
"v_mfma_f32_16x16x32_bf16 v[20:23], a[224:227], v[208:211], v[20:23]\n"
"s_waitcnt lgkmcnt(4)\n"
"v_exp_f32 v203, v203\n"
"v_mfma_f32_16x16x32_bf16 v[24:27], a[228:231], v[208:211], v[24:27]\n"
"ds_read_b64_tr_b16 a[140:141], v251 offset:6144\n"
"v_cvt_pk_bf16_f32 v213, v202, v203\n"
"v_mfma_f32_16x16x32_bf16 v[28:31], a[232:235], v[208:211], v[28:31]\n"
"ds_read_b64_tr_b16 a[142:143], v251 offset:6400\n"
"v_exp_f32 v204, v204\n"
"v_and_b32 v254, 49, v242\n"
"v_lshl_add_u32 v254, v254, 3, s3\n"
"v_and_b32 v253, 6, v242\n"
"v_mfma_f32_16x16x32_bf16 a[236:239], a[252:255], v[208:211], a[236:239]\n"
"buffer_load_dwordx4 v232, s[52:55], s24 offen offset:2048 lds\n"
"v_mfma_f32_16x16x32_bf16 v[192:195], a[172:175], v[160:163], 0\n"
"v_exp_f32 v205, v205\n"
"v_mfma_f32_16x16x32_bf16 v[192:195], a[176:179], v[164:167], v[192:195]\n"
"v_cvt_pk_bf16_f32 v214, v204, v205\n"
"v_lshl_add_u32 v254, v253, 4, v254\n"
"v_mov_b32 v253, s14\n"
"v_mfma_f32_16x16x32_bf16 v[192:195], a[180:183], v[168:171], v[192:195]\n"
"v_exp_f32 v206, v206\n"
"v_mfma_f32_16x16x32_bf16 v[192:195], a[184:187], v[172:175], v[192:195]\n"
"buffer_load_dwordx4 v233, s[52:55], s24 offen offset:3072 lds\n"
"v_mfma_f32_16x16x32_bf16 v[196:199], a[188:191], v[160:163], 0\n"
"v_exp_f32 v207, v207\n"
"v_mfma_f32_16x16x32_bf16 v[196:199], a[192:195], v[164:167], v[196:199]\n"
"v_cvt_pk_bf16_f32 v215, v206, v207\n"
"v_mfma_f32_16x16x32_bf16 v[196:199], a[196:199], v[168:171], v[196:199]\n"
"s_barrier\n"
"v_mfma_f32_16x16x32_bf16 v[196:199], a[200:203], v[172:175], v[196:199]\n"
"s_add_u32 m0, s30, 0\n"
"s_waitcnt lgkmcnt(0)\n"
"v_mfma_f32_16x16x32_bf16 v[248:251], a[128:131], a[144:147], 0\n"
"v_mfma_f32_16x16x32_bf16 v[248:251], a[132:135], a[148:151], v[248:251]\n"
"v_mfma_f32_16x16x32_bf16 v[248:251], a[136:139], a[152:155], v[248:251]\n"
"v_mfma_f32_16x16x32_bf16 v[248:251], a[140:143], a[156:159], v[248:251]\n"
"v_exp_f32 v192, v192\n"
"v_mfma_f32_16x16x32_bf16 v[32:35], a[204:207], v[212:215], v[32:35]\n"
"s_xor_b32 s25, s19, 98304\n"
"s_waitcnt vmcnt(12)\n"
"ds_read_b64_tr_b16 a[112:113], v234 offset:32768\n"
"v_mfma_f32_16x16x32_bf16 v[36:39], a[208:211], v[212:215], v[36:39]\n"
"v_exp_f32 v193, v193\n"
"v_mfma_f32_16x16x32_bf16 v[40:43], a[212:215], v[212:215], v[40:43]\n"
"ds_read_b64_tr_b16 a[114:115], v234 offset:34816\n"
"v_cvt_pk_bf16_f32 v208, v192, v193\n"
"v_mfma_f32_16x16x32_bf16 v[44:47], a[216:219], v[212:215], v[44:47]\n"
"v_exp_f32 v194, v194\n"
"v_mfma_f32_16x16x32_bf16 v[48:51], a[220:223], v[212:215], v[48:51]\n"
"buffer_load_dwordx4 v238, s[44:47], s25 offen offset:0 lds\n"
"v_mfma_f32_16x16x32_bf16 v[52:55], a[224:227], v[212:215], v[52:55]\n"
"v_exp_f32 v195, v195\n"
"v_mfma_f32_16x16x32_bf16 v[56:59], a[228:231], v[212:215], v[56:59]\n"
"ds_read_b64_tr_b16 a[116:117], v235 offset:32768\n"
"v_cvt_pk_bf16_f32 v209, v194, v195\n"
"v_mfma_f32_16x16x32_bf16 v[60:63], a[232:235], v[212:215], v[60:63]\n"
"v_exp_f32 v196, v196\n"
"v_mfma_f32_16x16x32_bf16 a[240:243], a[252:255], v[212:215], a[240:243]\n"
"buffer_load_dwordx4 v239, s[44:47], s25 offen offset:1024 lds\n"
"v_mfma_f32_16x16x32_bf16 v[200:203], a[172:175], v[176:179], 0\n"
"s_add_u32 m0, s30, 8192\n"
"v_exp_f32 v197, v197\n"
"v_mfma_f32_16x16x32_bf16 v[200:203], a[176:179], v[180:183], v[200:203]\n"
"ds_read_b128 a[172:175], v224 offset:16384\n"
"v_cvt_pk_bf16_f32 v210, v196, v197\n"
"v_mfma_f32_16x16x32_bf16 v[200:203], a[180:183], v[184:187], v[200:203]\n"
"v_exp_f32 v198, v198\n"
"v_mfma_f32_16x16x32_bf16 v[200:203], a[184:187], v[188:191], v[200:203]\n"
"ds_read_b128 a[176:179], v225 offset:16384\n"
"ds_read_b128 a[180:183], v226 offset:16384\n"
"v_mfma_f32_16x16x32_bf16 v[204:207], a[188:191], v[176:179], 0\n"
"buffer_load_dwordx4 v240, s[48:51], s25 offen offset:0 lds\n"
"v_exp_f32 v199, v199\n"
"v_mfma_f32_16x16x32_bf16 v[204:207], a[192:195], v[180:183], v[204:207]\n"
"ds_read_b128 a[184:187], v227 offset:16384\n"
"v_cvt_pk_bf16_f32 v211, v198, v199\n"
"v_mfma_f32_16x16x32_bf16 v[204:207], a[196:199], v[184:187], v[204:207]\n"
"ds_read_b128 a[188:191], v224 offset:20480\n"
"ds_read_b128 a[192:195], v225 offset:20480\n"
"v_mfma_f32_16x16x32_bf16 v[204:207], a[200:203], v[188:191], v[204:207]\n"
"v_exp_f32 v200, v200\n"
"v_mfma_f32_16x16x32_bf16 v[64:67], a[204:207], v[208:211], v[64:67]\n"
"ds_read_b128 a[196:199], v226 offset:20480\n"
"ds_read_b128 a[200:203], v227 offset:20480\n"
"buffer_load_dwordx4 v241, s[48:51], s25 offen offset:1024 lds\n"
"v_mfma_f32_16x16x32_bf16 v[68:71], a[208:211], v[208:211], v[68:71]\n"
"v_exp_f32 v201, v201\n"
"v_mfma_f32_16x16x32_bf16 v[72:75], a[212:215], v[208:211], v[72:75]\n"
"ds_read_b64_tr_b16 a[118:119], v235 offset:34816\n"
"v_cvt_pk_bf16_f32 v212, v200, v201\n"
"v_mfma_f32_16x16x32_bf16 v[76:79], a[216:219], v[208:211], v[76:79]\n"
"s_add_u32 m0, s90, 65536\n"
"v_exp_f32 v202, v202\n"
"v_mfma_f32_16x16x32_bf16 v[80:83], a[220:223], v[208:211], v[80:83]\n"
"ds_read_b64_tr_b16 a[120:121], v236 offset:32768\n"
"ds_read_b64_tr_b16 a[122:123], v236 offset:34816\n"
"v_mfma_f32_16x16x32_bf16 v[84:87], a[224:227], v[208:211], v[84:87]\n"
"s_xor_b32 s24, s89, 327680\n"
"v_exp_f32 v203, v203\n"
"v_mfma_f32_16x16x32_bf16 v[88:91], a[228:231], v[208:211], v[88:91]\n"
"ds_read_b64_tr_b16 a[124:125], v237 offset:32768\n"
"v_cvt_pk_bf16_f32 v213, v202, v203\n"
"v_mfma_f32_16x16x32_bf16 v[92:95], a[232:235], v[208:211], v[92:95]\n"
"v_exp_f32 v204, v204\n"
"v_mfma_f32_16x16x32_bf16 a[244:247], a[252:255], v[208:211], a[244:247]\n"
"s_waitcnt lgkmcnt(10)\n"
"ds_read_b64_tr_b16 a[126:127], v237 offset:34816\n"
"v_mfma_f32_16x16x32_bf16 v[192:195], a[172:175], v[128:131], 0\n"
"v_exp_f32 v205, v205\n"
"v_mfma_f32_16x16x32_bf16 v[192:195], a[176:179], v[132:135], v[192:195]\n"
"s_waitcnt lgkmcnt(9)\n"
"v_cvt_pk_bf16_f32 v214, v204, v205\n"
"v_cndmask_b32 v248, v248, v249, s[76:77]\n"
"v_cndmask_b32 v250, v250, v251, s[76:77]\n"
"v_cndmask_b32 v252, v248, v250, s[78:79]\n"
"v_mfma_f32_16x16x32_bf16 v[192:195], a[180:183], v[136:139], v[192:195]\n"
"buffer_store_dwordx2 v[252:253], v254, s[56:59], s6 offen sc1\n"
"v_exp_f32 v206, v206\n"
"v_mfma_f32_16x16x32_bf16 v[192:195], a[184:187], v[140:143], v[192:195]\n"
"s_waitcnt lgkmcnt(7)\n"
"v_lshrrev_b32 v248, 1, v242\n"
"v_and_b32 v249, 16, v243\n"
"v_lshl_add_u32 v248, v248, 10, v249\n"
"v_add_u32 v249, 31744, v248\n"
"v_mfma_f32_16x16x32_bf16 v[196:199], a[188:191], v[128:131], 0\n"
"v_exp_f32 v207, v207\n"
"v_mfma_f32_16x16x32_bf16 v[196:199], a[192:195], v[132:135], v[196:199]\n"
"s_waitcnt lgkmcnt(5)\n"
"v_cvt_pk_bf16_f32 v215, v206, v207\n"
"v_cmp_gt_u32 vcc, 22, v242\n"
"v_mov_b32 v250, 2130706432\n"
"v_mfma_f32_16x16x32_bf16 v[196:199], a[196:199], v[136:139], v[196:199]\n"
"buffer_load_dwordx4 v230, s[52:55], s24 offen offset:0 lds\n"
"v_cndmask_b32 v249, v250, v249, vcc\n"
"v_mfma_f32_16x16x32_bf16 v[196:199], a[200:203], v[140:143], v[196:199]\n"
"v_exp_f32 v192, v192\n"
"v_mfma_f32_16x16x32_bf16 v[96:99], a[204:207], v[212:215], v[96:99]\n"
"ds_read_b64_tr_b16 a[204:205], v216 offset:24576\n"
"ds_read_b64_tr_b16 a[206:207], v216 offset:28672\n"
"v_mfma_f32_16x16x32_bf16 v[100:103], a[208:211], v[212:215], v[100:103]\n"
"v_exp_f32 v193, v193\n"
"v_mfma_f32_16x16x32_bf16 v[104:107], a[212:215], v[212:215], v[104:107]\n"
"ds_read_b64_tr_b16 a[208:209], v217 offset:24576\n"
"v_cvt_pk_bf16_f32 v208, v192, v193\n"
"v_mfma_f32_16x16x32_bf16 v[108:111], a[216:219], v[212:215], v[108:111]\n"
"v_exp_f32 v194, v194\n"
"v_mfma_f32_16x16x32_bf16 v[112:115], a[220:223], v[212:215], v[112:115]\n"
"ds_read_b64_tr_b16 a[210:211], v217 offset:28672\n"
"ds_read_b64_tr_b16 a[212:213], v218 offset:24576\n"
"v_mfma_f32_16x16x32_bf16 v[116:119], a[224:227], v[212:215], v[116:119]\n"
"v_exp_f32 v195, v195\n"
"v_mfma_f32_16x16x32_bf16 v[120:123], a[228:231], v[212:215], v[120:123]\n"
"ds_read_b64_tr_b16 a[214:215], v218 offset:28672\n"
"v_cvt_pk_bf16_f32 v209, v194, v195\n"
"v_mfma_f32_16x16x32_bf16 v[124:127], a[232:235], v[212:215], v[124:127]\n"
"v_exp_f32 v196, v196\n"
"v_mfma_f32_16x16x32_bf16 a[248:251], a[252:255], v[212:215], a[248:251]\n"
"ds_read_b64_tr_b16 a[216:217], v219 offset:24576\n"
"ds_read_b64_tr_b16 a[218:219], v219 offset:28672\n"
"v_mfma_f32_16x16x32_bf16 v[200:203], a[172:175], v[144:147], 0\n"
"v_exp_f32 v197, v197\n"
"v_mfma_f32_16x16x32_bf16 v[200:203], a[176:179], v[148:151], v[200:203]\n"
"ds_read_b64_tr_b16 a[220:221], v220 offset:24576\n"
"v_cvt_pk_bf16_f32 v210, v196, v197\n"
"v_mfma_f32_16x16x32_bf16 v[200:203], a[180:183], v[152:155], v[200:203]\n"
"v_exp_f32 v198, v198\n"
"v_mfma_f32_16x16x32_bf16 v[200:203], a[184:187], v[156:159], v[200:203]\n"
"ds_read_b64_tr_b16 a[222:223], v220 offset:28672\n"
"ds_read_b64_tr_b16 a[224:225], v221 offset:24576\n"
"v_mfma_f32_16x16x32_bf16 v[204:207], a[188:191], v[144:147], 0\n"
"v_exp_f32 v199, v199\n"
"v_mfma_f32_16x16x32_bf16 v[204:207], a[192:195], v[148:151], v[204:207]\n"
"ds_read_b64_tr_b16 a[226:227], v221 offset:28672\n"
"v_cvt_pk_bf16_f32 v211, v198, v199\n"
"v_mfma_f32_16x16x32_bf16 v[204:207], a[196:199], v[152:155], v[204:207]\n"
"ds_read_b64_tr_b16 a[228:229], v222 offset:24576\n"
"ds_read_b64_tr_b16 a[230:231], v222 offset:28672\n"
"v_mfma_f32_16x16x32_bf16 v[204:207], a[200:203], v[156:159], v[204:207]\n"
"s_waitcnt lgkmcnt(10)\n"
"v_exp_f32 v200, v200\n"
"v_mfma_f32_16x16x32_bf16 v[0:3], a[204:207], v[208:211], v[0:3]\n"
"ds_read_b64_tr_b16 a[232:233], v223 offset:24576\n"
"ds_read_b64_tr_b16 a[234:235], v223 offset:28672\n"
"v_mfma_f32_16x16x32_bf16 v[4:7], a[208:211], v[208:211], v[4:7]\n"
"s_waitcnt lgkmcnt(8)\n"
"v_exp_f32 v201, v201\n"
"v_mfma_f32_16x16x32_bf16 v[8:11], a[212:215], v[208:211], v[8:11]\n"
"v_cvt_pk_bf16_f32 v212, v200, v201\n"
"v_mfma_f32_16x16x32_bf16 v[12:15], a[216:219], v[208:211], v[12:15]\n"
"s_waitcnt lgkmcnt(4)\n"
"v_exp_f32 v202, v202\n"
"v_mfma_f32_16x16x32_bf16 v[16:19], a[220:223], v[208:211], v[16:19]\n"
"buffer_load_dwordx4 v231, s[52:55], s24 offen offset:1024 lds\n"
"v_mfma_f32_16x16x32_bf16 v[20:23], a[224:227], v[208:211], v[20:23]\n"
"s_waitcnt lgkmcnt(0)\n"
"v_exp_f32 v203, v203\n"
"v_mfma_f32_16x16x32_bf16 v[24:27], a[228:231], v[208:211], v[24:27]\n"
"v_cvt_pk_bf16_f32 v213, v202, v203\n"
"v_mfma_f32_16x16x32_bf16 v[28:31], a[232:235], v[208:211], v[28:31]\n"
"v_exp_f32 v204, v204\n"
"v_mfma_f32_16x16x32_bf16 a[236:239], a[252:255], v[208:211], a[236:239]\n"
"buffer_load_dwordx4 v232, s[52:55], s24 offen offset:2048 lds\n"
"v_mfma_f32_16x16x32_bf16 v[192:195], a[172:175], v[160:163], 0\n"
"v_exp_f32 v205, v205\n"
"v_mfma_f32_16x16x32_bf16 v[192:195], a[176:179], v[164:167], v[192:195]\n"
"v_cvt_pk_bf16_f32 v214, v204, v205\n"
"v_mfma_f32_16x16x32_bf16 v[192:195], a[180:183], v[168:171], v[192:195]\n"
"v_exp_f32 v206, v206\n"
"v_mfma_f32_16x16x32_bf16 v[192:195], a[184:187], v[172:175], v[192:195]\n"
"buffer_load_dwordx4 v233, s[52:55], s24 offen offset:3072 lds\n"
"v_mfma_f32_16x16x32_bf16 v[196:199], a[188:191], v[160:163], 0\n"
"v_exp_f32 v207, v207\n"
"v_mfma_f32_16x16x32_bf16 v[196:199], a[192:195], v[164:167], v[196:199]\n"
"v_cvt_pk_bf16_f32 v215, v206, v207\n"
"v_mfma_f32_16x16x32_bf16 v[196:199], a[196:199], v[168:171], v[196:199]\n"
"s_barrier\n"
"v_mfma_f32_16x16x32_bf16 v[196:199], a[200:203], v[172:175], v[196:199]\n"
"s_add_u32 m0, s30, 16384\n"
"v_exp_f32 v192, v192\n"
"v_mfma_f32_16x16x32_bf16 v[32:35], a[204:207], v[212:215], v[32:35]\n"
"s_xor_b32 s25, s19, 106496\n"
"v_mfma_f32_16x16x32_bf16 v[36:39], a[208:211], v[212:215], v[36:39]\n"
"v_exp_f32 v193, v193\n"
"v_mfma_f32_16x16x32_bf16 v[40:43], a[212:215], v[212:215], v[40:43]\n"
"v_cvt_pk_bf16_f32 v208, v192, v193\n"
"v_mfma_f32_16x16x32_bf16 v[44:47], a[216:219], v[212:215], v[44:47]\n"
"v_exp_f32 v194, v194\n"
"v_mfma_f32_16x16x32_bf16 v[48:51], a[220:223], v[212:215], v[48:51]\n"
"buffer_load_dwordx4 v238, s[44:47], s25 offen offset:0 lds\n"
"v_mfma_f32_16x16x32_bf16 v[52:55], a[224:227], v[212:215], v[52:55]\n"
"v_exp_f32 v195, v195\n"
"v_mfma_f32_16x16x32_bf16 v[56:59], a[228:231], v[212:215], v[56:59]\n"
"v_cvt_pk_bf16_f32 v209, v194, v195\n"
"v_mfma_f32_16x16x32_bf16 v[60:63], a[232:235], v[212:215], v[60:63]\n"
"v_exp_f32 v196, v196\n"
"v_mfma_f32_16x16x32_bf16 a[240:243], a[252:255], v[212:215], a[240:243]\n"
"buffer_load_dwordx4 v239, s[44:47], s25 offen offset:1024 lds\n"
"v_mfma_f32_16x16x32_bf16 v[200:203], a[172:175], v[176:179], 0\n"
"s_add_u32 m0, s30, 24576\n"
"v_exp_f32 v197, v197\n"
"v_mfma_f32_16x16x32_bf16 v[200:203], a[176:179], v[180:183], v[200:203]\n"
"ds_read_b128 a[172:175], v224 offset:32768\n"
"v_cvt_pk_bf16_f32 v210, v196, v197\n"
"v_mfma_f32_16x16x32_bf16 v[200:203], a[180:183], v[184:187], v[200:203]\n"
"v_exp_f32 v198, v198\n"
"v_mfma_f32_16x16x32_bf16 v[200:203], a[184:187], v[188:191], v[200:203]\n"
"ds_read_b128 a[176:179], v225 offset:32768\n"
"ds_read_b128 a[180:183], v226 offset:32768\n"
"v_mfma_f32_16x16x32_bf16 v[204:207], a[188:191], v[176:179], 0\n"
"buffer_load_dwordx4 v240, s[48:51], s25 offen offset:0 lds\n"
"v_exp_f32 v199, v199\n"
"v_mfma_f32_16x16x32_bf16 v[204:207], a[192:195], v[180:183], v[204:207]\n"
"ds_read_b128 a[184:187], v227 offset:32768\n"
"v_cvt_pk_bf16_f32 v211, v198, v199\n"
"v_mfma_f32_16x16x32_bf16 v[204:207], a[196:199], v[184:187], v[204:207]\n"
"ds_read_b128 a[188:191], v224 offset:36864\n"
"ds_read_b128 a[192:195], v225 offset:36864\n"
"v_mfma_f32_16x16x32_bf16 v[204:207], a[200:203], v[188:191], v[204:207]\n"
"v_exp_f32 v200, v200\n"
"v_mfma_f32_16x16x32_bf16 v[64:67], a[204:207], v[208:211], v[64:67]\n"
"ds_read_b128 a[196:199], v226 offset:36864\n"
"ds_read_b128 a[200:203], v227 offset:36864\n"
"buffer_load_dwordx4 v241, s[48:51], s25 offen offset:1024 lds\n"
"v_mfma_f32_16x16x32_bf16 v[68:71], a[208:211], v[208:211], v[68:71]\n"
"v_exp_f32 v201, v201\n"
"v_mfma_f32_16x16x32_bf16 v[72:75], a[212:215], v[208:211], v[72:75]\n"
"s_add_u32 m0, s90, 81920\n"
"s_xor_b32 s24, s89, 360448\n"
"v_cvt_pk_bf16_f32 v212, v200, v201\n"
"v_mfma_f32_16x16x32_bf16 v[76:79], a[216:219], v[208:211], v[76:79]\n"
"v_exp_f32 v202, v202\n"
"v_mfma_f32_16x16x32_bf16 v[80:83], a[220:223], v[208:211], v[80:83]\n"
"buffer_load_dwordx4 v230, s[52:55], s24 offen offset:0 lds\n"
"v_mfma_f32_16x16x32_bf16 v[84:87], a[224:227], v[208:211], v[84:87]\n"
"v_exp_f32 v203, v203\n"
"v_mfma_f32_16x16x32_bf16 v[88:91], a[228:231], v[208:211], v[88:91]\n"
"v_cvt_pk_bf16_f32 v213, v202, v203\n"
"v_mfma_f32_16x16x32_bf16 v[92:95], a[232:235], v[208:211], v[92:95]\n"
"v_exp_f32 v204, v204\n"
"v_mfma_f32_16x16x32_bf16 a[244:247], a[252:255], v[208:211], a[244:247]\n"
"s_waitcnt lgkmcnt(6)\n"
"buffer_load_dwordx4 v231, s[52:55], s24 offen offset:1024 lds\n"
"v_mfma_f32_16x16x32_bf16 v[192:195], a[172:175], v[128:131], 0\n"
"v_exp_f32 v205, v205\n"
"v_mfma_f32_16x16x32_bf16 v[192:195], a[176:179], v[132:135], v[192:195]\n"
"s_waitcnt lgkmcnt(4)\n"
"v_cvt_pk_bf16_f32 v214, v204, v205\n"
"v_mfma_f32_16x16x32_bf16 v[192:195], a[180:183], v[136:139], v[192:195]\n"
"v_exp_f32 v206, v206\n"
"v_mfma_f32_16x16x32_bf16 v[192:195], a[184:187], v[140:143], v[192:195]\n"
"s_waitcnt lgkmcnt(2)\n"
"v_mfma_f32_16x16x32_bf16 v[196:199], a[188:191], v[128:131], 0\n"
"v_exp_f32 v207, v207\n"
"v_mfma_f32_16x16x32_bf16 v[196:199], a[192:195], v[132:135], v[196:199]\n"
"s_waitcnt lgkmcnt(0)\n"
"v_cvt_pk_bf16_f32 v215, v206, v207\n"
"v_mfma_f32_16x16x32_bf16 v[196:199], a[196:199], v[136:139], v[196:199]\n"
"buffer_load_dwordx4 v232, s[52:55], s24 offen offset:2048 lds\n"
"v_mfma_f32_16x16x32_bf16 v[196:199], a[200:203], v[140:143], v[196:199]\n"
"v_exp_f32 v192, v192\n"
"v_mfma_f32_16x16x32_bf16 v[96:99], a[204:207], v[212:215], v[96:99]\n"
"ds_read_b64_tr_b16 a[204:205], v216 offset:40960\n"
"ds_read_b64_tr_b16 a[206:207], v216 offset:45056\n"
"v_mfma_f32_16x16x32_bf16 v[100:103], a[208:211], v[212:215], v[100:103]\n"
"v_exp_f32 v193, v193\n"
"v_mfma_f32_16x16x32_bf16 v[104:107], a[212:215], v[212:215], v[104:107]\n"
"ds_read_b64_tr_b16 a[208:209], v217 offset:40960\n"
"buffer_load_dwordx4 v233, s[52:55], s24 offen offset:3072 lds\n"
"v_cvt_pk_bf16_f32 v208, v192, v193\n"
"v_mfma_f32_16x16x32_bf16 v[108:111], a[216:219], v[212:215], v[108:111]\n"
"v_exp_f32 v194, v194\n"
"v_mfma_f32_16x16x32_bf16 v[112:115], a[220:223], v[212:215], v[112:115]\n"
"ds_read_b64_tr_b16 a[210:211], v217 offset:45056\n"
"ds_read_b64_tr_b16 a[212:213], v218 offset:40960\n"
"v_mfma_f32_16x16x32_bf16 v[116:119], a[224:227], v[212:215], v[116:119]\n"
"s_add_u32 m0, s90, 98304\n"
"v_exp_f32 v195, v195\n"
"v_mfma_f32_16x16x32_bf16 v[120:123], a[228:231], v[212:215], v[120:123]\n"
"ds_read_b64_tr_b16 a[214:215], v218 offset:45056\n"
"v_cvt_pk_bf16_f32 v209, v194, v195\n"
"v_mfma_f32_16x16x32_bf16 v[124:127], a[232:235], v[212:215], v[124:127]\n"
"s_xor_b32 s24, s89, 393216\n"
"v_exp_f32 v196, v196\n"
"v_mfma_f32_16x16x32_bf16 a[248:251], a[252:255], v[212:215], a[248:251]\n"
"ds_read_b64_tr_b16 a[216:217], v219 offset:40960\n"
"ds_read_b64_tr_b16 a[218:219], v219 offset:45056\n"
"v_mfma_f32_16x16x32_bf16 v[200:203], a[172:175], v[144:147], 0\n"
"buffer_load_dwordx4 v230, s[52:55], s24 offen offset:0 lds\n"
"v_exp_f32 v197, v197\n"
"v_mfma_f32_16x16x32_bf16 v[200:203], a[176:179], v[148:151], v[200:203]\n"
"ds_read_b64_tr_b16 a[220:221], v220 offset:40960\n"
"v_cvt_pk_bf16_f32 v210, v196, v197\n"
"v_mfma_f32_16x16x32_bf16 v[200:203], a[180:183], v[152:155], v[200:203]\n"
"v_exp_f32 v198, v198\n"
"v_mfma_f32_16x16x32_bf16 v[200:203], a[184:187], v[156:159], v[200:203]\n"
"ds_read_b64_tr_b16 a[222:223], v220 offset:45056\n"
"ds_read_b64_tr_b16 a[224:225], v221 offset:40960\n"
"v_mfma_f32_16x16x32_bf16 v[204:207], a[188:191], v[144:147], 0\n"
"v_exp_f32 v199, v199\n"
"v_mfma_f32_16x16x32_bf16 v[204:207], a[192:195], v[148:151], v[204:207]\n"
"ds_read_b64_tr_b16 a[226:227], v221 offset:45056\n"
"v_cvt_pk_bf16_f32 v211, v198, v199\n"
"v_mfma_f32_16x16x32_bf16 v[204:207], a[196:199], v[152:155], v[204:207]\n"
"ds_read_b64_tr_b16 a[228:229], v222 offset:40960\n"
"ds_read_b64_tr_b16 a[230:231], v222 offset:45056\n"
"v_mfma_f32_16x16x32_bf16 v[204:207], a[200:203], v[156:159], v[204:207]\n"
"s_waitcnt lgkmcnt(10)\n"
"v_exp_f32 v200, v200\n"
"v_mfma_f32_16x16x32_bf16 v[0:3], a[204:207], v[208:211], v[0:3]\n"
"ds_read_b64_tr_b16 a[232:233], v223 offset:40960\n"
"ds_read_b64_tr_b16 a[234:235], v223 offset:45056\n"
"v_mfma_f32_16x16x32_bf16 v[4:7], a[208:211], v[208:211], v[4:7]\n"
"s_waitcnt lgkmcnt(8)\n"
"v_exp_f32 v201, v201\n"
"v_mfma_f32_16x16x32_bf16 v[8:11], a[212:215], v[208:211], v[8:11]\n"
"v_cvt_pk_bf16_f32 v212, v200, v201\n"
"v_mfma_f32_16x16x32_bf16 v[12:15], a[216:219], v[208:211], v[12:15]\n"
"s_waitcnt lgkmcnt(4)\n"
"v_exp_f32 v202, v202\n"
"v_mfma_f32_16x16x32_bf16 v[16:19], a[220:223], v[208:211], v[16:19]\n"
"buffer_load_dwordx4 v231, s[52:55], s24 offen offset:1024 lds\n"
"v_mfma_f32_16x16x32_bf16 v[20:23], a[224:227], v[208:211], v[20:23]\n"
"s_waitcnt lgkmcnt(0)\n"
"v_exp_f32 v203, v203\n"
"v_mfma_f32_16x16x32_bf16 v[24:27], a[228:231], v[208:211], v[24:27]\n"
"v_cvt_pk_bf16_f32 v213, v202, v203\n"
"v_mfma_f32_16x16x32_bf16 v[28:31], a[232:235], v[208:211], v[28:31]\n"
"v_exp_f32 v204, v204\n"
"v_mfma_f32_16x16x32_bf16 a[236:239], a[252:255], v[208:211], a[236:239]\n"
"buffer_load_dwordx4 v232, s[52:55], s24 offen offset:2048 lds\n"
"v_mfma_f32_16x16x32_bf16 v[192:195], a[172:175], v[160:163], 0\n"
"v_exp_f32 v205, v205\n"
"v_mfma_f32_16x16x32_bf16 v[192:195], a[176:179], v[164:167], v[192:195]\n"
"v_cvt_pk_bf16_f32 v214, v204, v205\n"
"v_mfma_f32_16x16x32_bf16 v[192:195], a[180:183], v[168:171], v[192:195]\n"
"v_exp_f32 v206, v206\n"
"v_mfma_f32_16x16x32_bf16 v[192:195], a[184:187], v[172:175], v[192:195]\n"
"buffer_load_dwordx4 v233, s[52:55], s24 offen offset:3072 lds\n"
"v_mfma_f32_16x16x32_bf16 v[196:199], a[188:191], v[160:163], 0\n"
"v_exp_f32 v207, v207\n"
"v_mfma_f32_16x16x32_bf16 v[196:199], a[192:195], v[164:167], v[196:199]\n"
"s_waitcnt vmcnt(29)\n"
"v_cvt_pk_bf16_f32 v215, v206, v207\n"
"v_mfma_f32_16x16x32_bf16 v[196:199], a[196:199], v[168:171], v[196:199]\n"
"s_barrier\n"
"v_mfma_f32_16x16x32_bf16 v[196:199], a[200:203], v[172:175], v[196:199]\n"
"s_add_u32 m0, s30, 32768\n"
"v_exp_f32 v192, v192\n"
"v_mfma_f32_16x16x32_bf16 v[32:35], a[204:207], v[212:215], v[32:35]\n"
"s_xor_b32 s25, s19, 114688\n"
"s_waitcnt vmcnt(25)\n"
"ds_read_b64_tr_b16 a[128:129], v234 offset:49152\n"
"v_mfma_f32_16x16x32_bf16 v[36:39], a[208:211], v[212:215], v[36:39]\n"
"ds_read_b64_tr_b16 a[130:131], v234 offset:51200\n"
"v_exp_f32 v193, v193\n"
"v_mfma_f32_16x16x32_bf16 v[40:43], a[212:215], v[212:215], v[40:43]\n"
"ds_read_b64_tr_b16 a[132:133], v235 offset:49152\n"
"v_cvt_pk_bf16_f32 v208, v192, v193\n"
"v_mfma_f32_16x16x32_bf16 v[44:47], a[216:219], v[212:215], v[44:47]\n"
"ds_read_b64_tr_b16 a[134:135], v235 offset:51200\n"
"v_exp_f32 v194, v194\n"
"v_mfma_f32_16x16x32_bf16 v[48:51], a[220:223], v[212:215], v[48:51]\n"
"buffer_load_dwordx4 v238, s[44:47], s25 offen offset:0 lds\n"
"v_mfma_f32_16x16x32_bf16 v[52:55], a[224:227], v[212:215], v[52:55]\n"
"v_exp_f32 v195, v195\n"
"v_mfma_f32_16x16x32_bf16 v[56:59], a[228:231], v[212:215], v[56:59]\n"
"ds_read_b64_tr_b16 a[136:137], v236 offset:49152\n"
"v_cvt_pk_bf16_f32 v209, v194, v195\n"
"v_mfma_f32_16x16x32_bf16 v[60:63], a[232:235], v[212:215], v[60:63]\n"
"ds_read_b64_tr_b16 a[138:139], v236 offset:51200\n"
"v_exp_f32 v196, v196\n"
"v_mfma_f32_16x16x32_bf16 a[240:243], a[252:255], v[212:215], a[240:243]\n"
"buffer_load_dwordx4 v239, s[44:47], s25 offen offset:1024 lds\n"
"v_mfma_f32_16x16x32_bf16 v[200:203], a[172:175], v[176:179], 0\n"
"s_add_u32 m0, s30, 40960\n"
"v_exp_f32 v197, v197\n"
"v_mfma_f32_16x16x32_bf16 v[200:203], a[176:179], v[180:183], v[200:203]\n"
"ds_read_b128 a[172:175], v224 offset:49152\n"
"v_cvt_pk_bf16_f32 v210, v196, v197\n"
"v_mfma_f32_16x16x32_bf16 v[200:203], a[180:183], v[184:187], v[200:203]\n"
"v_exp_f32 v198, v198\n"
"v_mfma_f32_16x16x32_bf16 v[200:203], a[184:187], v[188:191], v[200:203]\n"
"ds_read_b128 a[176:179], v225 offset:49152\n"
"ds_read_b128 a[180:183], v226 offset:49152\n"
"v_mfma_f32_16x16x32_bf16 v[204:207], a[188:191], v[176:179], 0\n"
"buffer_load_dwordx4 v240, s[48:51], s25 offen offset:0 lds\n"
"v_exp_f32 v199, v199\n"
"v_mfma_f32_16x16x32_bf16 v[204:207], a[192:195], v[180:183], v[204:207]\n"
"ds_read_b128 a[184:187], v227 offset:49152\n"
"v_cvt_pk_bf16_f32 v211, v198, v199\n"
"v_mfma_f32_16x16x32_bf16 v[204:207], a[196:199], v[184:187], v[204:207]\n"
"ds_read_b128 a[188:191], v224 offset:53248\n"
"ds_read_b128 a[192:195], v225 offset:53248\n"
"v_mfma_f32_16x16x32_bf16 v[204:207], a[200:203], v[188:191], v[204:207]\n"
"v_exp_f32 v200, v200\n"
"v_mfma_f32_16x16x32_bf16 v[64:67], a[204:207], v[208:211], v[64:67]\n"
"ds_read_b128 a[196:199], v226 offset:53248\n"
"ds_read_b128 a[200:203], v227 offset:53248\n"
"buffer_load_dwordx4 v241, s[48:51], s25 offen offset:1024 lds\n"
"v_mfma_f32_16x16x32_bf16 v[68:71], a[208:211], v[208:211], v[68:71]\n"
"s_mov_b32 m0, s1\n"
"v_exp_f32 v201, v201\n"
"v_mfma_f32_16x16x32_bf16 v[72:75], a[212:215], v[208:211], v[72:75]\n"
"ds_read_b64_tr_b16 a[140:141], v237 offset:49152\n"
"v_cvt_pk_bf16_f32 v212, v200, v201\n"
"v_mfma_f32_16x16x32_bf16 v[76:79], a[216:219], v[208:211], v[76:79]\n"
"ds_read_b64_tr_b16 a[142:143], v237 offset:51200\n"
"s_waitcnt vmcnt(25)\n"
"v_exp_f32 v202, v202\n"
"v_mfma_f32_16x16x32_bf16 v[80:83], a[220:223], v[208:211], v[80:83]\n"
"buffer_load_dwordx4 v248, s[72:75], s7 offen offset:0 lds\n"
"v_mfma_f32_16x16x32_bf16 v[84:87], a[224:227], v[208:211], v[84:87]\n"
"v_exp_f32 v203, v203\n"
"v_mfma_f32_16x16x32_bf16 v[88:91], a[228:231], v[208:211], v[88:91]\n"
"ds_read_b64_tr_b16 a[144:145], v244 offset:0\n"
"v_cvt_pk_bf16_f32 v213, v202, v203\n"
"v_mfma_f32_16x16x32_bf16 v[92:95], a[232:235], v[208:211], v[92:95]\n"
"ds_read_b64_tr_b16 a[146:147], v244 offset:2048\n"
"v_exp_f32 v204, v204\n"
"v_mfma_f32_16x16x32_bf16 a[244:247], a[252:255], v[208:211], a[244:247]\n"
"s_waitcnt lgkmcnt(10)\n"
"buffer_load_dwordx4 v249, s[72:75], s7 offen offset:1024 lds\n"
"ds_read_b64_tr_b16 a[148:149], v245 offset:0\n"
"v_mfma_f32_16x16x32_bf16 v[192:195], a[172:175], v[128:131], 0\n"
"s_waitcnt lgkmcnt(3)\n"
"v_exp_f32 v205, v205\n"
"v_mfma_f32_16x16x32_bf16 v[192:195], a[176:179], v[132:135], v[192:195]\n"
"s_add_u32 m0, s90, 114688\n"
"v_cvt_pk_bf16_f32 v214, v204, v205\n"
"v_mfma_f32_16x16x32_bf16 v[192:195], a[180:183], v[136:139], v[192:195]\n"
"s_xor_b32 s24, s89, 425984\n"
"v_exp_f32 v206, v206\n"
"v_mfma_f32_16x16x32_bf16 v[192:195], a[184:187], v[140:143], v[192:195]\n"
"ds_read_b64_tr_b16 a[150:151], v245 offset:2048\n"
"ds_read_b64_tr_b16 a[152:153], v246 offset:0\n"
"v_mfma_f32_16x16x32_bf16 v[196:199], a[188:191], v[128:131], 0\n"
"ds_read_b64_tr_b16 a[154:155], v246 offset:2048\n"
"v_exp_f32 v207, v207\n"
"v_mfma_f32_16x16x32_bf16 v[196:199], a[192:195], v[132:135], v[196:199]\n"
"buffer_load_dwordx4 v230, s[52:55], s24 offen offset:0 lds\n"
"v_cvt_pk_bf16_f32 v215, v206, v207\n"
"v_mfma_f32_16x16x32_bf16 v[196:199], a[196:199], v[136:139], v[196:199]\n"
"ds_read_b64_tr_b16 a[156:157], v247 offset:0\n"
"ds_read_b64_tr_b16 a[158:159], v247 offset:2048\n"
"v_mfma_f32_16x16x32_bf16 v[196:199], a[200:203], v[140:143], v[196:199]\n"
"v_exp_f32 v192, v192\n"
"v_mfma_f32_16x16x32_bf16 v[96:99], a[204:207], v[212:215], v[96:99]\n"
"ds_read_b64_tr_b16 a[204:205], v216 offset:57344\n"
"ds_read_b64_tr_b16 a[206:207], v216 offset:61440\n"
"v_mfma_f32_16x16x32_bf16 v[100:103], a[208:211], v[212:215], v[100:103]\n"
"buffer_load_dwordx4 v231, s[52:55], s24 offen offset:1024 lds\n"
"v_exp_f32 v193, v193\n"
"v_mfma_f32_16x16x32_bf16 v[104:107], a[212:215], v[212:215], v[104:107]\n"
"ds_read_b64_tr_b16 a[208:209], v217 offset:57344\n"
"v_cvt_pk_bf16_f32 v208, v192, v193\n"
"v_mfma_f32_16x16x32_bf16 v[108:111], a[216:219], v[212:215], v[108:111]\n"
"v_exp_f32 v194, v194\n"
"v_mfma_f32_16x16x32_bf16 v[112:115], a[220:223], v[212:215], v[112:115]\n"
"ds_read_b64_tr_b16 a[210:211], v217 offset:61440\n"
"ds_read_b64_tr_b16 a[212:213], v218 offset:57344\n"
"v_mfma_f32_16x16x32_bf16 v[116:119], a[224:227], v[212:215], v[116:119]\n"
"buffer_load_dwordx4 v232, s[52:55], s24 offen offset:2048 lds\n"
"v_exp_f32 v195, v195\n"
"v_mfma_f32_16x16x32_bf16 v[120:123], a[228:231], v[212:215], v[120:123]\n"
"ds_read_b64_tr_b16 a[214:215], v218 offset:61440\n"
"v_cvt_pk_bf16_f32 v209, v194, v195\n"
"v_mfma_f32_16x16x32_bf16 v[124:127], a[232:235], v[212:215], v[124:127]\n"
"v_exp_f32 v196, v196\n"
"v_mfma_f32_16x16x32_bf16 a[248:251], a[252:255], v[212:215], a[248:251]\n"
"ds_read_b64_tr_b16 a[216:217], v219 offset:57344\n"
"ds_read_b64_tr_b16 a[218:219], v219 offset:61440\n"
"v_mfma_f32_16x16x32_bf16 v[200:203], a[172:175], v[144:147], 0\n"
"buffer_load_dwordx4 v233, s[52:55], s24 offen offset:3072 lds\n"
"v_exp_f32 v197, v197\n"
"v_mfma_f32_16x16x32_bf16 v[200:203], a[176:179], v[148:151], v[200:203]\n"
"ds_read_b64_tr_b16 a[220:221], v220 offset:57344\n"
"v_cvt_pk_bf16_f32 v210, v196, v197\n"
"v_mfma_f32_16x16x32_bf16 v[200:203], a[180:183], v[152:155], v[200:203]\n"
"s_waitcnt lgkmcnt(9)\n"
"s_add_u32 m0, s90, 131072\n"
"v_exp_f32 v198, v198\n"
"v_mfma_f32_16x16x32_bf16 v[200:203], a[184:187], v[156:159], v[200:203]\n"
"ds_read_b64_tr_b16 a[222:223], v220 offset:61440\n"
"ds_read_b64_tr_b16 a[224:225], v221 offset:57344\n"
"v_mfma_f32_16x16x32_bf16 v[204:207], a[188:191], v[144:147], 0\n"
"s_xor_b32 s24, s89, 458752\n"
"buffer_load_dwordx4 v230, s[52:55], s24 offen offset:0 lds\n"
"v_exp_f32 v199, v199\n"
"v_mfma_f32_16x16x32_bf16 v[204:207], a[192:195], v[148:151], v[204:207]\n"
"ds_read_b64_tr_b16 a[226:227], v221 offset:61440\n"
"v_cvt_pk_bf16_f32 v211, v198, v199\n"
"v_mfma_f32_16x16x32_bf16 v[204:207], a[196:199], v[152:155], v[204:207]\n"
"ds_read_b64_tr_b16 a[228:229], v222 offset:57344\n"
"ds_read_b64_tr_b16 a[230:231], v222 offset:61440\n"
"v_mfma_f32_16x16x32_bf16 v[204:207], a[200:203], v[156:159], v[204:207]\n"
"s_waitcnt lgkmcnt(10)\n"
"v_exp_f32 v200, v200\n"
"v_mfma_f32_16x16x32_bf16 v[0:3], a[204:207], v[208:211], v[0:3]\n"
"ds_read_b64_tr_b16 a[232:233], v223 offset:57344\n"
"ds_read_b64_tr_b16 a[234:235], v223 offset:61440\n"
"v_mfma_f32_16x16x32_bf16 v[4:7], a[208:211], v[208:211], v[4:7]\n"
"s_waitcnt lgkmcnt(8)\n"
"v_exp_f32 v201, v201\n"
"v_mfma_f32_16x16x32_bf16 v[8:11], a[212:215], v[208:211], v[8:11]\n"
"v_cvt_pk_bf16_f32 v212, v200, v201\n"
"v_mfma_f32_16x16x32_bf16 v[12:15], a[216:219], v[208:211], v[12:15]\n"
"s_waitcnt lgkmcnt(4)\n"
"v_exp_f32 v202, v202\n"
"v_mfma_f32_16x16x32_bf16 v[16:19], a[220:223], v[208:211], v[16:19]\n"
"buffer_load_dwordx4 v231, s[52:55], s24 offen offset:1024 lds\n"
"v_mfma_f32_16x16x32_bf16 v[20:23], a[224:227], v[208:211], v[20:23]\n"
"s_waitcnt lgkmcnt(0)\n"
"v_exp_f32 v203, v203\n"
"v_mfma_f32_16x16x32_bf16 v[24:27], a[228:231], v[208:211], v[24:27]\n"
"v_cvt_pk_bf16_f32 v213, v202, v203\n"
"L_ic6:\n"
"s_cbranch_execz L_ic10\n"
"v_mfma_f32_16x16x32_bf16 v[28:31], a[232:235], v[208:211], v[28:31]\n"
"v_exp_f32 v204, v204\n"
"v_mfma_f32_16x16x32_bf16 a[236:239], a[252:255], v[208:211], a[236:239]\n"
"buffer_load_dwordx4 v232, s[52:55], s24 offen offset:2048 lds\n"
"v_mfma_f32_16x16x32_bf16 v[192:195], a[172:175], v[160:163], 0\n"
"v_exp_f32 v205, v205\n"
"v_mfma_f32_16x16x32_bf16 v[192:195], a[176:179], v[164:167], v[192:195]\n"
"v_cvt_pk_bf16_f32 v214, v204, v205\n"
"v_mfma_f32_16x16x32_bf16 v[192:195], a[180:183], v[168:171], v[192:195]\n"
"v_exp_f32 v206, v206\n"
"v_mfma_f32_16x16x32_bf16 v[192:195], a[184:187], v[172:175], v[192:195]\n"
"buffer_load_dwordx4 v233, s[52:55], s24 offen offset:3072 lds\n"
"v_mfma_f32_16x16x32_bf16 v[196:199], a[188:191], v[160:163], 0\n"
"v_exp_f32 v207, v207\n"
"v_mfma_f32_16x16x32_bf16 v[196:199], a[192:195], v[164:167], v[196:199]\n"
"s_waitcnt vmcnt(31)\n"
"v_cvt_pk_bf16_f32 v215, v206, v207\n"
"v_mfma_f32_16x16x32_bf16 v[196:199], a[196:199], v[168:171], v[196:199]\n"
"s_barrier\n"
"v_mfma_f32_16x16x32_bf16 v[196:199], a[200:203], v[172:175], v[196:199]\n"
"s_add_u32 m0, s30, 49152\n"
"v_exp_f32 v192, v192\n"
"v_mfma_f32_16x16x32_bf16 v[32:35], a[204:207], v[212:215], v[32:35]\n"
"s_xor_b32 s25, s19, 122880\n"
"v_mfma_f32_16x16x32_bf16 v[36:39], a[208:211], v[212:215], v[36:39]\n"
"v_exp_f32 v193, v193\n"
"v_mfma_f32_16x16x32_bf16 v[40:43], a[212:215], v[212:215], v[40:43]\n"
"v_cvt_pk_bf16_f32 v208, v192, v193\n"
"v_mfma_f32_16x16x32_bf16 v[44:47], a[216:219], v[212:215], v[44:47]\n"
"v_exp_f32 v194, v194\n"
"v_mfma_f32_16x16x32_bf16 v[48:51], a[220:223], v[212:215], v[48:51]\n"
"buffer_load_dwordx4 v238, s[44:47], s25 offen offset:0 lds\n"
"v_mfma_f32_16x16x32_bf16 v[52:55], a[224:227], v[212:215], v[52:55]\n"
"v_exp_f32 v195, v195\n"
"v_mfma_f32_16x16x32_bf16 v[56:59], a[228:231], v[212:215], v[56:59]\n"
"v_cvt_pk_bf16_f32 v209, v194, v195\n"
"v_mfma_f32_16x16x32_bf16 v[60:63], a[232:235], v[212:215], v[60:63]\n"
"v_exp_f32 v196, v196\n"
"v_mfma_f32_16x16x32_bf16 a[240:243], a[252:255], v[212:215], a[240:243]\n"
"buffer_load_dwordx4 v239, s[44:47], s25 offen offset:1024 lds\n"
"v_mfma_f32_16x16x32_bf16 v[200:203], a[172:175], v[176:179], 0\n"
"s_add_u32 m0, s30, 57344\n"
"v_exp_f32 v197, v197\n"
"v_mfma_f32_16x16x32_bf16 v[200:203], a[176:179], v[180:183], v[200:203]\n"
"ds_read_b128 a[172:175], v224 offset:0\n"
"v_cvt_pk_bf16_f32 v210, v196, v197\n"
"v_mfma_f32_16x16x32_bf16 v[200:203], a[180:183], v[184:187], v[200:203]\n"
"v_exp_f32 v198, v198\n"
"v_mfma_f32_16x16x32_bf16 v[200:203], a[184:187], v[188:191], v[200:203]\n"
"ds_read_b128 a[176:179], v225 offset:0\n"
"ds_read_b128 a[180:183], v226 offset:0\n"
"v_mfma_f32_16x16x32_bf16 v[204:207], a[188:191], v[176:179], 0\n"
"buffer_load_dwordx4 v240, s[48:51], s25 offen offset:0 lds\n"
"v_exp_f32 v199, v199\n"
"v_mfma_f32_16x16x32_bf16 v[204:207], a[192:195], v[180:183], v[204:207]\n"
"ds_read_b128 a[184:187], v227 offset:0\n"
"v_cvt_pk_bf16_f32 v211, v198, v199\n"
"v_mfma_f32_16x16x32_bf16 v[204:207], a[196:199], v[184:187], v[204:207]\n"
"ds_read_b128 a[188:191], v224 offset:4096\n"
"ds_read_b128 a[192:195], v225 offset:4096\n"
"v_mfma_f32_16x16x32_bf16 v[204:207], a[200:203], v[188:191], v[204:207]\n"
"v_exp_f32 v200, v200\n"
"v_mfma_f32_16x16x32_bf16 v[64:67], a[204:207], v[208:211], v[64:67]\n"
"ds_read_b128 a[196:199], v226 offset:4096\n"
"ds_read_b128 a[200:203], v227 offset:4096\n"
"buffer_load_dwordx4 v241, s[48:51], s25 offen offset:1024 lds\n"
"v_mfma_f32_16x16x32_bf16 v[68:71], a[208:211], v[208:211], v[68:71]\n"
"v_exp_f32 v201, v201\n"
"v_mfma_f32_16x16x32_bf16 v[72:75], a[212:215], v[208:211], v[72:75]\n"
"v_cvt_pk_bf16_f32 v212, v200, v201\n"
"v_mfma_f32_16x16x32_bf16 v[76:79], a[216:219], v[208:211], v[76:79]\n"
"v_exp_f32 v202, v202\n"
"v_mfma_f32_16x16x32_bf16 v[80:83], a[220:223], v[208:211], v[80:83]\n"
"v_mfma_f32_16x16x32_bf16 v[84:87], a[224:227], v[208:211], v[84:87]\n"
"v_exp_f32 v203, v203\n"
"v_mfma_f32_16x16x32_bf16 v[88:91], a[228:231], v[208:211], v[88:91]\n"
"v_cvt_pk_bf16_f32 v213, v202, v203\n"
"v_mfma_f32_16x16x32_bf16 v[92:95], a[232:235], v[208:211], v[92:95]\n"
"v_exp_f32 v204, v204\n"
"v_mfma_f32_16x16x32_bf16 a[244:247], a[252:255], v[208:211], a[244:247]\n"
"s_waitcnt lgkmcnt(6)\n"
"v_mfma_f32_16x16x32_bf16 v[192:195], a[172:175], v[128:131], 0\n"
"v_exp_f32 v205, v205\n"
"v_mfma_f32_16x16x32_bf16 v[192:195], a[176:179], v[132:135], v[192:195]\n"
"s_waitcnt lgkmcnt(4)\n"
"v_cvt_pk_bf16_f32 v214, v204, v205\n"
"v_mfma_f32_16x16x32_bf16 v[192:195], a[180:183], v[136:139], v[192:195]\n"
"v_exp_f32 v206, v206\n"
"v_mfma_f32_16x16x32_bf16 v[192:195], a[184:187], v[140:143], v[192:195]\n"
"s_waitcnt lgkmcnt(2)\n"
"v_mfma_f32_16x16x32_bf16 v[196:199], a[188:191], v[128:131], 0\n"
"v_exp_f32 v207, v207\n"
"v_mfma_f32_16x16x32_bf16 v[196:199], a[192:195], v[132:135], v[196:199]\n"
"s_waitcnt vmcnt(26)\n"
"s_waitcnt lgkmcnt(0)\n"
"v_cvt_pk_bf16_f32 v215, v206, v207\n"
"v_mfma_f32_16x16x32_bf16 v[196:199], a[196:199], v[136:139], v[196:199]\n"
"s_barrier\n"
"v_mfma_f32_16x16x32_bf16 v[196:199], a[200:203], v[140:143], v[196:199]\n"
"v_exp_f32 v192, v192\n"
"v_mfma_f32_16x16x32_bf16 v[96:99], a[204:207], v[212:215], v[96:99]\n"
"ds_read_b64_tr_b16 a[204:205], v216 offset:8192\n"
"ds_read_b64_tr_b16 a[206:207], v216 offset:12288\n"
"v_mfma_f32_16x16x32_bf16 v[100:103], a[208:211], v[212:215], v[100:103]\n"
"v_exp_f32 v193, v193\n"
"v_mfma_f32_16x16x32_bf16 v[104:107], a[212:215], v[212:215], v[104:107]\n"
"ds_read_b64_tr_b16 a[208:209], v217 offset:8192\n"
"v_cvt_pk_bf16_f32 v208, v192, v193\n"
"v_mfma_f32_16x16x32_bf16 v[108:111], a[216:219], v[212:215], v[108:111]\n"
"v_exp_f32 v194, v194\n"
"v_mfma_f32_16x16x32_bf16 v[112:115], a[220:223], v[212:215], v[112:115]\n"
"ds_read_b64_tr_b16 a[210:211], v217 offset:12288\n"
"ds_read_b64_tr_b16 a[212:213], v218 offset:8192\n"
"v_mfma_f32_16x16x32_bf16 v[116:119], a[224:227], v[212:215], v[116:119]\n"
"v_exp_f32 v195, v195\n"
"v_mfma_f32_16x16x32_bf16 v[120:123], a[228:231], v[212:215], v[120:123]\n"
"ds_read_b64_tr_b16 a[214:215], v218 offset:12288\n"
"v_cvt_pk_bf16_f32 v209, v194, v195\n"
"v_mfma_f32_16x16x32_bf16 v[124:127], a[232:235], v[212:215], v[124:127]\n"
"v_exp_f32 v196, v196\n"
"v_mfma_f32_16x16x32_bf16 a[248:251], a[252:255], v[212:215], a[248:251]\n"
"ds_read_b64_tr_b16 a[216:217], v219 offset:8192\n"
"ds_read_b64_tr_b16 a[218:219], v219 offset:12288\n"
"v_mfma_f32_16x16x32_bf16 v[200:203], a[172:175], v[144:147], 0\n"
"v_exp_f32 v197, v197\n"
"v_mfma_f32_16x16x32_bf16 v[200:203], a[176:179], v[148:151], v[200:203]\n"
"ds_read_b64_tr_b16 a[220:221], v220 offset:8192\n"
"v_cvt_pk_bf16_f32 v210, v196, v197\n"
"v_mfma_f32_16x16x32_bf16 v[200:203], a[180:183], v[152:155], v[200:203]\n"
"v_exp_f32 v198, v198\n"
"v_mfma_f32_16x16x32_bf16 v[200:203], a[184:187], v[156:159], v[200:203]\n"
"ds_read_b64_tr_b16 a[222:223], v220 offset:12288\n"
"ds_read_b64_tr_b16 a[224:225], v221 offset:8192\n"
"v_mfma_f32_16x16x32_bf16 v[204:207], a[188:191], v[144:147], 0\n"
"v_exp_f32 v199, v199\n"
"v_mfma_f32_16x16x32_bf16 v[204:207], a[192:195], v[148:151], v[204:207]\n"
"ds_read_b64_tr_b16 a[226:227], v221 offset:12288\n"
"v_cvt_pk_bf16_f32 v211, v198, v199\n"
"v_mfma_f32_16x16x32_bf16 v[204:207], a[196:199], v[152:155], v[204:207]\n"
"ds_read_b64_tr_b16 a[228:229], v222 offset:8192\n"
"ds_read_b64_tr_b16 a[230:231], v222 offset:12288\n"
"v_mfma_f32_16x16x32_bf16 v[204:207], a[200:203], v[156:159], v[204:207]\n"
"s_waitcnt lgkmcnt(10)\n"
"v_exp_f32 v200, v200\n"
"v_mfma_f32_16x16x32_bf16 v[0:3], a[204:207], v[208:211], v[0:3]\n"
"ds_read_b64_tr_b16 a[232:233], v223 offset:8192\n"
"ds_read_b64_tr_b16 a[234:235], v223 offset:12288\n"
"v_mfma_f32_16x16x32_bf16 v[4:7], a[208:211], v[208:211], v[4:7]\n"
"s_waitcnt lgkmcnt(8)\n"
"v_exp_f32 v201, v201\n"
"v_mfma_f32_16x16x32_bf16 v[8:11], a[212:215], v[208:211], v[8:11]\n"
"ds_read_b128 a[172:175], v224 offset:16384\n"
"v_cvt_pk_bf16_f32 v212, v200, v201\n"
"v_mfma_f32_16x16x32_bf16 v[12:15], a[216:219], v[208:211], v[12:15]\n"
"s_waitcnt lgkmcnt(5)\n"
"v_exp_f32 v202, v202\n"
"v_mfma_f32_16x16x32_bf16 v[16:19], a[220:223], v[208:211], v[16:19]\n"
"ds_read_b128 a[176:179], v225 offset:16384\n"
"ds_read_b128 a[180:183], v226 offset:16384\n"
"v_mfma_f32_16x16x32_bf16 v[20:23], a[224:227], v[208:211], v[20:23]\n"
"s_waitcnt lgkmcnt(3)\n"
"v_exp_f32 v203, v203\n"
"v_mfma_f32_16x16x32_bf16 v[24:27], a[228:231], v[208:211], v[24:27]\n"
"ds_read_b128 a[184:187], v227 offset:16384\n"
"v_cvt_pk_bf16_f32 v213, v202, v203\n"
"v_mfma_f32_16x16x32_bf16 v[28:31], a[232:235], v[208:211], v[28:31]\n"
"s_waitcnt lgkmcnt(2)\n"
"v_exp_f32 v204, v204\n"
"v_mfma_f32_16x16x32_bf16 a[236:239], a[252:255], v[208:211], a[236:239]\n"
"ds_read_b128 a[188:191], v224 offset:20480\n"
"ds_read_b128 a[192:195], v225 offset:20480\n"
"v_mfma_f32_16x16x32_bf16 v[192:195], a[172:175], v[128:131], 0\n"
"s_waitcnt lgkmcnt(2)\n"
"v_exp_f32 v205, v205\n"
"v_mfma_f32_16x16x32_bf16 v[192:195], a[176:179], v[132:135], v[192:195]\n"
"ds_read_b128 a[196:199], v226 offset:20480\n"
"v_cvt_pk_bf16_f32 v214, v204, v205\n"
"v_mfma_f32_16x16x32_bf16 v[192:195], a[180:183], v[136:139], v[192:195]\n"
"ds_read_b128 a[200:203], v227 offset:20480\n"
"v_exp_f32 v206, v206\n"
"v_mfma_f32_16x16x32_bf16 v[192:195], a[184:187], v[140:143], v[192:195]\n"
"s_waitcnt lgkmcnt(2)\n"
"v_mfma_f32_16x16x32_bf16 v[196:199], a[188:191], v[128:131], 0\n"
"v_exp_f32 v207, v207\n"
"v_mfma_f32_16x16x32_bf16 v[196:199], a[192:195], v[132:135], v[196:199]\n"
"s_waitcnt vmcnt(14)\n"
"s_waitcnt lgkmcnt(0)\n"
"v_cvt_pk_bf16_f32 v215, v206, v207\n"
"v_mfma_f32_16x16x32_bf16 v[196:199], a[196:199], v[136:139], v[196:199]\n"
"s_barrier\n"
"v_mfma_f32_16x16x32_bf16 v[196:199], a[200:203], v[140:143], v[196:199]\n"
"v_exp_f32 v192, v192\n"
"v_mfma_f32_16x16x32_bf16 v[32:35], a[204:207], v[212:215], v[32:35]\n"
"ds_read_b64_tr_b16 a[204:205], v216 offset:24576\n"
"ds_read_b64_tr_b16 a[206:207], v216 offset:28672\n"
"v_mfma_f32_16x16x32_bf16 v[36:39], a[208:211], v[212:215], v[36:39]\n"
"v_exp_f32 v193, v193\n"
"v_mfma_f32_16x16x32_bf16 v[40:43], a[212:215], v[212:215], v[40:43]\n"
"ds_read_b64_tr_b16 a[208:209], v217 offset:24576\n"
"v_cvt_pk_bf16_f32 v208, v192, v193\n"
"v_mfma_f32_16x16x32_bf16 v[44:47], a[216:219], v[212:215], v[44:47]\n"
"v_exp_f32 v194, v194\n"
"v_mfma_f32_16x16x32_bf16 v[48:51], a[220:223], v[212:215], v[48:51]\n"
"ds_read_b64_tr_b16 a[210:211], v217 offset:28672\n"
"ds_read_b64_tr_b16 a[212:213], v218 offset:24576\n"
"v_mfma_f32_16x16x32_bf16 v[52:55], a[224:227], v[212:215], v[52:55]\n"
"v_exp_f32 v195, v195\n"
"v_mfma_f32_16x16x32_bf16 v[56:59], a[228:231], v[212:215], v[56:59]\n"
"ds_read_b64_tr_b16 a[214:215], v218 offset:28672\n"
"v_cvt_pk_bf16_f32 v209, v194, v195\n"
"v_mfma_f32_16x16x32_bf16 v[60:63], a[232:235], v[212:215], v[60:63]\n"
"v_exp_f32 v196, v196\n"
"v_mfma_f32_16x16x32_bf16 a[240:243], a[252:255], v[212:215], a[240:243]\n"
"ds_read_b64_tr_b16 a[216:217], v219 offset:24576\n"
"ds_read_b64_tr_b16 a[218:219], v219 offset:28672\n"
"v_mfma_f32_16x16x32_bf16 v[200:203], a[172:175], v[144:147], 0\n"
"v_exp_f32 v197, v197\n"
"v_mfma_f32_16x16x32_bf16 v[200:203], a[176:179], v[148:151], v[200:203]\n"
"ds_read_b64_tr_b16 a[220:221], v220 offset:24576\n"
"v_cvt_pk_bf16_f32 v210, v196, v197\n"
"v_mfma_f32_16x16x32_bf16 v[200:203], a[180:183], v[152:155], v[200:203]\n"
"v_exp_f32 v198, v198\n"
"v_mfma_f32_16x16x32_bf16 v[200:203], a[184:187], v[156:159], v[200:203]\n"
"ds_read_b64_tr_b16 a[222:223], v220 offset:28672\n"
"ds_read_b64_tr_b16 a[224:225], v221 offset:24576\n"
"v_mfma_f32_16x16x32_bf16 v[204:207], a[188:191], v[144:147], 0\n"
"v_exp_f32 v199, v199\n"
"v_mfma_f32_16x16x32_bf16 v[204:207], a[192:195], v[148:151], v[204:207]\n"
"ds_read_b64_tr_b16 a[226:227], v221 offset:28672\n"
"v_cvt_pk_bf16_f32 v211, v198, v199\n"
"v_mfma_f32_16x16x32_bf16 v[204:207], a[196:199], v[152:155], v[204:207]\n"
"ds_read_b64_tr_b16 a[228:229], v222 offset:24576\n"
"ds_read_b64_tr_b16 a[230:231], v222 offset:28672\n"
"v_mfma_f32_16x16x32_bf16 v[204:207], a[200:203], v[156:159], v[204:207]\n"
"s_waitcnt lgkmcnt(10)\n"
"v_exp_f32 v200, v200\n"
"v_mfma_f32_16x16x32_bf16 v[0:3], a[204:207], v[208:211], v[0:3]\n"
"ds_read_b64_tr_b16 a[232:233], v223 offset:24576\n"
"ds_read_b64_tr_b16 a[234:235], v223 offset:28672\n"
"v_mfma_f32_16x16x32_bf16 v[4:7], a[208:211], v[208:211], v[4:7]\n"
"s_waitcnt lgkmcnt(8)\n"
"v_exp_f32 v201, v201\n"
"v_mfma_f32_16x16x32_bf16 v[8:11], a[212:215], v[208:211], v[8:11]\n"
"ds_read_b128 a[172:175], v224 offset:32768\n"
"v_cvt_pk_bf16_f32 v212, v200, v201\n"
"v_mfma_f32_16x16x32_bf16 v[12:15], a[216:219], v[208:211], v[12:15]\n"
"s_waitcnt lgkmcnt(5)\n"
"v_exp_f32 v202, v202\n"
"v_mfma_f32_16x16x32_bf16 v[16:19], a[220:223], v[208:211], v[16:19]\n"
"ds_read_b128 a[176:179], v225 offset:32768\n"
"ds_read_b128 a[180:183], v226 offset:32768\n"
"v_mfma_f32_16x16x32_bf16 v[20:23], a[224:227], v[208:211], v[20:23]\n"
"s_waitcnt lgkmcnt(3)\n"
"v_exp_f32 v203, v203\n"
"v_mfma_f32_16x16x32_bf16 v[24:27], a[228:231], v[208:211], v[24:27]\n"
"ds_read_b128 a[184:187], v227 offset:32768\n"
"v_cvt_pk_bf16_f32 v213, v202, v203\n"
"v_mfma_f32_16x16x32_bf16 v[28:31], a[232:235], v[208:211], v[28:31]\n"
"s_waitcnt lgkmcnt(2)\n"
"v_exp_f32 v204, v204\n"
"v_mfma_f32_16x16x32_bf16 a[236:239], a[252:255], v[208:211], a[236:239]\n"
"ds_read_b128 a[188:191], v224 offset:36864\n"
"ds_read_b128 a[192:195], v225 offset:36864\n"
"v_mfma_f32_16x16x32_bf16 v[192:195], a[172:175], v[128:131], 0\n"
"s_waitcnt lgkmcnt(2)\n"
"v_exp_f32 v205, v205\n"
"v_mfma_f32_16x16x32_bf16 v[192:195], a[176:179], v[132:135], v[192:195]\n"
"ds_read_b128 a[196:199], v226 offset:36864\n"
"v_cvt_pk_bf16_f32 v214, v204, v205\n"
"v_mfma_f32_16x16x32_bf16 v[192:195], a[180:183], v[136:139], v[192:195]\n"
"ds_read_b128 a[200:203], v227 offset:36864\n"
"v_exp_f32 v206, v206\n"
"v_mfma_f32_16x16x32_bf16 v[192:195], a[184:187], v[140:143], v[192:195]\n"
"s_waitcnt lgkmcnt(2)\n"
"v_mfma_f32_16x16x32_bf16 v[196:199], a[188:191], v[128:131], 0\n"
"v_exp_f32 v207, v207\n"
"v_mfma_f32_16x16x32_bf16 v[196:199], a[192:195], v[132:135], v[196:199]\n"
"s_waitcnt vmcnt(0)\n"
"s_waitcnt lgkmcnt(0)\n"
"v_cvt_pk_bf16_f32 v215, v206, v207\n"
"v_mfma_f32_16x16x32_bf16 v[196:199], a[196:199], v[136:139], v[196:199]\n"
"s_barrier\n"
"v_mfma_f32_16x16x32_bf16 v[196:199], a[200:203], v[140:143], v[196:199]\n"
"v_exp_f32 v192, v192\n"
"v_mfma_f32_16x16x32_bf16 v[32:35], a[204:207], v[212:215], v[32:35]\n"
"ds_read_b64_tr_b16 a[204:205], v216 offset:40960\n"
"ds_read_b64_tr_b16 a[206:207], v216 offset:45056\n"
"v_mfma_f32_16x16x32_bf16 v[36:39], a[208:211], v[212:215], v[36:39]\n"
"v_exp_f32 v193, v193\n"
"v_mfma_f32_16x16x32_bf16 v[40:43], a[212:215], v[212:215], v[40:43]\n"
"ds_read_b64_tr_b16 a[208:209], v217 offset:40960\n"
"v_cvt_pk_bf16_f32 v208, v192, v193\n"
"v_mfma_f32_16x16x32_bf16 v[44:47], a[216:219], v[212:215], v[44:47]\n"
"v_exp_f32 v194, v194\n"
"v_mfma_f32_16x16x32_bf16 v[48:51], a[220:223], v[212:215], v[48:51]\n"
"ds_read_b64_tr_b16 a[210:211], v217 offset:45056\n"
"ds_read_b64_tr_b16 a[212:213], v218 offset:40960\n"
"v_mfma_f32_16x16x32_bf16 v[52:55], a[224:227], v[212:215], v[52:55]\n"
"v_exp_f32 v195, v195\n"
"v_mfma_f32_16x16x32_bf16 v[56:59], a[228:231], v[212:215], v[56:59]\n"
"ds_read_b64_tr_b16 a[214:215], v218 offset:45056\n"
"v_cvt_pk_bf16_f32 v209, v194, v195\n"
"v_mfma_f32_16x16x32_bf16 v[60:63], a[232:235], v[212:215], v[60:63]\n"
"v_exp_f32 v196, v196\n"
"v_mfma_f32_16x16x32_bf16 a[240:243], a[252:255], v[212:215], a[240:243]\n"
"ds_read_b64_tr_b16 a[216:217], v219 offset:40960\n"
"ds_read_b64_tr_b16 a[218:219], v219 offset:45056\n"
"v_mfma_f32_16x16x32_bf16 v[200:203], a[172:175], v[144:147], 0\n"
"v_exp_f32 v197, v197\n"
"v_mfma_f32_16x16x32_bf16 v[200:203], a[176:179], v[148:151], v[200:203]\n"
"ds_read_b64_tr_b16 a[220:221], v220 offset:40960\n"
"v_cvt_pk_bf16_f32 v210, v196, v197\n"
"v_mfma_f32_16x16x32_bf16 v[200:203], a[180:183], v[152:155], v[200:203]\n"
"v_exp_f32 v198, v198\n"
"v_mfma_f32_16x16x32_bf16 v[200:203], a[184:187], v[156:159], v[200:203]\n"
"ds_read_b64_tr_b16 a[222:223], v220 offset:45056\n"
"ds_read_b64_tr_b16 a[224:225], v221 offset:40960\n"
"v_mfma_f32_16x16x32_bf16 v[204:207], a[188:191], v[144:147], 0\n"
"v_exp_f32 v199, v199\n"
"v_mfma_f32_16x16x32_bf16 v[204:207], a[192:195], v[148:151], v[204:207]\n"
"ds_read_b64_tr_b16 a[226:227], v221 offset:45056\n"
"v_cvt_pk_bf16_f32 v211, v198, v199\n"
"v_mfma_f32_16x16x32_bf16 v[204:207], a[196:199], v[152:155], v[204:207]\n"
"ds_read_b64_tr_b16 a[228:229], v222 offset:40960\n"
"ds_read_b64_tr_b16 a[230:231], v222 offset:45056\n"
"v_mfma_f32_16x16x32_bf16 v[204:207], a[200:203], v[156:159], v[204:207]\n"
"s_waitcnt lgkmcnt(10)\n"
"v_exp_f32 v200, v200\n"
"v_mfma_f32_16x16x32_bf16 v[0:3], a[204:207], v[208:211], v[0:3]\n"
"ds_read_b64_tr_b16 a[232:233], v223 offset:40960\n"
"ds_read_b64_tr_b16 a[234:235], v223 offset:45056\n"
"v_mfma_f32_16x16x32_bf16 v[4:7], a[208:211], v[208:211], v[4:7]\n"
"s_waitcnt lgkmcnt(8)\n"
"v_exp_f32 v201, v201\n"
"v_mfma_f32_16x16x32_bf16 v[8:11], a[212:215], v[208:211], v[8:11]\n"
"ds_read_b128 a[172:175], v224 offset:49152\n"
"v_cvt_pk_bf16_f32 v212, v200, v201\n"
"v_mfma_f32_16x16x32_bf16 v[12:15], a[216:219], v[208:211], v[12:15]\n"
"s_waitcnt lgkmcnt(5)\n"
"v_exp_f32 v202, v202\n"
"v_mfma_f32_16x16x32_bf16 v[16:19], a[220:223], v[208:211], v[16:19]\n"
"ds_read_b128 a[176:179], v225 offset:49152\n"
"ds_read_b128 a[180:183], v226 offset:49152\n"
"v_mfma_f32_16x16x32_bf16 v[20:23], a[224:227], v[208:211], v[20:23]\n"
"s_waitcnt lgkmcnt(3)\n"
"v_exp_f32 v203, v203\n"
"v_mfma_f32_16x16x32_bf16 v[24:27], a[228:231], v[208:211], v[24:27]\n"
"ds_read_b128 a[184:187], v227 offset:49152\n"
"v_cvt_pk_bf16_f32 v213, v202, v203\n"
"v_mfma_f32_16x16x32_bf16 v[28:31], a[232:235], v[208:211], v[28:31]\n"
"s_waitcnt lgkmcnt(2)\n"
"v_exp_f32 v204, v204\n"
"v_mfma_f32_16x16x32_bf16 a[236:239], a[252:255], v[208:211], a[236:239]\n"
"ds_read_b128 a[188:191], v224 offset:53248\n"
"ds_read_b128 a[192:195], v225 offset:53248\n"
"v_mfma_f32_16x16x32_bf16 v[192:195], a[172:175], v[128:131], 0\n"
"s_waitcnt lgkmcnt(2)\n"
"v_exp_f32 v205, v205\n"
"v_mfma_f32_16x16x32_bf16 v[192:195], a[176:179], v[132:135], v[192:195]\n"
"ds_read_b128 a[196:199], v226 offset:53248\n"
"v_cvt_pk_bf16_f32 v214, v204, v205\n"
"v_mfma_f32_16x16x32_bf16 v[192:195], a[180:183], v[136:139], v[192:195]\n"
"ds_read_b128 a[200:203], v227 offset:53248\n"
"v_exp_f32 v206, v206\n"
"v_mfma_f32_16x16x32_bf16 v[192:195], a[184:187], v[140:143], v[192:195]\n"
"s_waitcnt lgkmcnt(2)\n"
"v_mfma_f32_16x16x32_bf16 v[196:199], a[188:191], v[128:131], 0\n"
"v_exp_f32 v207, v207\n"
"v_mfma_f32_16x16x32_bf16 v[196:199], a[192:195], v[132:135], v[196:199]\n"
"s_waitcnt lgkmcnt(0)\n"
"v_cvt_pk_bf16_f32 v215, v206, v207\n"
"v_mfma_f32_16x16x32_bf16 v[196:199], a[196:199], v[136:139], v[196:199]\n"
"v_mfma_f32_16x16x32_bf16 v[196:199], a[200:203], v[140:143], v[196:199]\n"
"v_exp_f32 v192, v192\n"
"v_mfma_f32_16x16x32_bf16 v[32:35], a[204:207], v[212:215], v[32:35]\n"
"ds_read_b64_tr_b16 a[204:205], v216 offset:57344\n"
"ds_read_b64_tr_b16 a[206:207], v216 offset:61440\n"
"v_mfma_f32_16x16x32_bf16 v[36:39], a[208:211], v[212:215], v[36:39]\n"
"v_exp_f32 v193, v193\n"
"v_mfma_f32_16x16x32_bf16 v[40:43], a[212:215], v[212:215], v[40:43]\n"
"ds_read_b64_tr_b16 a[208:209], v217 offset:57344\n"
"v_cvt_pk_bf16_f32 v208, v192, v193\n"
"v_mfma_f32_16x16x32_bf16 v[44:47], a[216:219], v[212:215], v[44:47]\n"
"v_exp_f32 v194, v194\n"
"v_mfma_f32_16x16x32_bf16 v[48:51], a[220:223], v[212:215], v[48:51]\n"
"ds_read_b64_tr_b16 a[210:211], v217 offset:61440\n"
"ds_read_b64_tr_b16 a[212:213], v218 offset:57344\n"
"v_mfma_f32_16x16x32_bf16 v[52:55], a[224:227], v[212:215], v[52:55]\n"
"v_exp_f32 v195, v195\n"
"v_mfma_f32_16x16x32_bf16 v[56:59], a[228:231], v[212:215], v[56:59]\n"
"ds_read_b64_tr_b16 a[214:215], v218 offset:61440\n"
"v_cvt_pk_bf16_f32 v209, v194, v195\n"
"v_mfma_f32_16x16x32_bf16 v[60:63], a[232:235], v[212:215], v[60:63]\n"
"v_exp_f32 v196, v196\n"
"v_mfma_f32_16x16x32_bf16 a[240:243], a[252:255], v[212:215], a[240:243]\n"
"ds_read_b64_tr_b16 a[216:217], v219 offset:57344\n"
"ds_read_b64_tr_b16 a[218:219], v219 offset:61440\n"
"v_mfma_f32_16x16x32_bf16 v[200:203], a[172:175], v[144:147], 0\n"
"v_exp_f32 v197, v197\n"
"v_mfma_f32_16x16x32_bf16 v[200:203], a[176:179], v[148:151], v[200:203]\n"
"ds_read_b64_tr_b16 a[220:221], v220 offset:57344\n"
"v_cvt_pk_bf16_f32 v210, v196, v197\n"
"v_mfma_f32_16x16x32_bf16 v[200:203], a[180:183], v[152:155], v[200:203]\n"
"v_exp_f32 v198, v198\n"
"v_mfma_f32_16x16x32_bf16 v[200:203], a[184:187], v[156:159], v[200:203]\n"
"ds_read_b64_tr_b16 a[222:223], v220 offset:61440\n"
"ds_read_b64_tr_b16 a[224:225], v221 offset:57344\n"
"v_mfma_f32_16x16x32_bf16 v[204:207], a[188:191], v[144:147], 0\n"
"v_exp_f32 v199, v199\n"
"v_mfma_f32_16x16x32_bf16 v[204:207], a[192:195], v[148:151], v[204:207]\n"
"ds_read_b64_tr_b16 a[226:227], v221 offset:61440\n"
"v_cvt_pk_bf16_f32 v211, v198, v199\n"
"v_mfma_f32_16x16x32_bf16 v[204:207], a[196:199], v[152:155], v[204:207]\n"
"ds_read_b64_tr_b16 a[228:229], v222 offset:57344\n"
"ds_read_b64_tr_b16 a[230:231], v222 offset:61440\n"
"v_mfma_f32_16x16x32_bf16 v[204:207], a[200:203], v[156:159], v[204:207]\n"
"s_waitcnt lgkmcnt(10)\n"
"v_exp_f32 v200, v200\n"
"v_mfma_f32_16x16x32_bf16 v[0:3], a[204:207], v[208:211], v[0:3]\n"
"ds_read_b64_tr_b16 a[232:233], v223 offset:57344\n"
"ds_read_b64_tr_b16 a[234:235], v223 offset:61440\n"
"v_mfma_f32_16x16x32_bf16 v[4:7], a[208:211], v[208:211], v[4:7]\n"
"s_waitcnt lgkmcnt(8)\n"
"v_exp_f32 v201, v201\n"
"v_mfma_f32_16x16x32_bf16 v[8:11], a[212:215], v[208:211], v[8:11]\n"
"ds_read_b128 a[172:175], v224 offset:0\n"
"v_cvt_pk_bf16_f32 v212, v200, v201\n"
"v_mfma_f32_16x16x32_bf16 v[12:15], a[216:219], v[208:211], v[12:15]\n"
"s_waitcnt lgkmcnt(5)\n"
"v_exp_f32 v202, v202\n"
"v_mfma_f32_16x16x32_bf16 v[16:19], a[220:223], v[208:211], v[16:19]\n"
"ds_read_b128 a[176:179], v225 offset:0\n"
"ds_read_b128 a[180:183], v226 offset:0\n"
"v_mfma_f32_16x16x32_bf16 v[20:23], a[224:227], v[208:211], v[20:23]\n"
"s_waitcnt lgkmcnt(3)\n"
"v_exp_f32 v203, v203\n"
"v_mfma_f32_16x16x32_bf16 v[24:27], a[228:231], v[208:211], v[24:27]\n"
"ds_read_b128 a[184:187], v227 offset:0\n"
"v_cvt_pk_bf16_f32 v213, v202, v203\n"
"v_mfma_f32_16x16x32_bf16 v[28:31], a[232:235], v[208:211], v[28:31]\n"
"s_waitcnt lgkmcnt(2)\n"
"v_exp_f32 v204, v204\n"
"v_mfma_f32_16x16x32_bf16 a[236:239], a[252:255], v[208:211], a[236:239]\n"
"ds_read_b128 a[188:191], v224 offset:4096\n"
"ds_read_b128 a[192:195], v225 offset:4096\n"
"v_mfma_f32_16x16x32_bf16 v[192:195], a[172:175], v[160:163], 0\n"
"s_waitcnt lgkmcnt(2)\n"
"v_exp_f32 v205, v205\n"
"v_mfma_f32_16x16x32_bf16 v[192:195], a[176:179], v[164:167], v[192:195]\n"
"ds_read_b128 a[196:199], v226 offset:4096\n"
"v_cvt_pk_bf16_f32 v214, v204, v205\n"
"v_mfma_f32_16x16x32_bf16 v[192:195], a[180:183], v[168:171], v[192:195]\n"
"ds_read_b128 a[200:203], v227 offset:4096\n"
"v_exp_f32 v206, v206\n"
"v_accvgpr_read_b32 v248, a236\n"
"s_add_u32 s99, s88, 0\n"
"v_subrev_u32 v249, 0x30800000, v248\n"
"v_mfma_f32_16x16x32_bf16 v[192:195], a[184:187], v[172:175], v[192:195]\n"
"s_waitcnt lgkmcnt(2)\n"
"v_rcp_f32 v248, v248\n"
"v_cmp_le_u32 vcc, 0x1e000000, v249\n"
"s_or_b64 s[92:93], s[92:93], vcc\n"
"v_mfma_f32_16x16x32_bf16 v[196:199], a[188:191], v[160:163], 0\n"
"v_exp_f32 v207, v207\n"
"v_mul_f32 v0, v0, v248\n"
"v_mul_f32 v1, v1, v248\n"
"v_mul_f32 v2, v2, v248\n"
"v_mfma_f32_16x16x32_bf16 v[196:199], a[192:195], v[164:167], v[196:199]\n"
"s_waitcnt lgkmcnt(0)\n"
"v_cvt_pk_bf16_f32 v215, v206, v207\n"
"v_mul_f32 v3, v3, v248\n"
"v_mul_f32 v4, v4, v248\n"
"v_mul_f32 v5, v5, v248\n"
"v_mfma_f32_16x16x32_bf16 v[196:199], a[196:199], v[168:171], v[196:199]\n"
"v_mul_f32 v6, v6, v248\n"
"v_mul_f32 v7, v7, v248\n"
"v_cvt_pk_bf16_f32 v0, v0, v1\n"
"v_mfma_f32_16x16x32_bf16 v[196:199], a[200:203], v[172:175], v[196:199]\n"
"v_exp_f32 v192, v192\n"
"v_cvt_pk_bf16_f32 v1, v2, v3\n"
"v_cvt_pk_bf16_f32 v2, v4, v5\n"
"v_cvt_pk_bf16_f32 v3, v6, v7\n"
"v_mfma_f32_16x16x32_bf16 v[32:35], a[204:207], v[212:215], v[32:35]\n"
"ds_read_b64_tr_b16 a[204:205], v216 offset:8192\n"
"ds_read_b64_tr_b16 a[206:207], v216 offset:12288\n"
"buffer_store_dwordx4 v[0:3], v243, s[56:59], s99 offen offset:0 sc1\n"
"v_mul_f32 v8, v8, v248\n"
"v_mul_f32 v9, v9, v248\n"
"v_mfma_f32_16x16x32_bf16 v[36:39], a[208:211], v[212:215], v[36:39]\n"
"v_exp_f32 v193, v193\n"
"v_mul_f32 v10, v10, v248\n"
"v_mul_f32 v11, v11, v248\n"
"v_mul_f32 v12, v12, v248\n"
"v_mfma_f32_16x16x32_bf16 v[40:43], a[212:215], v[212:215], v[40:43]\n"
"ds_read_b64_tr_b16 a[208:209], v217 offset:8192\n"
"v_cvt_pk_bf16_f32 v208, v192, v193\n"
"v_mul_f32 v13, v13, v248\n"
"v_mul_f32 v14, v14, v248\n"
"v_mul_f32 v15, v15, v248\n"
"v_mfma_f32_16x16x32_bf16 v[44:47], a[216:219], v[212:215], v[44:47]\n"
"v_exp_f32 v194, v194\n"
"v_cvt_pk_bf16_f32 v8, v8, v9\n"
"v_cvt_pk_bf16_f32 v9, v10, v11\n"
"v_cvt_pk_bf16_f32 v10, v12, v13\n"
"v_mfma_f32_16x16x32_bf16 v[48:51], a[220:223], v[212:215], v[48:51]\n"
"ds_read_b64_tr_b16 a[210:211], v217 offset:12288\n"
"ds_read_b64_tr_b16 a[212:213], v218 offset:8192\n"
"v_cvt_pk_bf16_f32 v11, v14, v15\n"
"buffer_store_dwordx4 v[8:11], v243, s[56:59], s99 offen offset:1024 sc1\n"
"v_mul_f32 v16, v16, v248\n"
"v_mfma_f32_16x16x32_bf16 v[52:55], a[224:227], v[212:215], v[52:55]\n"
"v_exp_f32 v195, v195\n"
"v_mul_f32 v17, v17, v248\n"
"v_mul_f32 v18, v18, v248\n"
"v_mul_f32 v19, v19, v248\n"
"v_mfma_f32_16x16x32_bf16 v[56:59], a[228:231], v[212:215], v[56:59]\n"
"ds_read_b64_tr_b16 a[214:215], v218 offset:12288\n"
"v_cvt_pk_bf16_f32 v209, v194, v195\n"
"v_mul_f32 v20, v20, v248\n"
"v_mul_f32 v21, v21, v248\n"
"v_mul_f32 v22, v22, v248\n"
"v_mfma_f32_16x16x32_bf16 v[60:63], a[232:235], v[212:215], v[60:63]\n"
"v_exp_f32 v196, v196\n"
"v_mul_f32 v23, v23, v248\n"
"v_cvt_pk_bf16_f32 v16, v16, v17\n"
"v_cvt_pk_bf16_f32 v17, v18, v19\n"
"v_mfma_f32_16x16x32_bf16 a[240:243], a[252:255], v[212:215], a[240:243]\n"
"ds_read_b64_tr_b16 a[216:217], v219 offset:8192\n"
"ds_read_b64_tr_b16 a[218:219], v219 offset:12288\n"
"v_cvt_pk_bf16_f32 v18, v20, v21\n"
"v_cvt_pk_bf16_f32 v19, v22, v23\n"
"buffer_store_dwordx4 v[16:19], v243, s[56:59], s99 offen offset:2048 sc1\n"
"v_mfma_f32_16x16x32_bf16 v[200:203], a[172:175], v[176:179], 0\n"
"v_exp_f32 v197, v197\n"
"v_mul_f32 v24, v24, v248\n"
"v_mul_f32 v25, v25, v248\n"
"v_mul_f32 v26, v26, v248\n"
"v_mfma_f32_16x16x32_bf16 v[200:203], a[176:179], v[180:183], v[200:203]\n"
"ds_read_b64_tr_b16 a[220:221], v220 offset:8192\n"
"v_cvt_pk_bf16_f32 v210, v196, v197\n"
"v_mul_f32 v27, v27, v248\n"
"v_mul_f32 v28, v28, v248\n"
"v_mul_f32 v29, v29, v248\n"
"v_mfma_f32_16x16x32_bf16 v[200:203], a[180:183], v[184:187], v[200:203]\n"
"v_exp_f32 v198, v198\n"
"v_mul_f32 v30, v30, v248\n"
"v_mul_f32 v31, v31, v248\n"
"v_cvt_pk_bf16_f32 v24, v24, v25\n"
"v_mfma_f32_16x16x32_bf16 v[200:203], a[184:187], v[188:191], v[200:203]\n"
"ds_read_b64_tr_b16 a[222:223], v220 offset:12288\n"
"ds_read_b64_tr_b16 a[224:225], v221 offset:8192\n"
"v_cvt_pk_bf16_f32 v25, v26, v27\n"
"v_cvt_pk_bf16_f32 v26, v28, v29\n"
"v_cvt_pk_bf16_f32 v27, v30, v31\n"
"v_mfma_f32_16x16x32_bf16 v[204:207], a[188:191], v[176:179], 0\n"
"v_exp_f32 v199, v199\n"
"buffer_store_dwordx4 v[24:27], v243, s[56:59], s99 offen offset:3072 sc1\n"
"v_accvgpr_read_b32 v250, a240\n"
"s_add_u32 s99, s88, 32768\n"
"v_subrev_u32 v251, 0x30800000, v250\n"
"v_mfma_f32_16x16x32_bf16 v[204:207], a[192:195], v[180:183], v[204:207]\n"
"ds_read_b64_tr_b16 a[226:227], v221 offset:12288\n"
"v_cvt_pk_bf16_f32 v211, v198, v199\n"
"v_rcp_f32 v250, v250\n"
"v_cmp_le_u32 vcc, 0x1e000000, v251\n"
"s_or_b64 s[92:93], s[92:93], vcc\n"
"v_mfma_f32_16x16x32_bf16 v[204:207], a[196:199], v[184:187], v[204:207]\n"
"ds_read_b64_tr_b16 a[228:229], v222 offset:8192\n"
"ds_read_b64_tr_b16 a[230:231], v222 offset:12288\n"
"v_mul_f32 v32, v32, v250\n"
"v_mul_f32 v33, v33, v250\n"
"v_mul_f32 v34, v34, v250\n"
"v_mfma_f32_16x16x32_bf16 v[204:207], a[200:203], v[188:191], v[204:207]\n"
"s_waitcnt lgkmcnt(10)\n"
"v_exp_f32 v200, v200\n"
"v_mul_f32 v35, v35, v250\n"
"v_mul_f32 v36, v36, v250\n"
"v_mul_f32 v37, v37, v250\n"
"v_mfma_f32_16x16x32_bf16 v[64:67], a[204:207], v[208:211], v[64:67]\n"
"ds_read_b64_tr_b16 a[232:233], v223 offset:8192\n"
"ds_read_b64_tr_b16 a[234:235], v223 offset:12288\n"
"v_mul_f32 v38, v38, v250\n"
"v_mul_f32 v39, v39, v250\n"
"v_cvt_pk_bf16_f32 v32, v32, v33\n"
"v_mfma_f32_16x16x32_bf16 v[68:71], a[208:211], v[208:211], v[68:71]\n"
"s_waitcnt lgkmcnt(8)\n"
"v_exp_f32 v201, v201\n"
"v_cvt_pk_bf16_f32 v33, v34, v35\n"
"v_cvt_pk_bf16_f32 v34, v36, v37\n"
"v_cvt_pk_bf16_f32 v35, v38, v39\n"
"v_mfma_f32_16x16x32_bf16 v[72:75], a[212:215], v[208:211], v[72:75]\n"
"ds_read_b128 a[172:175], v224 offset:16384\n"
"v_cvt_pk_bf16_f32 v212, v200, v201\n"
"buffer_store_dwordx4 v[32:35], v243, s[56:59], s99 offen offset:0 sc1\n"
"v_mul_f32 v40, v40, v250\n"
"v_mul_f32 v41, v41, v250\n"
"v_mfma_f32_16x16x32_bf16 v[76:79], a[216:219], v[208:211], v[76:79]\n"
"s_waitcnt lgkmcnt(5)\n"
"v_exp_f32 v202, v202\n"
"v_mul_f32 v42, v42, v250\n"
"v_mul_f32 v43, v43, v250\n"
"v_mul_f32 v44, v44, v250\n"
"v_mfma_f32_16x16x32_bf16 v[80:83], a[220:223], v[208:211], v[80:83]\n"
"ds_read_b128 a[176:179], v225 offset:16384\n"
"ds_read_b128 a[180:183], v226 offset:16384\n"
"v_mul_f32 v45, v45, v250\n"
"v_mul_f32 v46, v46, v250\n"
"v_mul_f32 v47, v47, v250\n"
"v_mfma_f32_16x16x32_bf16 v[84:87], a[224:227], v[208:211], v[84:87]\n"
"L_ic7:\n"
"s_cbranch_execz L_ic11\n"
"s_waitcnt lgkmcnt(3)\n"
"v_exp_f32 v203, v203\n"
"v_cvt_pk_bf16_f32 v40, v40, v41\n"
"v_cvt_pk_bf16_f32 v41, v42, v43\n"
"v_cvt_pk_bf16_f32 v42, v44, v45\n"
"v_mfma_f32_16x16x32_bf16 v[88:91], a[228:231], v[208:211], v[88:91]\n"
"ds_read_b128 a[184:187], v227 offset:16384\n"
"v_cvt_pk_bf16_f32 v213, v202, v203\n"
"v_cvt_pk_bf16_f32 v43, v46, v47\n"
"buffer_store_dwordx4 v[40:43], v243, s[56:59], s99 offen offset:1024 sc1\n"
"v_mul_f32 v48, v48, v250\n"
"v_mfma_f32_16x16x32_bf16 v[92:95], a[232:235], v[208:211], v[92:95]\n"
"s_waitcnt lgkmcnt(2)\n"
"v_exp_f32 v204, v204\n"
"v_mul_f32 v49, v49, v250\n"
"v_mul_f32 v50, v50, v250\n"
"v_mul_f32 v51, v51, v250\n"
"v_mfma_f32_16x16x32_bf16 a[244:247], a[252:255], v[208:211], a[244:247]\n"
"ds_read_b128 a[188:191], v224 offset:20480\n"
"ds_read_b128 a[192:195], v225 offset:20480\n"
"v_mul_f32 v52, v52, v250\n"
"v_mul_f32 v53, v53, v250\n"
"v_mul_f32 v54, v54, v250\n"
"v_mfma_f32_16x16x32_bf16 v[192:195], a[172:175], v[160:163], 0\n"
"s_waitcnt lgkmcnt(2)\n"
"v_exp_f32 v205, v205\n"
"v_mul_f32 v55, v55, v250\n"
"v_cvt_pk_bf16_f32 v48, v48, v49\n"
"v_cvt_pk_bf16_f32 v49, v50, v51\n"
"v_mfma_f32_16x16x32_bf16 v[192:195], a[176:179], v[164:167], v[192:195]\n"
"ds_read_b128 a[196:199], v226 offset:20480\n"
"v_cvt_pk_bf16_f32 v214, v204, v205\n"
"v_cvt_pk_bf16_f32 v50, v52, v53\n"
"v_cvt_pk_bf16_f32 v51, v54, v55\n"
"buffer_store_dwordx4 v[48:51], v243, s[56:59], s99 offen offset:2048 sc1\n"
"v_mfma_f32_16x16x32_bf16 v[192:195], a[180:183], v[168:171], v[192:195]\n"
"ds_read_b128 a[200:203], v227 offset:20480\n"
"v_exp_f32 v206, v206\n"
"v_mul_f32 v56, v56, v250\n"
"v_mul_f32 v57, v57, v250\n"
"v_mul_f32 v58, v58, v250\n"
"v_mfma_f32_16x16x32_bf16 v[192:195], a[184:187], v[172:175], v[192:195]\n"
"s_waitcnt lgkmcnt(2)\n"
"v_mul_f32 v59, v59, v250\n"
"v_mul_f32 v60, v60, v250\n"
"v_mul_f32 v61, v61, v250\n"
"v_mfma_f32_16x16x32_bf16 v[196:199], a[188:191], v[160:163], 0\n"
"v_exp_f32 v207, v207\n"
"v_mul_f32 v62, v62, v250\n"
"v_mul_f32 v63, v63, v250\n"
"v_cvt_pk_bf16_f32 v56, v56, v57\n"
"v_mfma_f32_16x16x32_bf16 v[196:199], a[192:195], v[164:167], v[196:199]\n"
"s_waitcnt lgkmcnt(0)\n"
"v_cvt_pk_bf16_f32 v215, v206, v207\n"
"v_cvt_pk_bf16_f32 v57, v58, v59\n"
"v_cvt_pk_bf16_f32 v58, v60, v61\n"
"v_cvt_pk_bf16_f32 v59, v62, v63\n"
"v_mfma_f32_16x16x32_bf16 v[196:199], a[196:199], v[168:171], v[196:199]\n"
"s_barrier\n"
"v_mfma_f32_16x16x32_bf16 v[196:199], a[200:203], v[172:175], v[196:199]\n"
"v_exp_f32 v192, v192\n"
"buffer_store_dwordx4 v[56:59], v243, s[56:59], s99 offen offset:3072 sc1\n"
"v_mfma_f32_16x16x32_bf16 v[96:99], a[204:207], v[212:215], v[96:99]\n"
"ds_read_b64_tr_b16 a[204:205], v216 offset:24576\n"
"ds_read_b64_tr_b16 a[206:207], v216 offset:28672\n"
"v_mfma_f32_16x16x32_bf16 v[100:103], a[208:211], v[212:215], v[100:103]\n"
"s_add_u32 s20, s32, 44032\n"
"s_add_u32 s22, s32, 88064\n"
"v_exp_f32 v193, v193\n"
"v_mfma_f32_16x16x32_bf16 v[104:107], a[212:215], v[212:215], v[104:107]\n"
"ds_read_b64_tr_b16 a[208:209], v217 offset:24576\n"
"v_cvt_pk_bf16_f32 v208, v192, v193\n"
"v_mfma_f32_16x16x32_bf16 v[108:111], a[216:219], v[212:215], v[108:111]\n"
"s_add_u32 s23, s32, 132096\n"
"v_exp_f32 v194, v194\n"
"v_cmp_gt_u32 vcc, 43, v242\n"
"v_mov_b32 v45, 2130706432\n"
"v_lshlrev_b32 v46, 5, v242\n"
"v_cndmask_b32 v46, v45, v46, vcc\n"
"v_mfma_f32_16x16x32_bf16 v[112:115], a[220:223], v[212:215], v[112:115]\n"
"ds_read_b64_tr_b16 a[210:211], v217 offset:28672\n"
"ds_read_b64_tr_b16 a[212:213], v218 offset:24576\n"
"v_mfma_f32_16x16x32_bf16 v[116:119], a[224:227], v[212:215], v[116:119]\n"
"s_lshl3_add_u32 s97, s10, s87\n"
"v_exp_f32 v195, v195\n"
"v_mfma_f32_16x16x32_bf16 v[120:123], a[228:231], v[212:215], v[120:123]\n"
"ds_read_b64_tr_b16 a[214:215], v218 offset:28672\n"
"v_cvt_pk_bf16_f32 v209, v194, v195\n"
"v_mfma_f32_16x16x32_bf16 v[124:127], a[232:235], v[212:215], v[124:127]\n"
"s_lshl_b32 s8, s97, 10\n"
"s_lshl_b32 s96, s85, 7\n"
"s_add_u32 s8, s8, s96\n"
"s_lshl_b32 s9, s97, 3\n"
"s_add_u32 s9, s9, 16384\n"
"s_add_u32 s96, s8, 32768\n"
"v_exp_f32 v196, v196\n"
"v_mfma_f32_16x16x32_bf16 a[248:251], a[252:255], v[212:215], a[248:251]\n"
"ds_read_b64_tr_b16 a[216:217], v219 offset:24576\n"
"ds_read_b64_tr_b16 a[218:219], v219 offset:28672\n"
"v_mfma_f32_16x16x32_bf16 v[200:203], a[172:175], v[176:179], 0\n"
"v_exp_f32 v197, v197\n"
"v_mfma_f32_16x16x32_bf16 v[200:203], a[176:179], v[180:183], v[200:203]\n"
"ds_read_b64_tr_b16 a[220:221], v220 offset:24576\n"
"v_cvt_pk_bf16_f32 v210, v196, v197\n"
"v_mfma_f32_16x16x32_bf16 v[200:203], a[180:183], v[184:187], v[200:203]\n"
"v_exp_f32 v198, v198\n"
"v_mfma_f32_16x16x32_bf16 v[200:203], a[184:187], v[188:191], v[200:203]\n"
"ds_read_b64_tr_b16 a[222:223], v220 offset:28672\n"
"ds_read_b64_tr_b16 a[224:225], v221 offset:24576\n"
"v_mfma_f32_16x16x32_bf16 v[204:207], a[188:191], v[176:179], 0\n"
"v_exp_f32 v199, v199\n"
"v_mfma_f32_16x16x32_bf16 v[204:207], a[192:195], v[180:183], v[204:207]\n"
"ds_read_b64_tr_b16 a[226:227], v221 offset:28672\n"
"v_cvt_pk_bf16_f32 v211, v198, v199\n"
"v_mfma_f32_16x16x32_bf16 v[204:207], a[196:199], v[184:187], v[204:207]\n"
"ds_read_b64_tr_b16 a[228:229], v222 offset:24576\n"
"ds_read_b64_tr_b16 a[230:231], v222 offset:28672\n"
"v_mfma_f32_16x16x32_bf16 v[204:207], a[200:203], v[188:191], v[204:207]\n"
"s_waitcnt lgkmcnt(10)\n"
"v_exp_f32 v200, v200\n"
"v_mfma_f32_16x16x32_bf16 v[64:67], a[204:207], v[208:211], v[64:67]\n"
"ds_read_b64_tr_b16 a[232:233], v223 offset:24576\n"
"ds_read_b64_tr_b16 a[234:235], v223 offset:28672\n"
"v_mfma_f32_16x16x32_bf16 v[68:71], a[208:211], v[208:211], v[68:71]\n"
"s_waitcnt lgkmcnt(8)\n"
"v_exp_f32 v201, v201\n"
"v_mfma_f32_16x16x32_bf16 v[72:75], a[212:215], v[208:211], v[72:75]\n"
"ds_read_b128 a[172:175], v224 offset:32768\n"
"v_cvt_pk_bf16_f32 v212, v200, v201\n"
"v_mfma_f32_16x16x32_bf16 v[76:79], a[216:219], v[208:211], v[76:79]\n"
"s_waitcnt lgkmcnt(5)\n"
"v_exp_f32 v202, v202\n"
"v_mfma_f32_16x16x32_bf16 v[80:83], a[220:223], v[208:211], v[80:83]\n"
"ds_read_b128 a[176:179], v225 offset:32768\n"
"ds_read_b128 a[180:183], v226 offset:32768\n"
"v_mfma_f32_16x16x32_bf16 v[84:87], a[224:227], v[208:211], v[84:87]\n"
"s_waitcnt lgkmcnt(3)\n"
"v_exp_f32 v203, v203\n"
"v_mfma_f32_16x16x32_bf16 v[88:91], a[228:231], v[208:211], v[88:91]\n"
"ds_read_b128 a[184:187], v227 offset:32768\n"
"v_cvt_pk_bf16_f32 v213, v202, v203\n"
"v_mfma_f32_16x16x32_bf16 v[92:95], a[232:235], v[208:211], v[92:95]\n"
"s_waitcnt lgkmcnt(2)\n"
"v_exp_f32 v204, v204\n"
"v_mfma_f32_16x16x32_bf16 a[244:247], a[252:255], v[208:211], a[244:247]\n"
"ds_read_b128 a[188:191], v224 offset:36864\n"
"ds_read_b128 a[192:195], v225 offset:36864\n"
"v_mfma_f32_16x16x32_bf16 v[192:195], a[172:175], v[160:163], 0\n"
"s_waitcnt lgkmcnt(2)\n"
"v_exp_f32 v205, v205\n"
"v_mfma_f32_16x16x32_bf16 v[192:195], a[176:179], v[164:167], v[192:195]\n"
"ds_read_b128 a[196:199], v226 offset:36864\n"
"v_cvt_pk_bf16_f32 v214, v204, v205\n"
"v_mfma_f32_16x16x32_bf16 v[192:195], a[180:183], v[168:171], v[192:195]\n"
"ds_read_b128 a[200:203], v227 offset:36864\n"
"v_exp_f32 v206, v206\n"
"v_mfma_f32_16x16x32_bf16 v[192:195], a[184:187], v[172:175], v[192:195]\n"
"s_waitcnt lgkmcnt(2)\n"
"v_lshrrev_b32 v47, 3, v242\n"
"v_and_b32 v44, 0x70, v243\n"
"v_lshl_add_u32 v44, v47, 10, v44\n"
"v_lshlrev_b32 v45, 3, v47\n"
"v_mfma_f32_16x16x32_bf16 v[196:199], a[188:191], v[160:163], 0\n"
"s_add_u32 m0, s90, 0\n"
"v_exp_f32 v207, v207\n"
"v_mfma_f32_16x16x32_bf16 v[196:199], a[192:195], v[164:167], v[196:199]\n"
"s_waitcnt lgkmcnt(0)\n"
"s_xor_b32 s24, s89, 491520\n"
"v_cvt_pk_bf16_f32 v215, v206, v207\n"
"v_mfma_f32_16x16x32_bf16 v[196:199], a[196:199], v[168:171], v[196:199]\n"
"buffer_load_dwordx4 v230, s[52:55], s24 offen offset:0 lds\n"
"v_mfma_f32_16x16x32_bf16 v[196:199], a[200:203], v[172:175], v[196:199]\n"
"v_exp_f32 v192, v192\n"
"v_mfma_f32_16x16x32_bf16 v[96:99], a[204:207], v[212:215], v[96:99]\n"
"ds_read_b64_tr_b16 a[204:205], v216 offset:40960\n"
"ds_read_b64_tr_b16 a[206:207], v216 offset:45056\n"
"v_mfma_f32_16x16x32_bf16 v[100:103], a[208:211], v[212:215], v[100:103]\n"
"v_exp_f32 v193, v193\n"
"v_mfma_f32_16x16x32_bf16 v[104:107], a[212:215], v[212:215], v[104:107]\n"
"ds_read_b64_tr_b16 a[208:209], v217 offset:40960\n"
"buffer_load_dwordx4 v231, s[52:55], s24 offen offset:1024 lds\n"
"v_cvt_pk_bf16_f32 v208, v192, v193\n"
"v_mfma_f32_16x16x32_bf16 v[108:111], a[216:219], v[212:215], v[108:111]\n"
"v_exp_f32 v194, v194\n"
"v_mfma_f32_16x16x32_bf16 v[112:115], a[220:223], v[212:215], v[112:115]\n"
"ds_read_b64_tr_b16 a[210:211], v217 offset:45056\n"
"ds_read_b64_tr_b16 a[212:213], v218 offset:40960\n"
"v_mfma_f32_16x16x32_bf16 v[116:119], a[224:227], v[212:215], v[116:119]\n"
"v_exp_f32 v195, v195\n"
"v_mfma_f32_16x16x32_bf16 v[120:123], a[228:231], v[212:215], v[120:123]\n"
"ds_read_b64_tr_b16 a[214:215], v218 offset:45056\n"
"buffer_load_dwordx4 v232, s[52:55], s24 offen offset:2048 lds\n"
"v_cvt_pk_bf16_f32 v209, v194, v195\n"
"v_mfma_f32_16x16x32_bf16 v[124:127], a[232:235], v[212:215], v[124:127]\n"
"v_exp_f32 v196, v196\n"
"v_mfma_f32_16x16x32_bf16 a[248:251], a[252:255], v[212:215], a[248:251]\n"
"ds_read_b64_tr_b16 a[216:217], v219 offset:40960\n"
"ds_read_b64_tr_b16 a[218:219], v219 offset:45056\n"
"v_mfma_f32_16x16x32_bf16 v[200:203], a[172:175], v[176:179], 0\n"
"v_exp_f32 v197, v197\n"
"v_mfma_f32_16x16x32_bf16 v[200:203], a[176:179], v[180:183], v[200:203]\n"
"ds_read_b64_tr_b16 a[220:221], v220 offset:40960\n"
"buffer_load_dwordx4 v233, s[52:55], s24 offen offset:3072 lds\n"
"v_cvt_pk_bf16_f32 v210, v196, v197\n"
"v_mfma_f32_16x16x32_bf16 v[200:203], a[180:183], v[184:187], v[200:203]\n"
"v_exp_f32 v198, v198\n"
"v_mfma_f32_16x16x32_bf16 v[200:203], a[184:187], v[188:191], v[200:203]\n"
"ds_read_b64_tr_b16 a[222:223], v220 offset:45056\n"
"ds_read_b64_tr_b16 a[224:225], v221 offset:40960\n"
"v_mfma_f32_16x16x32_bf16 v[204:207], a[188:191], v[176:179], 0\n"
"v_exp_f32 v199, v199\n"
"v_mfma_f32_16x16x32_bf16 v[204:207], a[192:195], v[180:183], v[204:207]\n"
"ds_read_b64_tr_b16 a[226:227], v221 offset:45056\n"
"buffer_load_dwordx4 v[0:3], v46, s[56:59], s32 offen offset:0 sc1\n"
"v_cvt_pk_bf16_f32 v211, v198, v199\n"
"v_mfma_f32_16x16x32_bf16 v[204:207], a[196:199], v[184:187], v[204:207]\n"
"ds_read_b64_tr_b16 a[228:229], v222 offset:40960\n"
"ds_read_b64_tr_b16 a[230:231], v222 offset:45056\n"
"v_mfma_f32_16x16x32_bf16 v[204:207], a[200:203], v[188:191], v[204:207]\n"
"s_waitcnt lgkmcnt(10)\n"
"v_exp_f32 v200, v200\n"
"v_mfma_f32_16x16x32_bf16 v[64:67], a[204:207], v[208:211], v[64:67]\n"
"ds_read_b64_tr_b16 a[232:233], v223 offset:40960\n"
"ds_read_b64_tr_b16 a[234:235], v223 offset:45056\n"
"v_mfma_f32_16x16x32_bf16 v[68:71], a[208:211], v[208:211], v[68:71]\n"
"s_waitcnt lgkmcnt(8)\n"
"buffer_load_dwordx4 v[4:7], v46, s[56:59], s32 offen offset:16 sc1\n"
"v_exp_f32 v201, v201\n"
"v_mfma_f32_16x16x32_bf16 v[72:75], a[212:215], v[208:211], v[72:75]\n"
"ds_read_b128 a[172:175], v224 offset:49152\n"
"v_cvt_pk_bf16_f32 v212, v200, v201\n"
"v_mfma_f32_16x16x32_bf16 v[76:79], a[216:219], v[208:211], v[76:79]\n"
"s_waitcnt lgkmcnt(5)\n"
"v_exp_f32 v202, v202\n"
"v_mfma_f32_16x16x32_bf16 v[80:83], a[220:223], v[208:211], v[80:83]\n"
"ds_read_b128 a[176:179], v225 offset:49152\n"
"ds_read_b128 a[180:183], v226 offset:49152\n"
"v_mfma_f32_16x16x32_bf16 v[84:87], a[224:227], v[208:211], v[84:87]\n"
"s_waitcnt lgkmcnt(3)\n"
"buffer_load_dwordx4 v[8:11], v46, s[56:59], s20 offen offset:0 sc1\n"
"v_exp_f32 v203, v203\n"
"v_mfma_f32_16x16x32_bf16 v[88:91], a[228:231], v[208:211], v[88:91]\n"
"ds_read_b128 a[184:187], v227 offset:49152\n"
"v_cvt_pk_bf16_f32 v213, v202, v203\n"
"v_mfma_f32_16x16x32_bf16 v[92:95], a[232:235], v[208:211], v[92:95]\n"
"s_waitcnt lgkmcnt(2)\n"
"v_exp_f32 v204, v204\n"
"v_mfma_f32_16x16x32_bf16 a[244:247], a[252:255], v[208:211], a[244:247]\n"
"ds_read_b128 a[188:191], v224 offset:53248\n"
"ds_read_b128 a[192:195], v225 offset:53248\n"
"v_mfma_f32_16x16x32_bf16 v[192:195], a[172:175], v[160:163], 0\n"
"s_waitcnt lgkmcnt(2)\n"
"buffer_load_dwordx4 v[12:15], v46, s[56:59], s20 offen offset:16 sc1\n"
"v_exp_f32 v205, v205\n"
"v_mfma_f32_16x16x32_bf16 v[192:195], a[176:179], v[164:167], v[192:195]\n"
"ds_read_b128 a[196:199], v226 offset:53248\n"
"v_cvt_pk_bf16_f32 v214, v204, v205\n"
"v_mfma_f32_16x16x32_bf16 v[192:195], a[180:183], v[168:171], v[192:195]\n"
"ds_read_b128 a[200:203], v227 offset:53248\n"
"v_exp_f32 v206, v206\n"
"v_mfma_f32_16x16x32_bf16 v[192:195], a[184:187], v[172:175], v[192:195]\n"
"s_waitcnt lgkmcnt(2)\n"
"s_waitcnt vmcnt(8)\n"
"v_mfma_f32_16x16x32_bf16 v[196:199], a[188:191], v[160:163], 0\n"
"buffer_load_dwordx4 v[16:19], v46, s[56:59], s22 offen offset:0 sc1\n"
"v_exp_f32 v207, v207\n"
"v_mfma_f32_16x16x32_bf16 v[196:199], a[192:195], v[164:167], v[196:199]\n"
"s_waitcnt lgkmcnt(0)\n"
"v_cvt_pk_bf16_f32 v215, v206, v207\n"
"v_mfma_f32_16x16x32_bf16 v[196:199], a[196:199], v[168:171], v[196:199]\n"
"s_add_u32 s98, s91, 0\n"
"s_cmp_eq_u64 s[92:93], 0\n"
"s_cselect_b32 s98, s98, 2130706432\n"
"v_mov_b32 v252, s14\n"
"v_mov_b32 v253, s98\n"
"s_mov_b64 exec, 1\n"
"buffer_store_dword v252, v253, s[56:59], 0 offen sc1\n"
"s_mov_b64 exec, -1\n"
"v_mfma_f32_16x16x32_bf16 v[196:199], a[200:203], v[172:175], v[196:199]\n"
"v_exp_f32 v192, v192\n"
"v_mfma_f32_16x16x32_bf16 v[96:99], a[204:207], v[212:215], v[96:99]\n"
"ds_read_b64_tr_b16 a[204:205], v216 offset:57344\n"
"ds_read_b64_tr_b16 a[206:207], v216 offset:61440\n"
"buffer_load_dwordx4 v[20:23], v46, s[56:59], s22 offen offset:16 sc1\n"
"v_mfma_f32_16x16x32_bf16 v[100:103], a[208:211], v[212:215], v[100:103]\n"
"v_exp_f32 v193, v193\n"
"v_mfma_f32_16x16x32_bf16 v[104:107], a[212:215], v[212:215], v[104:107]\n"
"ds_read_b64_tr_b16 a[208:209], v217 offset:57344\n"
"v_cvt_pk_bf16_f32 v208, v192, v193\n"
"v_mfma_f32_16x16x32_bf16 v[108:111], a[216:219], v[212:215], v[108:111]\n"
"v_exp_f32 v194, v194\n"
"v_mfma_f32_16x16x32_bf16 v[112:115], a[220:223], v[212:215], v[112:115]\n"
"ds_read_b64_tr_b16 a[210:211], v217 offset:61440\n"
"ds_read_b64_tr_b16 a[212:213], v218 offset:57344\n"
"buffer_load_dwordx4 v[24:27], v46, s[56:59], s23 offen offset:0 sc1\n"
"v_mfma_f32_16x16x32_bf16 v[116:119], a[224:227], v[212:215], v[116:119]\n"
"v_exp_f32 v195, v195\n"
"v_mfma_f32_16x16x32_bf16 v[120:123], a[228:231], v[212:215], v[120:123]\n"
"ds_read_b64_tr_b16 a[214:215], v218 offset:61440\n"
"v_cvt_pk_bf16_f32 v209, v194, v195\n"
"v_mfma_f32_16x16x32_bf16 v[124:127], a[232:235], v[212:215], v[124:127]\n"
"v_exp_f32 v196, v196\n"
"v_mfma_f32_16x16x32_bf16 a[248:251], a[252:255], v[212:215], a[248:251]\n"
"ds_read_b64_tr_b16 a[216:217], v219 offset:57344\n"
"ds_read_b64_tr_b16 a[218:219], v219 offset:61440\n"
"buffer_load_dwordx4 v[28:31], v46, s[56:59], s23 offen offset:16 sc1\n"
"v_mfma_f32_16x16x32_bf16 v[200:203], a[172:175], v[176:179], 0\n"
"v_exp_f32 v197, v197\n"
"v_mfma_f32_16x16x32_bf16 v[200:203], a[176:179], v[180:183], v[200:203]\n"
"ds_read_b64_tr_b16 a[220:221], v220 offset:57344\n"
"v_cvt_pk_bf16_f32 v210, v196, v197\n"
"v_mfma_f32_16x16x32_bf16 v[200:203], a[180:183], v[184:187], v[200:203]\n"
"v_exp_f32 v198, v198\n"
"v_mfma_f32_16x16x32_bf16 v[200:203], a[184:187], v[188:191], v[200:203]\n"
"ds_read_b64_tr_b16 a[222:223], v220 offset:61440\n"
"ds_read_b64_tr_b16 a[224:225], v221 offset:57344\n"
"buffer_load_dwordx4 v[32:35], v44, s[36:39], s8 offen\n"
"v_mfma_f32_16x16x32_bf16 v[204:207], a[188:191], v[176:179], 0\n"
"v_exp_f32 v199, v199\n"
"v_mfma_f32_16x16x32_bf16 v[204:207], a[192:195], v[180:183], v[204:207]\n"
"ds_read_b64_tr_b16 a[226:227], v221 offset:61440\n"
"v_cvt_pk_bf16_f32 v211, v198, v199\n"
"v_mfma_f32_16x16x32_bf16 v[204:207], a[196:199], v[184:187], v[204:207]\n"
"ds_read_b64_tr_b16 a[228:229], v222 offset:57344\n"
"ds_read_b64_tr_b16 a[230:231], v222 offset:61440\n"
"v_mfma_f32_16x16x32_bf16 v[204:207], a[200:203], v[188:191], v[204:207]\n"
"ds_read_b64_tr_b16 a[232:233], v223 offset:57344\n"
"buffer_load_dwordx4 v[36:39], v44, s[36:39], s96 offen\n"
"v_exp_f32 v200, v200\n"
"s_waitcnt lgkmcnt(11)\n"
"v_mfma_f32_16x16x32_bf16 v[64:67], a[204:207], v[208:211], v[64:67]\n"
"ds_read_b64_tr_b16 a[234:235], v223 offset:61440\n"
"v_exp_f32 v201, v201\n"
"v_mfma_f32_16x16x32_bf16 v[68:71], a[208:211], v[208:211], v[68:71]\n"
"s_waitcnt lgkmcnt(8)\n"
"v_exp_f32 v202, v202\n"
"v_mfma_f32_16x16x32_bf16 v[72:75], a[212:215], v[208:211], v[72:75]\n"
"v_exp_f32 v203, v203\n"
"v_mfma_f32_16x16x32_bf16 v[76:79], a[216:219], v[208:211], v[76:79]\n"
"s_waitcnt lgkmcnt(4)\n"
"buffer_load_dwordx2 v[40:41], v45, s[56:59], s9 offen offset:0 sc1\n"
"v_exp_f32 v204, v204\n"
"v_mfma_f32_16x16x32_bf16 v[80:83], a[220:223], v[208:211], v[80:83]\n"
"v_exp_f32 v205, v205\n"
"v_mfma_f32_16x16x32_bf16 v[84:87], a[224:227], v[208:211], v[84:87]\n"
"s_waitcnt lgkmcnt(0)\n"
"v_exp_f32 v206, v206\n"
"v_mfma_f32_16x16x32_bf16 v[88:91], a[228:231], v[208:211], v[88:91]\n"
"v_exp_f32 v207, v207\n"
"v_mfma_f32_16x16x32_bf16 v[92:95], a[232:235], v[208:211], v[92:95]\n"
"buffer_load_dwordx2 v[42:43], v45, s[56:59], s9 offen offset:256 sc1\n"
"v_cvt_pk_bf16_f32 v212, v200, v201\n"
"v_cvt_pk_bf16_f32 v213, v202, v203\n"
"v_mfma_f32_16x16x32_bf16 a[244:247], a[252:255], v[208:211], a[244:247]\n"
"v_cvt_pk_bf16_f32 v214, v204, v205\n"
"v_cvt_pk_bf16_f32 v215, v206, v207\n"
"s_nop 1\n"
"v_mfma_f32_16x16x32_bf16 v[96:99], a[204:207], v[212:215], v[96:99]\n"
"v_mfma_f32_16x16x32_bf16 v[100:103], a[208:211], v[212:215], v[100:103]\n"
"v_mfma_f32_16x16x32_bf16 v[104:107], a[212:215], v[212:215], v[104:107]\n"
"v_mfma_f32_16x16x32_bf16 v[108:111], a[216:219], v[212:215], v[108:111]\n"
"v_accvgpr_read_b32 v248, a244\n"
"s_add_u32 s99, s88, 8388608\n"
"v_mfma_f32_16x16x32_bf16 v[112:115], a[220:223], v[212:215], v[112:115]\n"
"v_subrev_u32 v249, 0x30800000, v248\n"
"v_rcp_f32 v248, v248\n"
"v_mfma_f32_16x16x32_bf16 v[116:119], a[224:227], v[212:215], v[116:119]\n"
"v_cmp_le_u32 vcc, 0x1e000000, v249\n"
"s_or_b64 s[92:93], s[92:93], vcc\n"
"v_mfma_f32_16x16x32_bf16 v[120:123], a[228:231], v[212:215], v[120:123]\n"
"v_mfma_f32_16x16x32_bf16 v[124:127], a[232:235], v[212:215], v[124:127]\n"
"v_mfma_f32_16x16x32_bf16 a[248:251], a[252:255], v[212:215], a[248:251]\n"
"v_subrev_u32 v238, 65536, v234\n"
"v_subrev_u32 v239, 65536, v235\n"
"v_subrev_u32 v240, 65536, v236\n"
"v_subrev_u32 v241, 65536, v237\n"
"s_nop 3\n"
"v_accvgpr_read_b32 v250, a248\n"
"v_subrev_u32 v251, 0x30800000, v250\n"
"v_rcp_f32 v250, v250\n"
"v_cmp_le_u32 vcc, 0x1e000000, v251\n"
"s_or_b64 s[92:93], s[92:93], vcc\n"
"s_cmp_lg_u64 s[92:93], 0\n"
"s_cbranch_scc1 L_fallback\n"
"L_fb_back:\n"
"s_lshl_b32 s94, s87, 2\n"
"s_bitset0_b32 s94, 8\n"
"s_and_b32 s95, s86, 0x70\n"
"s_add_u32 s94, s94, s95\n"
"s_add_u32 s94, s94, 4096\n"
"s_add_u32 s95, s94, 128\n"
"s_mov_b32 s72, s14\n"
"s_mov_b32 s73, s14\n"
"v_and_b32 v229, 3, v242\n"
"v_lshlrev_b32 v229, 2, v229\n"
"v_and_b32 v228, 4, v242\n"
"v_lshl_add_u32 v229, v228, 6, v229\n"
"v_pk_mul_f32 v[64:65], v[64:65], v[248:249] op_sel_hi:[1,0]\n"
"v_pk_mul_f32 v[66:67], v[66:67], v[248:249] op_sel_hi:[1,0]\n"
"v_pk_mul_f32 v[68:69], v[68:69], v[248:249] op_sel_hi:[1,0]\n"
"v_pk_mul_f32 v[70:71], v[70:71], v[248:249] op_sel_hi:[1,0]\n"
"v_cvt_pk_bf16_f32 v64, v64, v65\n"
"v_cvt_pk_bf16_f32 v65, v66, v67\n"
"v_cvt_pk_bf16_f32 v66, v68, v69\n"
"v_cvt_pk_bf16_f32 v67, v70, v71\n"
"buffer_store_dwordx4 v[64:67], v243, s[56:59], s99 offen offset:0 sc1\n"
"v_pk_mul_f32 v[72:73], v[72:73], v[248:249] op_sel_hi:[1,0]\n"
"v_pk_mul_f32 v[74:75], v[74:75], v[248:249] op_sel_hi:[1,0]\n"
"v_pk_mul_f32 v[76:77], v[76:77], v[248:249] op_sel_hi:[1,0]\n"
"v_pk_mul_f32 v[78:79], v[78:79], v[248:249] op_sel_hi:[1,0]\n"
"v_cvt_pk_bf16_f32 v72, v72, v73\n"
"v_cvt_pk_bf16_f32 v73, v74, v75\n"
"v_cvt_pk_bf16_f32 v74, v76, v77\n"
"v_cvt_pk_bf16_f32 v75, v78, v79\n"
"buffer_store_dwordx4 v[72:75], v243, s[56:59], s99 offen offset:1024 sc1\n"
"v_pk_mul_f32 v[80:81], v[80:81], v[248:249] op_sel_hi:[1,0]\n"
"v_pk_mul_f32 v[82:83], v[82:83], v[248:249] op_sel_hi:[1,0]\n"
"v_pk_mul_f32 v[84:85], v[84:85], v[248:249] op_sel_hi:[1,0]\n"
"v_pk_mul_f32 v[86:87], v[86:87], v[248:249] op_sel_hi:[1,0]\n"
"v_cvt_pk_bf16_f32 v80, v80, v81\n"
"v_cvt_pk_bf16_f32 v81, v82, v83\n"
"v_cvt_pk_bf16_f32 v82, v84, v85\n"
"v_cvt_pk_bf16_f32 v83, v86, v87\n"
"buffer_store_dwordx4 v[80:83], v243, s[56:59], s99 offen offset:2048 sc1\n"
"v_pk_mul_f32 v[88:89], v[88:89], v[248:249] op_sel_hi:[1,0]\n"
"v_pk_mul_f32 v[90:91], v[90:91], v[248:249] op_sel_hi:[1,0]\n"
"v_pk_mul_f32 v[92:93], v[92:93], v[248:249] op_sel_hi:[1,0]\n"
"v_pk_mul_f32 v[94:95], v[94:95], v[248:249] op_sel_hi:[1,0]\n"
"v_cvt_pk_bf16_f32 v88, v88, v89\n"
"v_cvt_pk_bf16_f32 v89, v90, v91\n"
"v_cvt_pk_bf16_f32 v90, v92, v93\n"
"v_cvt_pk_bf16_f32 v91, v94, v95\n"
"buffer_store_dwordx4 v[88:91], v243, s[56:59], s99 offen offset:3072 sc1\n"
"s_add_u32 s99, s88, 8421376\n"
"v_pk_mul_f32 v[96:97], v[96:97], v[250:251] op_sel_hi:[1,0]\n"
"v_pk_mul_f32 v[98:99], v[98:99], v[250:251] op_sel_hi:[1,0]\n"
"v_pk_mul_f32 v[100:101], v[100:101], v[250:251] op_sel_hi:[1,0]\n"
"v_pk_mul_f32 v[102:103], v[102:103], v[250:251] op_sel_hi:[1,0]\n"
"v_cvt_pk_bf16_f32 v96, v96, v97\n"
"v_cvt_pk_bf16_f32 v97, v98, v99\n"
"v_cvt_pk_bf16_f32 v98, v100, v101\n"
"v_cvt_pk_bf16_f32 v99, v102, v103\n"
"buffer_store_dwordx4 v[96:99], v243, s[56:59], s99 offen offset:0 sc1\n"
"v_pk_mul_f32 v[104:105], v[104:105], v[250:251] op_sel_hi:[1,0]\n"
"v_pk_mul_f32 v[106:107], v[106:107], v[250:251] op_sel_hi:[1,0]\n"
"v_pk_mul_f32 v[108:109], v[108:109], v[250:251] op_sel_hi:[1,0]\n"
"v_pk_mul_f32 v[110:111], v[110:111], v[250:251] op_sel_hi:[1,0]\n"
"v_cvt_pk_bf16_f32 v104, v104, v105\n"
"v_cvt_pk_bf16_f32 v105, v106, v107\n"
"v_cvt_pk_bf16_f32 v106, v108, v109\n"
"v_cvt_pk_bf16_f32 v107, v110, v111\n"
"buffer_store_dwordx4 v[104:107], v243, s[56:59], s99 offen offset:1024 sc1\n"
"v_pk_mul_f32 v[112:113], v[112:113], v[250:251] op_sel_hi:[1,0]\n"
"v_pk_mul_f32 v[114:115], v[114:115], v[250:251] op_sel_hi:[1,0]\n"
"v_pk_mul_f32 v[116:117], v[116:117], v[250:251] op_sel_hi:[1,0]\n"
"v_pk_mul_f32 v[118:119], v[118:119], v[250:251] op_sel_hi:[1,0]\n"
"v_cvt_pk_bf16_f32 v112, v112, v113\n"
"v_cvt_pk_bf16_f32 v113, v114, v115\n"
"v_cvt_pk_bf16_f32 v114, v116, v117\n"
"v_cvt_pk_bf16_f32 v115, v118, v119\n"
"buffer_store_dwordx4 v[112:115], v243, s[56:59], s99 offen offset:2048 sc1\n"
"v_pk_mul_f32 v[120:121], v[120:121], v[250:251] op_sel_hi:[1,0]\n"
"v_pk_mul_f32 v[122:123], v[122:123], v[250:251] op_sel_hi:[1,0]\n"
"v_pk_mul_f32 v[124:125], v[124:125], v[250:251] op_sel_hi:[1,0]\n"
"v_pk_mul_f32 v[126:127], v[126:127], v[250:251] op_sel_hi:[1,0]\n"
"v_cvt_pk_bf16_f32 v120, v120, v121\n"
"v_cvt_pk_bf16_f32 v121, v122, v123\n"
"v_cvt_pk_bf16_f32 v122, v124, v125\n"
"v_cvt_pk_bf16_f32 v123, v126, v127\n"
"buffer_store_dwordx4 v[120:123], v243, s[56:59], s99 offen offset:3072 sc1\n"
"buffer_load_dword v228, v229, s[56:59], s94 offen sc1\n"
"ds_read_b64_tr_b16 v[128:129], v234 offset:0\n"
"ds_read_b64_tr_b16 v[130:131], v234 offset:2048\n"
"ds_read_b64_tr_b16 v[132:133], v235 offset:0\n"
"ds_read_b64_tr_b16 v[134:135], v235 offset:2048\n"
"ds_read_b64_tr_b16 v[136:137], v236 offset:0\n"
"ds_read_b64_tr_b16 v[138:139], v236 offset:2048\n"
"ds_read_b64_tr_b16 v[140:141], v237 offset:0\n"
"ds_read_b64_tr_b16 v[142:143], v237 offset:2048\n"
"ds_read_b64_tr_b16 v[144:145], v234 offset:16384\n"
"ds_read_b64_tr_b16 v[146:147], v234 offset:18432\n"
"ds_read_b64_tr_b16 v[148:149], v235 offset:16384\n"
"ds_read_b64_tr_b16 v[150:151], v235 offset:18432\n"
"ds_read_b64_tr_b16 v[152:153], v236 offset:16384\n"
"ds_read_b64_tr_b16 v[154:155], v236 offset:18432\n"
"ds_read_b64_tr_b16 v[156:157], v237 offset:16384\n"
"ds_read_b64_tr_b16 v[158:159], v237 offset:18432\n"
"ds_read_b64_tr_b16 v[160:161], v234 offset:32768\n"
"ds_read_b64_tr_b16 v[162:163], v234 offset:34816\n"
"ds_read_b64_tr_b16 v[164:165], v235 offset:32768\n"
"ds_read_b64_tr_b16 v[166:167], v235 offset:34816\n"
"ds_read_b64_tr_b16 v[168:169], v236 offset:32768\n"
"ds_read_b64_tr_b16 v[170:171], v236 offset:34816\n"
"ds_read_b64_tr_b16 v[172:173], v237 offset:32768\n"
"ds_read_b64_tr_b16 v[174:175], v237 offset:34816\n"
"ds_read_b64_tr_b16 v[176:177], v234 offset:49152\n"
"ds_read_b64_tr_b16 v[178:179], v234 offset:51200\n"
"ds_read_b64_tr_b16 v[180:181], v235 offset:49152\n"
"ds_read_b64_tr_b16 v[182:183], v235 offset:51200\n"
"ds_read_b64_tr_b16 v[184:185], v236 offset:49152\n"
"ds_read_b64_tr_b16 v[186:187], v236 offset:51200\n"
"ds_read_b64_tr_b16 v[188:189], v237 offset:49152\n"
"ds_read_b64_tr_b16 v[190:191], v237 offset:51200\n"
"ds_read_b64_tr_b16 a[160:161], v244 offset:0\n"
"ds_read_b64_tr_b16 a[162:163], v244 offset:2048\n"
"ds_read_b64_tr_b16 a[164:165], v245 offset:0\n"
"ds_read_b64_tr_b16 a[166:167], v245 offset:2048\n"
"ds_read_b64_tr_b16 a[168:169], v246 offset:0\n"
"ds_read_b64_tr_b16 a[170:171], v246 offset:2048\n"
"ds_read_b64_tr_b16 a[172:173], v247 offset:0\n"
"ds_read_b64_tr_b16 a[174:175], v247 offset:2048\n"
"s_lshr_b32 s12, s86, 5\n"
"s_lshl_b32 s12, s12, 11\n"
"s_and_b32 s21, s86, 31\n"
"s_add_u32 s12, s12, s21\n"
"s_add_u32 s12, s12, s87\n"
"s_lshl_b32 s12, s12, 10\n"
"s_add_u32 s12, s12, 1048576\n"
"v_add_u32 v216, 0, v243\n"
"v_add_u32 v217, 32768, v243\n"
"v_add_u32 v218, 8388608, v243\n"
"v_add_u32 v219, 8421376, v243\n"
"s_waitcnt vmcnt(22)\n"
"ds_read_b64_tr_b16 a[176:177], v238 offset:0\n"
"ds_read_b64_tr_b16 a[178:179], v238 offset:2048\n"
"ds_read_b64_tr_b16 a[180:181], v239 offset:0\n"
"ds_read_b64_tr_b16 a[182:183], v239 offset:2048\n"
"ds_read_b64_tr_b16 a[184:185], v240 offset:0\n"
"ds_read_b64_tr_b16 a[186:187], v240 offset:2048\n"
"ds_read_b64_tr_b16 a[188:189], v241 offset:0\n"
"ds_read_b64_tr_b16 a[190:191], v241 offset:2048\n"
"s_waitcnt vmcnt(13)\n"
"v_min3_u32 v252, v1, v3, s14\n"
"v_max3_u32 v253, v1, v3, s14\n"
"v_min3_u32 v252, v252, v5, v7\n"
"v_max3_u32 v253, v253, v5, v7\n"
"v_min3_u32 v252, v252, v9, v11\n"
"v_max3_u32 v253, v253, v9, v11\n"
"v_min3_u32 v252, v252, v13, v15\n"
"v_max3_u32 v253, v253, v13, v15\n"
"v_min3_u32 v252, v252, v17, v19\n"
"v_max3_u32 v253, v253, v17, v19\n"
"v_min3_u32 v252, v252, v21, v23\n"
"v_max3_u32 v253, v253, v21, v23\n"
"v_min3_u32 v252, v252, v25, v27\n"
"v_max3_u32 v253, v253, v25, v27\n"
"v_min3_u32 v252, v252, v29, v31\n"
"v_max3_u32 v253, v253, v29, v31\n"
"v_cmp_ne_u32 vcc, v252, v253\n"
"s_and_b32 vcc_hi, vcc_hi, 0x7ff\n"
"s_mov_b64 vcc, vcc\n"
"s_cbranch_vccnz L_gul_retry\n"
"L_gul_back:\n"
"v_add_f32 v0, v0, v8\n"
"v_add_f32 v16, v16, v24\n"
"v_add_f32 v2, v2, v10\n"
"v_add_f32 v18, v18, v26\n"
"v_add_f32 v4, v4, v12\n"
"v_add_f32 v20, v20, v28\n"
"v_add_f32 v6, v6, v14\n"
"v_add_f32 v22, v22, v30\n"
"v_add_f32 v0, v0, v16\n"
"v_add_f32 v2, v2, v18\n"
"v_add_f32 v4, v4, v20\n"
"v_add_f32 v6, v6, v22\n"
"v_mul_f32 v252, 0xbfb8aa3b, v0\n"
"v_mul_f32 v253, 0xbfb8aa3b, v2\n"
"v_exp_f32 v252, v252\n"
"v_exp_f32 v253, v253\n"
"v_mul_f32 v0, v0, v4\n"
"v_mul_f32 v2, v2, v6\n"
"v_add_f32 v252, 1.0, v252\n"
"v_add_f32 v253, 1.0, v253\n"
"v_rcp_f32 v252, v252\n"
"v_rcp_f32 v253, v253\n"
"v_lshl_add_u32 v250, v242, 1, s1\n"
"v_mul_f32 v0, v0, v252\n"
"v_mul_f32 v2, v2, v253\n"
"v_cvt_pk_bf16_f32 v251, v0, v2\n"
"ds_write_b16 v250, v251 offset:2048\n"
"ds_write_b16_d16_hi v250, v251 offset:2176\n"
"v_and_b32 v252, 1, v242\n"
"v_and_b32 v253, 48, v242\n"
"v_lshl_add_u32 v252, v252, 7, v253\n"
"v_add_u32 v252, s1, v252\n"
"v_lshl_add_u32 v253, v253, 4, s1\n"
"v_and_b32 v254, 15, v242\n"
"v_lshl_add_u32 v253, v254, 3, v253\n"
"ds_read_b128 v[8:11], v252 offset:2048\n"
"ds_read_b128 v[12:15], v252 offset:2112\n"
"ds_read_b64_tr_b16 v[16:17], v253 offset:0\n"
"ds_read_b64_tr_b16 v[18:19], v253 offset:128\n"
"ds_read_b64_tr_b16 v[20:21], v253 offset:1024\n"
"ds_read_b64_tr_b16 v[22:23], v253 offset:1152\n"
"v_lshl_add_u32 v252, v242, 2, s1\n"
"v_subrev_u32 v253, s90, v252\n"
"s_waitcnt lgkmcnt(0)\n"
"v_mfma_f32_16x16x32_bf16 v[0:3], v[16:19], v[8:11], 0\n"
"L_ic8:\n"
"s_cbranch_execz L_ic12\n"
"v_mfma_f32_16x16x32_bf16 v[0:3], v[20:23], v[12:15], v[0:3]\n"
"s_nop 7\n"
"v_cndmask_b32 v0, v0, v1, s[76:77]\n"
"v_cndmask_b32 v2, v2, v3, s[76:77]\n"
"v_cndmask_b32 v0, v0, v2, s[78:79]\n"
"ds_write_b32 v252, v0 offset:3072\n"
"s_waitcnt lgkmcnt(0)\n"
"s_waitcnt vmcnt(9)\n"
"v_xor_b32 v208, s14, v41\n"
"v_xor_b32 v209, s14, v43\n"
"v_or_b32 v208, v208, v209\n"
"v_cmp_ne_u32 vcc, 0, v208\n"
"s_cbranch_vccnz L_ex_retry\n"
"L_ex_back:\n"
"v_and_b32 v208, 0xffff0000, v40\n"
"v_lshlrev_b32 v210, 16, v40\n"
"v_and_b32 v209, 0xffff0000, v42\n"
"v_lshlrev_b32 v211, 16, v42\n"
"v_rcp_f32 v208, v208\n"
"v_rcp_f32 v209, v209\n"
"v_lshlrev_b32 v48, 16, v32\n"
"v_and_b32 v49, 0xffff0000, v32\n"
"v_lshlrev_b32 v50, 16, v33\n"
"v_and_b32 v51, 0xffff0000, v33\n"
"v_lshlrev_b32 v52, 16, v34\n"
"v_and_b32 v53, 0xffff0000, v34\n"
"v_lshlrev_b32 v54, 16, v35\n"
"v_and_b32 v55, 0xffff0000, v35\n"
"v_lshlrev_b32 v56, 16, v36\n"
"v_and_b32 v57, 0xffff0000, v36\n"
"v_lshlrev_b32 v58, 16, v37\n"
"v_and_b32 v59, 0xffff0000, v37\n"
"v_lshlrev_b32 v60, 16, v38\n"
"v_and_b32 v61, 0xffff0000, v38\n"
"v_lshlrev_b32 v62, 16, v39\n"
"v_and_b32 v63, 0xffff0000, v39\n"
"v_sub_f32 v48, v48, v210\n"
"v_sub_f32 v49, v49, v210\n"
"v_sub_f32 v50, v50, v210\n"
"v_sub_f32 v51, v51, v210\n"
"v_sub_f32 v52, v52, v210\n"
"v_sub_f32 v53, v53, v210\n"
"v_sub_f32 v54, v54, v210\n"
"v_sub_f32 v55, v55, v210\n"
"v_sub_f32 v56, v56, v211\n"
"v_sub_f32 v57, v57, v211\n"
"v_sub_f32 v58, v58, v211\n"
"v_sub_f32 v59, v59, v211\n"
"v_sub_f32 v60, v60, v211\n"
"v_sub_f32 v61, v61, v211\n"
"v_sub_f32 v62, v62, v211\n"
"v_sub_f32 v63, v63, v211\n"
"v_mul_f32 v48, v48, v208\n"
"v_mul_f32 v49, v49, v208\n"
"v_mul_f32 v50, v50, v208\n"
"v_mul_f32 v51, v51, v208\n"
"v_mul_f32 v52, v52, v208\n"
"v_mul_f32 v53, v53, v208\n"
"v_mul_f32 v54, v54, v208\n"
"v_mul_f32 v55, v55, v208\n"
"v_mul_f32 v56, v56, v209\n"
"v_mul_f32 v57, v57, v209\n"
"v_mul_f32 v58, v58, v209\n"
"v_mul_f32 v59, v59, v209\n"
"v_mul_f32 v60, v60, v209\n"
"v_mul_f32 v61, v61, v209\n"
"v_mul_f32 v62, v62, v209\n"
"v_mul_f32 v63, v63, v209\n"
"s_waitcnt vmcnt(1)\n"
"s_add_u32 s98, s91, 128\n"
"v_mov_b32 v248, s14\n"
"v_mov_b32 v249, s98\n"
"s_mov_b64 exec, 1\n"
"buffer_store_dword v248, v249, s[56:59], 0 offen sc1\n"
"s_mov_b64 exec, -1\n"
"s_waitcnt vmcnt(1)\n"
"v_cmp_ne_u32 s[64:65], s14, v228\n"
"s_cmp_lg_u64 s[64:65], 0\n"
"s_cbranch_scc1 L_poll0_retry\n"
"L_poll0_back:\n"
"s_xor_b32 s13, s12, 65536\n"
"buffer_load_dwordx4 a[192:195], v216, s[56:59], s13 offen sc1\n"
"s_xor_b32 s15, s12, 66560\n"
"buffer_load_dwordx4 a[196:199], v216, s[56:59], s15 offen sc1\n"
"buffer_load_dwordx4 a[200:203], v217, s[56:59], s13 offen sc1\n"
"buffer_load_dwordx4 a[204:207], v217, s[56:59], s15 offen sc1\n"
"s_xor_b32 s13, s12, 67584\n"
"buffer_load_dwordx4 a[208:211], v216, s[56:59], s13 offen sc1\n"
"s_xor_b32 s15, s12, 68608\n"
"buffer_load_dwordx4 a[212:215], v216, s[56:59], s15 offen sc1\n"
"buffer_load_dwordx4 a[216:219], v217, s[56:59], s13 offen sc1\n"
"buffer_load_dwordx4 a[220:223], v217, s[56:59], s15 offen sc1\n"
"s_xor_b32 s13, s12, 69632\n"
"buffer_load_dwordx4 a[224:227], v216, s[56:59], s13 offen sc1\n"
"s_xor_b32 s15, s12, 70656\n"
"buffer_load_dwordx4 a[228:231], v216, s[56:59], s15 offen sc1\n"
"buffer_load_dwordx4 a[232:235], v217, s[56:59], s13 offen sc1\n"
"buffer_load_dwordx4 a[236:239], v217, s[56:59], s15 offen sc1\n"
"s_xor_b32 s13, s12, 71680\n"
"buffer_load_dwordx4 a[240:243], v216, s[56:59], s13 offen sc1\n"
"s_xor_b32 s15, s12, 72704\n"
"buffer_load_dwordx4 a[244:247], v216, s[56:59], s15 offen sc1\n"
"buffer_load_dwordx4 a[248:251], v217, s[56:59], s13 offen sc1\n"
"buffer_load_dwordx4 a[252:255], v217, s[56:59], s15 offen sc1\n"
"s_waitcnt vmcnt(14)\n"
"v_mfma_f32_16x16x32_bf16 v[64:67], a[0:3], a[192:195], 0\n"
"v_mfma_f32_16x16x32_bf16 v[64:67], a[16:19], a[196:199], v[64:67]\n"
"v_mfma_f32_16x16x32_bf16 v[68:71], a[20:23], a[196:199], 0\n"
"v_mfma_f32_16x16x32_bf16 v[68:71], a[4:7], a[192:195], v[68:71]\n"
"v_mfma_f32_16x16x32_bf16 v[72:75], a[8:11], a[192:195], 0\n"
"s_barrier\n"
"v_mfma_f32_16x16x32_bf16 v[72:75], a[24:27], a[196:199], v[72:75]\n"
"ds_read_b32 v0, v253 offset:3072\n"
"ds_read_b32 v2, v253 offset:7168\n"
"v_mfma_f32_16x16x32_bf16 v[76:79], a[28:31], a[196:199], 0\n"
"v_and_b32 v249, 3, v242\n"
"v_lshlrev_b32 v249, 2, v249\n"
"v_mfma_f32_16x16x32_bf16 v[76:79], a[12:15], a[192:195], v[76:79]\n"
"s_xor_b32 s13, s12, 73728\n"
"v_and_b32 v248, 4, v242\n"
"v_lshl_add_u32 v249, v248, 6, v249\n"
"s_waitcnt vmcnt(12)\n"
"v_mfma_f32_16x16x32_bf16 v[80:83], a[0:3], a[200:203], 0\n"
"buffer_load_dwordx4 a[192:195], v216, s[56:59], s13 offen sc1\n"
"v_mfma_f32_16x16x32_bf16 v[80:83], a[16:19], a[204:207], v[80:83]\n"
"s_xor_b32 s15, s12, 74752\n"
"ds_read_b32 v4, v253 offset:11264\n"
"v_mfma_f32_16x16x32_bf16 v[84:87], a[20:23], a[204:207], 0\n"
"buffer_load_dwordx4 a[196:199], v216, s[56:59], s15 offen sc1\n"
"v_mfma_f32_16x16x32_bf16 v[84:87], a[4:7], a[200:203], v[84:87]\n"
"ds_read_b32 v6, v253 offset:15360\n"
"v_and_b32 v254, 1, v242\n"
"v_mfma_f32_16x16x32_bf16 v[88:91], a[8:11], a[200:203], 0\n"
"v_and_b32 v252, 48, v242\n"
"v_lshl_add_u32 v252, v252, 1, s0\n"
"v_mfma_f32_16x16x32_bf16 v[88:91], a[24:27], a[204:207], v[88:91]\n"
"v_lshl_add_u32 v254, v254, 15, v252\n"
"v_and_b32 v252, 6, v242\n"
"v_mfma_f32_16x16x32_bf16 v[92:95], a[28:31], a[204:207], 0\n"
"v_lshl_add_u32 v254, v252, 2, v254\n"
"v_mov_b32 v1, s14\n"
"v_mfma_f32_16x16x32_bf16 v[92:95], a[12:15], a[200:203], v[92:95]\n"
"buffer_load_dwordx4 a[200:203], v217, s[56:59], s13 offen sc1\n"
"s_waitcnt vmcnt(13)\n"
"v_mfma_f32_16x16x32_bf16 v[64:67], a[32:35], a[208:211], v[64:67]\n"
"s_waitcnt lgkmcnt(0)\n"
"v_add_f32 v0, v0, v2\n"
"v_add_f32 v4, v4, v6\n"
"v_mfma_f32_16x16x32_bf16 v[64:67], a[48:51], a[212:215], v[64:67]\n"
"buffer_load_dwordx4 a[204:207], v217, s[56:59], s15 offen sc1\n"
"v_mfma_f32_16x16x32_bf16 v[68:71], a[52:55], a[212:215], v[68:71]\n"
"v_add_f32 v0, v0, v4\n"
"v_mfma_f32_16x16x32_bf16 v[68:71], a[36:39], a[208:211], v[68:71]\n"
"buffer_store_dwordx2 v[0:1], v254, s[56:59], s33 offen sc1\n"
"v_mfma_f32_16x16x32_bf16 v[72:75], a[40:43], a[208:211], v[72:75]\n"
"s_add_u32 s96, s90, 65536\n"
"v_and_b32 v208, 15, v242\n"
"v_lshrrev_b32 v209, 4, v242\n"
"v_mfma_f32_16x16x32_bf16 v[72:75], a[56:59], a[212:215], v[72:75]\n"
"v_xor_b32 v209, v209, v208\n"
"v_lshl_add_u32 v209, v209, 4, s96\n"
"v_mfma_f32_16x16x32_bf16 v[76:79], a[60:63], a[212:215], v[76:79]\n"
"v_lshl_add_u32 v230, v208, 8, v209\n"
"v_xor_b32 v231, 64, v230\n"
"v_mfma_f32_16x16x32_bf16 v[76:79], a[44:47], a[208:211], v[76:79]\n"
"s_waitcnt vmcnt(13)\n"
"s_xor_b32 s13, s12, 75776\n"
"v_xor_b32 v232, 128, v230\n"
"v_mfma_f32_16x16x32_bf16 v[80:83], a[32:35], a[216:219], v[80:83]\n"
"buffer_load_dwordx4 a[208:211], v216, s[56:59], s13 offen sc1\n"
"v_mfma_f32_16x16x32_bf16 v[80:83], a[48:51], a[220:223], v[80:83]\n"
"s_xor_b32 s15, s12, 76800\n"
"v_xor_b32 v233, 192, v230\n"
"v_mfma_f32_16x16x32_bf16 v[84:87], a[52:55], a[220:223], v[84:87]\n"
"buffer_load_dwordx4 a[212:215], v216, s[56:59], s15 offen sc1\n"
"v_mfma_f32_16x16x32_bf16 v[84:87], a[36:39], a[216:219], v[84:87]\n"
"s_and_b32 s96, s10, 1\n"
"s_lshl_b32 s96, s96, 3\n"
"v_or_b32 v208, s96, v47\n"
"v_mfma_f32_16x16x32_bf16 v[88:91], a[40:43], a[216:219], v[88:91]\n"
"v_and_b32 v209, 7, v242\n"
"v_lshlrev_b32 v209, 1, v209\n"
"v_mfma_f32_16x16x32_bf16 v[88:91], a[56:59], a[220:223], v[88:91]\n"
"v_xor_b32 v209, v209, v208\n"
"v_mfma_f32_16x16x32_bf16 v[92:95], a[60:63], a[220:223], v[92:95]\n"
"s_lshr_b32 s96, s10, 1\n"
"s_add_u32 s96, s96, 4\n"
"s_lshl_b32 s96, s96, 14\n"
"v_mfma_f32_16x16x32_bf16 v[92:95], a[44:47], a[216:219], v[92:95]\n"
"buffer_load_dwordx4 a[216:219], v217, s[56:59], s13 offen sc1\n"
"s_waitcnt vmcnt(14)\n"
"v_mfma_f32_16x16x32_bf16 v[64:67], a[64:67], a[224:227], v[64:67]\n"
"v_lshl_add_u32 v209, v209, 4, s96\n"
"v_lshl_add_u32 v234, v208, 8, v209\n"
"v_mfma_f32_16x16x32_bf16 v[64:67], a[80:83], a[228:231], v[64:67]\n"
"buffer_load_dwordx4 a[220:223], v217, s[56:59], s15 offen sc1\n"
"v_mfma_f32_16x16x32_bf16 v[68:71], a[84:87], a[228:231], v[68:71]\n"
"v_xor_b32 v235, 16, v234\n"
"v_subrev_u32 v236, 65536, v230\n"
"v_mfma_f32_16x16x32_bf16 v[68:71], a[68:71], a[224:227], v[68:71]\n"
"v_subrev_u32 v237, 65536, v231\n"
"v_subrev_u32 v238, 65536, v232\n"
"v_mfma_f32_16x16x32_bf16 v[72:75], a[72:75], a[224:227], v[72:75]\n"
"v_subrev_u32 v239, 65536, v233\n"
"v_subrev_u32 v240, 65536, v234\n"
"v_mfma_f32_16x16x32_bf16 v[72:75], a[88:91], a[228:231], v[72:75]\n"
"v_subrev_u32 v241, 65536, v235\n"
"v_mfma_f32_16x16x32_bf16 v[76:79], a[92:95], a[228:231], v[76:79]\n"
"s_lshl_b32 s17, s11, 14\n"
"s_lshl_b32 s96, s10, 11\n"
"s_add_u32 s17, s17, s96\n"
"v_mfma_f32_16x16x32_bf16 v[76:79], a[76:79], a[224:227], v[76:79]\n"
"s_waitcnt vmcnt(13)\n"
"s_xor_b32 s13, s12, 77824\n"
"s_xor_b32 s18, s17, 16384\n"
"v_mfma_f32_16x16x32_bf16 v[80:83], a[64:67], a[232:235], v[80:83]\n"
"buffer_load_dwordx4 a[224:227], v216, s[56:59], s13 offen sc1\n"
"v_mfma_f32_16x16x32_bf16 v[80:83], a[80:83], a[236:239], v[80:83]\n"
"s_xor_b32 s15, s12, 78848\n"
"s_add_u32 s17, s17, 17825792\n"
"s_add_u32 s18, s18, 17825792\n"
"v_mfma_f32_16x16x32_bf16 v[84:87], a[84:87], a[236:239], v[84:87]\n"
"buffer_load_dwordx4 a[228:231], v216, s[56:59], s15 offen sc1\n"
"v_mfma_f32_16x16x32_bf16 v[84:87], a[68:71], a[232:235], v[84:87]\n"
"v_mfma_f32_16x16x32_bf16 v[88:91], a[72:75], a[232:235], v[88:91]\n"
"v_mfma_f32_16x16x32_bf16 v[88:91], a[88:91], a[236:239], v[88:91]\n"
"v_mfma_f32_16x16x32_bf16 v[92:95], a[92:95], a[236:239], v[92:95]\n"
"v_mfma_f32_16x16x32_bf16 v[92:95], a[76:79], a[232:235], v[92:95]\n"
"buffer_load_dwordx4 a[232:235], v217, s[56:59], s13 offen sc1\n"
"s_waitcnt vmcnt(14)\n"
"v_mfma_f32_16x16x32_bf16 v[64:67], a[96:99], a[240:243], v[64:67]\n"
"v_mfma_f32_16x16x32_bf16 v[64:67], a[112:115], a[244:247], v[64:67]\n"
"buffer_load_dwordx4 a[236:239], v217, s[56:59], s15 offen sc1\n"
"v_mfma_f32_16x16x32_bf16 v[68:71], a[116:119], a[244:247], v[68:71]\n"
"v_mfma_f32_16x16x32_bf16 v[68:71], a[100:103], a[240:243], v[68:71]\n"
"v_mfma_f32_16x16x32_bf16 v[72:75], a[104:107], a[240:243], v[72:75]\n"
"v_mfma_f32_16x16x32_bf16 v[72:75], a[120:123], a[244:247], v[72:75]\n"
"v_mfma_f32_16x16x32_bf16 v[76:79], a[124:127], a[244:247], v[76:79]\n"
"v_mfma_f32_16x16x32_bf16 v[76:79], a[108:111], a[240:243], v[76:79]\n"
"s_waitcnt vmcnt(13)\n"
"s_xor_b32 s13, s12, 79872\n"
"v_mfma_f32_16x16x32_bf16 v[80:83], a[96:99], a[248:251], v[80:83]\n"
"buffer_load_dwordx4 a[240:243], v216, s[56:59], s13 offen sc1\n"
"v_mfma_f32_16x16x32_bf16 v[80:83], a[112:115], a[252:255], v[80:83]\n"
"s_xor_b32 s15, s12, 80896\n"
"v_mfma_f32_16x16x32_bf16 v[84:87], a[116:119], a[252:255], v[84:87]\n"
"buffer_load_dwordx4 a[244:247], v216, s[56:59], s15 offen sc1\n"
"v_mfma_f32_16x16x32_bf16 v[84:87], a[100:103], a[248:251], v[84:87]\n"
"v_mfma_f32_16x16x32_bf16 v[88:91], a[104:107], a[248:251], v[88:91]\n"
"v_mfma_f32_16x16x32_bf16 v[88:91], a[120:123], a[252:255], v[88:91]\n"
"v_mfma_f32_16x16x32_bf16 v[92:95], a[124:127], a[252:255], v[92:95]\n"
"v_mfma_f32_16x16x32_bf16 v[92:95], a[108:111], a[248:251], v[92:95]\n"
"buffer_load_dwordx4 a[248:251], v217, s[56:59], s13 offen sc1\n"
"s_waitcnt vmcnt(14)\n"
"v_mfma_f32_16x16x32_bf16 v[64:67], a[128:131], a[192:195], v[64:67]\n"
"v_mfma_f32_16x16x32_bf16 v[64:67], a[144:147], a[196:199], v[64:67]\n"
"buffer_load_dwordx4 a[252:255], v217, s[56:59], s15 offen sc1\n"
"v_mfma_f32_16x16x32_bf16 v[68:71], a[148:151], a[196:199], v[68:71]\n"
"v_mfma_f32_16x16x32_bf16 v[68:71], a[132:135], a[192:195], v[68:71]\n"
"v_mfma_f32_16x16x32_bf16 v[72:75], a[136:139], a[192:195], v[72:75]\n"
"v_mfma_f32_16x16x32_bf16 v[72:75], a[152:155], a[196:199], v[72:75]\n"
"v_mfma_f32_16x16x32_bf16 v[76:79], a[156:159], a[196:199], v[76:79]\n"
"v_mfma_f32_16x16x32_bf16 v[76:79], a[140:143], a[192:195], v[76:79]\n"
"s_waitcnt vmcnt(13)\n"
"s_xor_b32 s13, s12, 0\n"
"v_mfma_f32_16x16x32_bf16 v[80:83], a[128:131], a[200:203], v[80:83]\n"
"buffer_load_dwordx4 a[192:195], v216, s[56:59], s13 offen sc1\n"
"v_mfma_f32_16x16x32_bf16 v[80:83], a[144:147], a[204:207], v[80:83]\n"
"s_xor_b32 s15, s12, 1024\n"
"v_mfma_f32_16x16x32_bf16 v[84:87], a[148:151], a[204:207], v[84:87]\n"
"buffer_load_dwordx4 a[196:199], v216, s[56:59], s15 offen sc1\n"
"v_mfma_f32_16x16x32_bf16 v[84:87], a[132:135], a[200:203], v[84:87]\n"
"v_mfma_f32_16x16x32_bf16 v[88:91], a[136:139], a[200:203], v[88:91]\n"
"v_mfma_f32_16x16x32_bf16 v[88:91], a[152:155], a[204:207], v[88:91]\n"
"v_mfma_f32_16x16x32_bf16 v[92:95], a[156:159], a[204:207], v[92:95]\n"
"v_mfma_f32_16x16x32_bf16 v[92:95], a[140:143], a[200:203], v[92:95]\n"
"buffer_load_dwordx4 a[200:203], v217, s[56:59], s13 offen sc1\n"
"s_waitcnt vmcnt(13)\n"
"v_mfma_f32_16x16x32_bf16 v[64:67], v[128:131], a[208:211], v[64:67]\n"
"v_mfma_f32_16x16x32_bf16 v[64:67], v[144:147], a[212:215], v[64:67]\n"
"buffer_load_dwordx4 a[204:207], v217, s[56:59], s15 offen sc1\n"
"v_mfma_f32_16x16x32_bf16 v[68:71], v[148:151], a[212:215], v[68:71]\n"
"v_mfma_f32_16x16x32_bf16 v[68:71], v[132:135], a[208:211], v[68:71]\n"
"v_mfma_f32_16x16x32_bf16 v[72:75], v[136:139], a[208:211], v[72:75]\n"
"v_mfma_f32_16x16x32_bf16 v[72:75], v[152:155], a[212:215], v[72:75]\n"
"v_mfma_f32_16x16x32_bf16 v[76:79], v[156:159], a[212:215], v[76:79]\n"
"v_mfma_f32_16x16x32_bf16 v[76:79], v[140:143], a[208:211], v[76:79]\n"
"s_waitcnt vmcnt(12)\n"
"s_xor_b32 s13, s12, 2048\n"
"v_mfma_f32_16x16x32_bf16 v[80:83], v[128:131], a[216:219], v[80:83]\n"
"buffer_load_dwordx4 a[208:211], v216, s[56:59], s13 offen sc1\n"
"v_mfma_f32_16x16x32_bf16 v[80:83], v[144:147], a[220:223], v[80:83]\n"
"s_xor_b32 s15, s12, 3072\n"
"v_mfma_f32_16x16x32_bf16 v[84:87], v[148:151], a[220:223], v[84:87]\n"
"buffer_load_dwordx4 a[212:215], v216, s[56:59], s15 offen sc1\n"
"v_mfma_f32_16x16x32_bf16 v[84:87], v[132:135], a[216:219], v[84:87]\n"
"v_mfma_f32_16x16x32_bf16 v[88:91], v[136:139], a[216:219], v[88:91]\n"
"v_mfma_f32_16x16x32_bf16 v[88:91], v[152:155], a[220:223], v[88:91]\n"
"v_mfma_f32_16x16x32_bf16 v[92:95], v[156:159], a[220:223], v[92:95]\n"
"v_mfma_f32_16x16x32_bf16 v[92:95], v[140:143], a[216:219], v[92:95]\n"
"buffer_load_dwordx4 a[216:219], v217, s[56:59], s13 offen sc1\n"
"s_waitcnt vmcnt(13)\n"
"v_mfma_f32_16x16x32_bf16 v[64:67], v[160:163], a[224:227], v[64:67]\n"
"v_mfma_f32_16x16x32_bf16 v[64:67], v[176:179], a[228:231], v[64:67]\n"
"buffer_load_dwordx4 a[220:223], v217, s[56:59], s15 offen sc1\n"
"v_mfma_f32_16x16x32_bf16 v[68:71], v[180:183], a[228:231], v[68:71]\n"
"v_mfma_f32_16x16x32_bf16 v[68:71], v[164:167], a[224:227], v[68:71]\n"
"v_mfma_f32_16x16x32_bf16 v[72:75], v[168:171], a[224:227], v[72:75]\n"
"v_mfma_f32_16x16x32_bf16 v[72:75], v[184:187], a[228:231], v[72:75]\n"
"v_mfma_f32_16x16x32_bf16 v[76:79], v[188:191], a[228:231], v[76:79]\n"
"v_mfma_f32_16x16x32_bf16 v[76:79], v[172:175], a[224:227], v[76:79]\n"
"s_waitcnt vmcnt(12)\n"
"s_xor_b32 s13, s12, 4096\n"
"v_mfma_f32_16x16x32_bf16 v[80:83], v[160:163], a[232:235], v[80:83]\n"
"buffer_load_dwordx4 a[224:227], v216, s[56:59], s13 offen sc1\n"
"v_mfma_f32_16x16x32_bf16 v[80:83], v[176:179], a[236:239], v[80:83]\n"
"s_xor_b32 s15, s12, 5120\n"
"v_mfma_f32_16x16x32_bf16 v[84:87], v[180:183], a[236:239], v[84:87]\n"
"buffer_load_dwordx4 a[228:231], v216, s[56:59], s15 offen sc1\n"
"v_mfma_f32_16x16x32_bf16 v[84:87], v[164:167], a[232:235], v[84:87]\n"
"v_mfma_f32_16x16x32_bf16 v[88:91], v[168:171], a[232:235], v[88:91]\n"
"v_mfma_f32_16x16x32_bf16 v[88:91], v[184:187], a[236:239], v[88:91]\n"
"v_mfma_f32_16x16x32_bf16 v[92:95], v[188:191], a[236:239], v[92:95]\n"
"v_mfma_f32_16x16x32_bf16 v[92:95], v[172:175], a[232:235], v[92:95]\n"
"buffer_load_dword v248, v249, s[56:59], s95 offen sc1\n"
"s_waitcnt vmcnt(13)\n"
"v_mfma_f32_16x16x32_bf16 v[64:67], a[160:163], a[240:243], v[64:67]\n"
"v_mfma_f32_16x16x32_bf16 v[64:67], a[176:179], a[244:247], v[64:67]\n"
"buffer_load_dwordx4 a[232:235], v217, s[56:59], s13 offen sc1\n"
"v_mfma_f32_16x16x32_bf16 v[68:71], a[180:183], a[244:247], v[68:71]\n"
"v_mfma_f32_16x16x32_bf16 v[68:71], a[164:167], a[240:243], v[68:71]\n"
"buffer_load_dwordx4 a[236:239], v217, s[56:59], s15 offen sc1\n"
"v_mfma_f32_16x16x32_bf16 v[72:75], a[168:171], a[240:243], v[72:75]\n"
"v_mfma_f32_16x16x32_bf16 v[72:75], a[184:187], a[244:247], v[72:75]\n"
"v_mfma_f32_16x16x32_bf16 v[76:79], a[188:191], a[244:247], v[76:79]\n"
"v_mfma_f32_16x16x32_bf16 v[76:79], a[172:175], a[240:243], v[76:79]\n"
"s_waitcnt vmcnt(13)\n"
"s_xor_b32 s13, s12, 6144\n"
"v_mfma_f32_16x16x32_bf16 v[80:83], a[160:163], a[248:251], v[80:83]\n"
"buffer_load_dwordx4 a[240:243], v216, s[56:59], s13 offen sc1\n"
"v_mfma_f32_16x16x32_bf16 v[80:83], a[176:179], a[252:255], v[80:83]\n"
"s_xor_b32 s15, s12, 7168\n"
"v_mfma_f32_16x16x32_bf16 v[84:87], a[180:183], a[252:255], v[84:87]\n"
"buffer_load_dwordx4 a[244:247], v216, s[56:59], s15 offen sc1\n"
"v_mfma_f32_16x16x32_bf16 v[84:87], a[164:167], a[248:251], v[84:87]\n"
"v_mfma_f32_16x16x32_bf16 v[88:91], a[168:171], a[248:251], v[88:91]\n"
"v_mfma_f32_16x16x32_bf16 v[88:91], a[184:187], a[252:255], v[88:91]\n"
"v_mfma_f32_16x16x32_bf16 v[92:95], a[188:191], a[252:255], v[92:95]\n"
"v_mfma_f32_16x16x32_bf16 v[92:95], a[172:175], a[248:251], v[92:95]\n"
"buffer_load_dwordx4 a[248:251], v217, s[56:59], s13 offen sc1\n"
"s_waitcnt vmcnt(14)\n"
"v_mfma_f32_16x16x32_bf16 v[96:99], a[0:3], a[192:195], 0\n"
"ds_write_b128 v230, v[64:67] offset:0\n"
"v_mfma_f32_16x16x32_bf16 v[96:99], a[16:19], a[196:199], v[96:99]\n"
"buffer_load_dwordx4 a[252:255], v217, s[56:59], s15 offen sc1\n"
"v_mfma_f32_16x16x32_bf16 v[100:103], a[20:23], a[196:199], 0\n"
"ds_write_b128 v231, v[68:71] offset:0\n"
"v_mfma_f32_16x16x32_bf16 v[100:103], a[4:7], a[192:195], v[100:103]\n"
"v_mfma_f32_16x16x32_bf16 v[104:107], a[8:11], a[192:195], 0\n"
"ds_write_b128 v232, v[72:75] offset:0\n"
"v_mfma_f32_16x16x32_bf16 v[104:107], a[24:27], a[196:199], v[104:107]\n"
"v_mfma_f32_16x16x32_bf16 v[108:111], a[28:31], a[196:199], 0\n"
"ds_write_b128 v233, v[76:79] offset:0\n"
"v_mfma_f32_16x16x32_bf16 v[108:111], a[12:15], a[192:195], v[108:111]\n"
"s_waitcnt vmcnt(13)\n"
"s_waitcnt vmcnt(6)\n"
"v_cmp_ne_u32 s[64:65], s14, v248\n"
"v_mfma_f32_16x16x32_bf16 v[112:115], a[0:3], a[200:203], 0\n"
"ds_write_b128 v230, v[80:83] offset:16384\n"
"v_mfma_f32_16x16x32_bf16 v[112:115], a[16:19], a[204:207], v[112:115]\n"
"s_cmp_lg_u64 s[64:65], 0\n"
"s_cbranch_scc1 L_poll1_retry\n"
"L_poll1_back:\n"
"v_mfma_f32_16x16x32_bf16 v[116:119], a[20:23], a[204:207], 0\n"
"s_xor_b32 s13, s12, 65536\n"
"ds_write_b128 v231, v[84:87] offset:16384\n"
"v_mfma_f32_16x16x32_bf16 v[116:119], a[4:7], a[200:203], v[116:119]\n"
"buffer_load_dwordx4 a[192:195], v218, s[56:59], s13 offen sc1\n"
"v_mfma_f32_16x16x32_bf16 v[120:123], a[8:11], a[200:203], 0\n"
"s_xor_b32 s15, s12, 66560\n"
"ds_write_b128 v232, v[88:91] offset:16384\n"
"v_mfma_f32_16x16x32_bf16 v[120:123], a[24:27], a[204:207], v[120:123]\n"
"buffer_load_dwordx4 a[196:199], v218, s[56:59], s15 offen sc1\n"
"v_mfma_f32_16x16x32_bf16 v[124:127], a[28:31], a[204:207], 0\n"
"ds_write_b128 v233, v[92:95] offset:16384\n"
"v_mfma_f32_16x16x32_bf16 v[124:127], a[12:15], a[200:203], v[124:127]\n"
"buffer_load_dwordx4 a[200:203], v219, s[56:59], s13 offen sc1\n"
"v_mfma_f32_16x16x32_bf16 v[96:99], a[32:35], a[208:211], v[96:99]\n"
"v_mfma_f32_16x16x32_bf16 v[96:99], a[48:51], a[212:215], v[96:99]\n"
"buffer_load_dwordx4 a[204:207], v219, s[56:59], s15 offen sc1\n"
"v_mfma_f32_16x16x32_bf16 v[100:103], a[52:55], a[212:215], v[100:103]\n"
"v_mfma_f32_16x16x32_bf16 v[100:103], a[36:39], a[208:211], v[100:103]\n"
"v_mfma_f32_16x16x32_bf16 v[104:107], a[40:43], a[208:211], v[104:107]\n"
"v_mfma_f32_16x16x32_bf16 v[104:107], a[56:59], a[212:215], v[104:107]\n"
"v_mfma_f32_16x16x32_bf16 v[108:111], a[60:63], a[212:215], v[108:111]\n"
"v_mfma_f32_16x16x32_bf16 v[108:111], a[44:47], a[208:211], v[108:111]\n"
"s_xor_b32 s13, s12, 67584\n"
"v_mfma_f32_16x16x32_bf16 v[112:115], a[32:35], a[216:219], v[112:115]\n"
"buffer_load_dwordx4 a[208:211], v218, s[56:59], s13 offen sc1\n"
"v_mfma_f32_16x16x32_bf16 v[112:115], a[48:51], a[220:223], v[112:115]\n"
"s_xor_b32 s15, s12, 68608\n"
"v_mfma_f32_16x16x32_bf16 v[116:119], a[52:55], a[220:223], v[116:119]\n"
"buffer_load_dwordx4 a[212:215], v218, s[56:59], s15 offen sc1\n"
"v_mfma_f32_16x16x32_bf16 v[116:119], a[36:39], a[216:219], v[116:119]\n"
"s_waitcnt lgkmcnt(0)\n"
"v_mfma_f32_16x16x32_bf16 v[120:123], a[40:43], a[216:219], v[120:123]\n"
"s_barrier\n"
"v_mfma_f32_16x16x32_bf16 v[120:123], a[56:59], a[220:223], v[120:123]\n"
"ds_read_b128 v[0:3], v234 offset:0\n"
"ds_read_b128 v[4:7], v234 offset:4096\n"
"v_mfma_f32_16x16x32_bf16 v[124:127], a[60:63], a[220:223], v[124:127]\n"
"ds_read_b128 v[8:11], v234 offset:8192\n"
"ds_read_b128 v[12:15], v234 offset:12288\n"
"v_mfma_f32_16x16x32_bf16 v[124:127], a[44:47], a[216:219], v[124:127]\n"
"buffer_load_dwordx4 a[216:219], v219, s[56:59], s13 offen sc1\n"
"v_mfma_f32_16x16x32_bf16 v[96:99], a[64:67], a[224:227], v[96:99]\n"
"ds_read_b128 v[16:19], v235 offset:0\n"
"ds_read_b128 v[20:23], v235 offset:4096\n"
"v_mfma_f32_16x16x32_bf16 v[96:99], a[80:83], a[228:231], v[96:99]\n"
"buffer_load_dwordx4 a[220:223], v219, s[56:59], s15 offen sc1\n"
"v_mfma_f32_16x16x32_bf16 v[100:103], a[84:87], a[228:231], v[100:103]\n"
"ds_read_b128 v[24:27], v235 offset:8192\n"
"ds_read_b128 v[28:31], v235 offset:12288\n"
"v_mfma_f32_16x16x32_bf16 v[100:103], a[68:71], a[224:227], v[100:103]\n"
"v_mfma_f32_16x16x32_bf16 v[104:107], a[72:75], a[224:227], v[104:107]\n"
"v_mfma_f32_16x16x32_bf16 v[104:107], a[88:91], a[228:231], v[104:107]\n"
"s_waitcnt lgkmcnt(4)\n"
"v_add_f32 v0, v0, v4\n"
"v_add_f32 v8, v8, v12\n"
"v_mfma_f32_16x16x32_bf16 v[108:111], a[92:95], a[228:231], v[108:111]\n"
"v_add_f32 v1, v1, v5\n"
"v_add_f32 v9, v9, v13\n"
"v_mfma_f32_16x16x32_bf16 v[108:111], a[76:79], a[224:227], v[108:111]\n"
"s_waitcnt vmcnt(12)\n"
"s_xor_b32 s13, s12, 69632\n"
"v_add_f32 v2, v2, v6\n"
"v_mfma_f32_16x16x32_bf16 v[112:115], a[64:67], a[232:235], v[112:115]\n"
"buffer_load_dwordx4 a[224:227], v218, s[56:59], s13 offen sc1\n"
"v_mfma_f32_16x16x32_bf16 v[112:115], a[80:83], a[236:239], v[112:115]\n"
"s_xor_b32 s15, s12, 70656\n"
"v_add_f32 v10, v10, v14\n"
"v_add_f32 v3, v3, v7\n"
"v_mfma_f32_16x16x32_bf16 v[116:119], a[84:87], a[236:239], v[116:119]\n"
"buffer_load_dwordx4 a[228:231], v218, s[56:59], s15 offen sc1\n"
"v_mfma_f32_16x16x32_bf16 v[116:119], a[68:71], a[232:235], v[116:119]\n"
"v_add_f32 v11, v11, v15\n"
"v_add_f32 v0, v0, v8\n"
"v_mfma_f32_16x16x32_bf16 v[120:123], a[72:75], a[232:235], v[120:123]\n"
"v_add_f32 v1, v1, v9\n"
"v_add_f32 v2, v2, v10\n"
"v_mfma_f32_16x16x32_bf16 v[120:123], a[88:91], a[236:239], v[120:123]\n"
"v_add_f32 v3, v3, v11\n"
"v_mfma_f32_16x16x32_bf16 v[124:127], a[92:95], a[236:239], v[124:127]\n"
"s_waitcnt lgkmcnt(0)\n"
"v_add_f32 v16, v16, v20\n"
"v_add_f32 v24, v24, v28\n"
"v_mfma_f32_16x16x32_bf16 v[124:127], a[76:79], a[232:235], v[124:127]\n"
"buffer_load_dwordx4 a[232:235], v219, s[56:59], s13 offen sc1\n"
"s_waitcnt vmcnt(13)\n"
"v_mfma_f32_16x16x32_bf16 v[96:99], a[96:99], a[240:243], v[96:99]\n"
"v_add_f32 v17, v17, v21\n"
"v_add_f32 v25, v25, v29\n"
"v_mfma_f32_16x16x32_bf16 v[96:99], a[112:115], a[244:247], v[96:99]\n"
"buffer_load_dwordx4 a[236:239], v219, s[56:59], s15 offen sc1\n"
"v_mfma_f32_16x16x32_bf16 v[100:103], a[116:119], a[244:247], v[100:103]\n"
"v_add_f32 v18, v18, v22\n"
"v_add_f32 v26, v26, v30\n"
"v_mfma_f32_16x16x32_bf16 v[100:103], a[100:103], a[240:243], v[100:103]\n"
"v_add_f32 v19, v19, v23\n"
"v_add_f32 v27, v27, v31\n"
"v_mfma_f32_16x16x32_bf16 v[104:107], a[104:107], a[240:243], v[104:107]\n"
"v_add_f32 v16, v16, v24\n"
"v_add_f32 v17, v17, v25\n"
"v_mfma_f32_16x16x32_bf16 v[104:107], a[120:123], a[244:247], v[104:107]\n"
"v_add_f32 v18, v18, v26\n"
"v_add_f32 v19, v19, v27\n"
"v_mfma_f32_16x16x32_bf16 v[108:111], a[124:127], a[244:247], v[108:111]\n"
"s_add_u32 s96, s17, 0\n"
"v_cvt_pk_bf16_f32 v4, v0, v1\n"
"v_cvt_pk_bf16_f32 v6, v2, v3\n"
"v_mfma_f32_16x16x32_bf16 v[108:111], a[108:111], a[240:243], v[108:111]\n"
"s_waitcnt vmcnt(12)\n"
"s_xor_b32 s13, s12, 71680\n"
"v_mov_b32 v5, s14\n"
"v_mfma_f32_16x16x32_bf16 v[112:115], a[96:99], a[248:251], v[112:115]\n"
"buffer_load_dwordx4 a[240:243], v218, s[56:59], s13 offen sc1\n"
"v_mfma_f32_16x16x32_bf16 v[112:115], a[112:115], a[252:255], v[112:115]\n"
"s_xor_b32 s15, s12, 72704\n"
"v_mov_b32 v7, s14\n"
"v_mfma_f32_16x16x32_bf16 v[116:119], a[116:119], a[252:255], v[116:119]\n"
"buffer_load_dwordx4 a[244:247], v218, s[56:59], s15 offen sc1\n"
"v_mfma_f32_16x16x32_bf16 v[116:119], a[100:103], a[248:251], v[116:119]\n"
"buffer_store_dwordx4 v[4:7], v243, s[56:59], s96 offen offset:0 sc1\n"
"v_mfma_f32_16x16x32_bf16 v[120:123], a[104:107], a[248:251], v[120:123]\n"
"v_cvt_pk_bf16_f32 v20, v16, v17\n"
"v_cvt_pk_bf16_f32 v22, v18, v19\n"
"v_mfma_f32_16x16x32_bf16 v[120:123], a[120:123], a[252:255], v[120:123]\n"
"v_mov_b32 v21, s14\n"
"v_mov_b32 v23, s14\n"
"v_mfma_f32_16x16x32_bf16 v[124:127], a[124:127], a[252:255], v[124:127]\n"
"buffer_store_dwordx4 v[20:23], v243, s[56:59], s96 offen offset:1024 sc1\n"
"v_mfma_f32_16x16x32_bf16 v[124:127], a[108:111], a[248:251], v[124:127]\n"
"buffer_load_dwordx4 a[248:251], v219, s[56:59], s13 offen sc1\n"
"s_waitcnt vmcnt(15)\n"
"v_mfma_f32_16x16x32_bf16 v[64:67], a[0:3], a[192:195], 0\n"
"s_xor_b32 s99, s82, s97\n"
"s_and_b32 s99, s99, 0xffffffd8\n"
"s_xor_b32 s96, s83, s97\n"
"v_mfma_f32_16x16x32_bf16 v[64:67], a[16:19], a[196:199], v[64:67]\n"
"buffer_load_dwordx4 a[252:255], v219, s[56:59], s15 offen sc1\n"
"v_mfma_f32_16x16x32_bf16 v[68:71], a[20:23], a[196:199], 0\n"
"s_and_b32 s96, s96, 0xffffffd8\n"
"s_min_u32 s99, s99, s96\n"
"v_mfma_f32_16x16x32_bf16 v[68:71], a[4:7], a[192:195], v[68:71]\n"
"s_cmp_eq_u32 s99, 0\n"
"s_cbranch_scc1 L_yload\n"
"L_yload_back:\n"
"v_mfma_f32_16x16x32_bf16 v[72:75], a[8:11], a[192:195], 0\n"
"v_mfma_f32_16x16x32_bf16 v[72:75], a[24:27], a[196:199], v[72:75]\n"
"v_mfma_f32_16x16x32_bf16 v[76:79], a[28:31], a[196:199], 0\n"
"v_mfma_f32_16x16x32_bf16 v[76:79], a[12:15], a[192:195], v[76:79]\n"
"s_waitcnt vmcnt(14)\n"
"s_xor_b32 s13, s12, 73728\n"
"v_mfma_f32_16x16x32_bf16 v[80:83], a[0:3], a[200:203], 0\n"
"buffer_load_dwordx4 a[192:195], v218, s[56:59], s13 offen sc1\n"
"v_mfma_f32_16x16x32_bf16 v[80:83], a[16:19], a[204:207], v[80:83]\n"
"s_xor_b32 s15, s12, 74752\n"
"v_mfma_f32_16x16x32_bf16 v[84:87], a[20:23], a[204:207], 0\n"
"buffer_load_dwordx4 a[196:199], v218, s[56:59], s15 offen sc1\n"
"v_mfma_f32_16x16x32_bf16 v[84:87], a[4:7], a[200:203], v[84:87]\n"
"v_mfma_f32_16x16x32_bf16 v[88:91], a[8:11], a[200:203], 0\n"
"v_mfma_f32_16x16x32_bf16 v[88:91], a[24:27], a[204:207], v[88:91]\n"
"v_mfma_f32_16x16x32_bf16 v[92:95], a[28:31], a[204:207], 0\n"
"v_mfma_f32_16x16x32_bf16 v[92:95], a[12:15], a[200:203], v[92:95]\n"
"buffer_load_dwordx4 a[200:203], v219, s[56:59], s13 offen sc1\n"
"s_waitcnt vmcnt(15)\n"
"v_mfma_f32_16x16x32_bf16 v[64:67], a[32:35], a[208:211], v[64:67]\n"
"v_mfma_f32_16x16x32_bf16 v[64:67], a[48:51], a[212:215], v[64:67]\n"
"buffer_load_dwordx4 a[204:207], v219, s[56:59], s15 offen sc1\n"
"v_mfma_f32_16x16x32_bf16 v[68:71], a[52:55], a[212:215], v[68:71]\n"
"v_mfma_f32_16x16x32_bf16 v[68:71], a[36:39], a[208:211], v[68:71]\n"
"v_mfma_f32_16x16x32_bf16 v[72:75], a[40:43], a[208:211], v[72:75]\n"
"v_mfma_f32_16x16x32_bf16 v[72:75], a[56:59], a[212:215], v[72:75]\n"
"v_mfma_f32_16x16x32_bf16 v[76:79], a[60:63], a[212:215], v[76:79]\n"
"v_mfma_f32_16x16x32_bf16 v[76:79], a[44:47], a[208:211], v[76:79]\n"
"s_waitcnt vmcnt(14)\n"
"s_xor_b32 s13, s12, 75776\n"
"v_mfma_f32_16x16x32_bf16 v[80:83], a[32:35], a[216:219], v[80:83]\n"
"buffer_load_dwordx4 a[208:211], v218, s[56:59], s13 offen sc1\n"
"v_mfma_f32_16x16x32_bf16 v[80:83], a[48:51], a[220:223], v[80:83]\n"
"s_xor_b32 s15, s12, 76800\n"
"v_mfma_f32_16x16x32_bf16 v[84:87], a[52:55], a[220:223], v[84:87]\n"
"buffer_load_dwordx4 a[212:215], v218, s[56:59], s15 offen sc1\n"
"v_mfma_f32_16x16x32_bf16 v[84:87], a[36:39], a[216:219], v[84:87]\n"
"v_mfma_f32_16x16x32_bf16 v[88:91], a[40:43], a[216:219], v[88:91]\n"
"v_mfma_f32_16x16x32_bf16 v[88:91], a[56:59], a[220:223], v[88:91]\n"
"v_mfma_f32_16x16x32_bf16 v[92:95], a[60:63], a[220:223], v[92:95]\n"
"v_mfma_f32_16x16x32_bf16 v[92:95], a[44:47], a[216:219], v[92:95]\n"
"buffer_load_dwordx4 a[216:219], v219, s[56:59], s13 offen sc1\n"
"s_waitcnt vmcnt(15)\n"
"v_mfma_f32_16x16x32_bf16 v[64:67], a[64:67], a[224:227], v[64:67]\n"
"v_mfma_f32_16x16x32_bf16 v[64:67], a[80:83], a[228:231], v[64:67]\n"
"buffer_load_dwordx4 a[220:223], v219, s[56:59], s15 offen sc1\n"
"v_mfma_f32_16x16x32_bf16 v[68:71], a[84:87], a[228:231], v[68:71]\n"
"v_mfma_f32_16x16x32_bf16 v[68:71], a[68:71], a[224:227], v[68:71]\n"
"v_mfma_f32_16x16x32_bf16 v[72:75], a[72:75], a[224:227], v[72:75]\n"
"v_mfma_f32_16x16x32_bf16 v[72:75], a[88:91], a[228:231], v[72:75]\n"
"v_mfma_f32_16x16x32_bf16 v[76:79], a[92:95], a[228:231], v[76:79]\n"
"v_mfma_f32_16x16x32_bf16 v[76:79], a[76:79], a[224:227], v[76:79]\n"
"s_waitcnt vmcnt(14)\n"
"s_xor_b32 s13, s12, 77824\n"
"v_mfma_f32_16x16x32_bf16 v[80:83], a[64:67], a[232:235], v[80:83]\n"
"buffer_load_dwordx4 a[224:227], v218, s[56:59], s13 offen sc1\n"
"v_mfma_f32_16x16x32_bf16 v[80:83], a[80:83], a[236:239], v[80:83]\n"
"s_xor_b32 s15, s12, 78848\n"
"v_mfma_f32_16x16x32_bf16 v[84:87], a[84:87], a[236:239], v[84:87]\n"
"buffer_load_dwordx4 a[228:231], v218, s[56:59], s15 offen sc1\n"
"v_mfma_f32_16x16x32_bf16 v[84:87], a[68:71], a[232:235], v[84:87]\n"
"v_mfma_f32_16x16x32_bf16 v[88:91], a[72:75], a[232:235], v[88:91]\n"
"v_mfma_f32_16x16x32_bf16 v[88:91], a[88:91], a[236:239], v[88:91]\n"
"v_mfma_f32_16x16x32_bf16 v[92:95], a[92:95], a[236:239], v[92:95]\n"
"v_mfma_f32_16x16x32_bf16 v[92:95], a[76:79], a[232:235], v[92:95]\n"
"buffer_load_dwordx4 a[232:235], v219, s[56:59], s13 offen sc1\n"
"s_waitcnt vmcnt(15)\n"
"v_mfma_f32_16x16x32_bf16 v[64:67], a[96:99], a[240:243], v[64:67]\n"
"v_mfma_f32_16x16x32_bf16 v[64:67], a[112:115], a[244:247], v[64:67]\n"
"buffer_load_dwordx4 a[236:239], v219, s[56:59], s15 offen sc1\n"
"v_mfma_f32_16x16x32_bf16 v[68:71], a[116:119], a[244:247], v[68:71]\n"
"v_mfma_f32_16x16x32_bf16 v[68:71], a[100:103], a[240:243], v[68:71]\n"
"v_mfma_f32_16x16x32_bf16 v[72:75], a[104:107], a[240:243], v[72:75]\n"
"v_mfma_f32_16x16x32_bf16 v[72:75], a[120:123], a[244:247], v[72:75]\n"
"v_mfma_f32_16x16x32_bf16 v[76:79], a[124:127], a[244:247], v[76:79]\n"
"v_mfma_f32_16x16x32_bf16 v[76:79], a[108:111], a[240:243], v[76:79]\n"
"s_waitcnt vmcnt(12)\n"
"s_xor_b32 s13, s12, 79872\n"
"L_ic9:\n"
"s_cbranch_execz L_ic_ret\n"
"v_mfma_f32_16x16x32_bf16 v[80:83], a[96:99], a[248:251], v[80:83]\n"
"buffer_load_dwordx4 a[240:243], v218, s[56:59], s13 offen sc1\n"
"v_mfma_f32_16x16x32_bf16 v[80:83], a[112:115], a[252:255], v[80:83]\n"
"s_xor_b32 s15, s12, 80896\n"
"v_mfma_f32_16x16x32_bf16 v[84:87], a[116:119], a[252:255], v[84:87]\n"
"buffer_load_dwordx4 a[244:247], v218, s[56:59], s15 offen sc1\n"
"v_mfma_f32_16x16x32_bf16 v[84:87], a[100:103], a[248:251], v[84:87]\n"
"v_mfma_f32_16x16x32_bf16 v[88:91], a[104:107], a[248:251], v[88:91]\n"
"v_mfma_f32_16x16x32_bf16 v[88:91], a[120:123], a[252:255], v[88:91]\n"
"v_mfma_f32_16x16x32_bf16 v[92:95], a[124:127], a[252:255], v[92:95]\n"
"v_mfma_f32_16x16x32_bf16 v[92:95], a[108:111], a[248:251], v[92:95]\n"
"buffer_load_dwordx4 a[248:251], v219, s[56:59], s13 offen sc1\n"
"s_waitcnt vmcnt(13)\n"
"v_mfma_f32_16x16x32_bf16 v[64:67], a[128:131], a[192:195], v[64:67]\n"
"v_mfma_f32_16x16x32_bf16 v[64:67], a[144:147], a[196:199], v[64:67]\n"
"buffer_load_dwordx4 a[252:255], v219, s[56:59], s15 offen sc1\n"
"v_mfma_f32_16x16x32_bf16 v[68:71], a[148:151], a[196:199], v[68:71]\n"
"v_mfma_f32_16x16x32_bf16 v[68:71], a[132:135], a[192:195], v[68:71]\n"
"v_mfma_f32_16x16x32_bf16 v[72:75], a[136:139], a[192:195], v[72:75]\n"
"v_mfma_f32_16x16x32_bf16 v[72:75], a[152:155], a[196:199], v[72:75]\n"
"v_mfma_f32_16x16x32_bf16 v[76:79], a[156:159], a[196:199], v[76:79]\n"
"v_mfma_f32_16x16x32_bf16 v[76:79], a[140:143], a[192:195], v[76:79]\n"
"s_waitcnt vmcnt(12)\n"
"s_xor_b32 s13, s12, 8192\n"
"v_mfma_f32_16x16x32_bf16 v[80:83], a[128:131], a[200:203], v[80:83]\n"
"buffer_load_dwordx4 a[192:195], v216, s[56:59], s13 offen sc1\n"
"v_mfma_f32_16x16x32_bf16 v[80:83], a[144:147], a[204:207], v[80:83]\n"
"s_xor_b32 s15, s12, 9216\n"
"v_mfma_f32_16x16x32_bf16 v[84:87], a[148:151], a[204:207], v[84:87]\n"
"buffer_load_dwordx4 a[196:199], v216, s[56:59], s15 offen sc1\n"
"v_mfma_f32_16x16x32_bf16 v[84:87], a[132:135], a[200:203], v[84:87]\n"
"v_mfma_f32_16x16x32_bf16 v[88:91], a[136:139], a[200:203], v[88:91]\n"
"v_mfma_f32_16x16x32_bf16 v[88:91], a[152:155], a[204:207], v[88:91]\n"
"s_cmp_eq_u32 s99, 0\n"
"s_cbranch_scc1 L_yuse\n"
"L_yuse_back:\n"
"v_mul_f32 v192, 0xbfb8aa3b, v48\n"
"v_mfma_f32_16x16x32_bf16 v[92:95], a[156:159], a[204:207], v[92:95]\n"
"v_mul_f32 v193, 0xbfb8aa3b, v49\n"
"v_mul_f32 v194, 0xbfb8aa3b, v50\n"
"v_mfma_f32_16x16x32_bf16 v[92:95], a[140:143], a[200:203], v[92:95]\n"
"buffer_load_dwordx4 a[200:203], v217, s[56:59], s13 offen sc1\n"
"s_waitcnt vmcnt(13)\n"
"v_mfma_f32_16x16x32_bf16 v[64:67], v[128:131], a[208:211], v[64:67]\n"
"v_mul_f32 v195, 0xbfb8aa3b, v51\n"
"v_mul_f32 v196, 0xbfb8aa3b, v52\n"
"v_mfma_f32_16x16x32_bf16 v[64:67], v[144:147], a[212:215], v[64:67]\n"
"buffer_load_dwordx4 a[204:207], v217, s[56:59], s15 offen sc1\n"
"v_mfma_f32_16x16x32_bf16 v[68:71], v[148:151], a[212:215], v[68:71]\n"
"v_mul_f32 v197, 0xbfb8aa3b, v53\n"
"v_mul_f32 v198, 0xbfb8aa3b, v54\n"
"v_mfma_f32_16x16x32_bf16 v[68:71], v[132:135], a[208:211], v[68:71]\n"
"v_mul_f32 v199, 0xbfb8aa3b, v55\n"
"v_mul_f32 v200, 0xbfb8aa3b, v56\n"
"v_mfma_f32_16x16x32_bf16 v[72:75], v[136:139], a[208:211], v[72:75]\n"
"v_mul_f32 v201, 0xbfb8aa3b, v57\n"
"v_mul_f32 v202, 0xbfb8aa3b, v58\n"
"v_mfma_f32_16x16x32_bf16 v[72:75], v[152:155], a[212:215], v[72:75]\n"
"v_mul_f32 v203, 0xbfb8aa3b, v59\n"
"v_mul_f32 v204, 0xbfb8aa3b, v60\n"
"v_mfma_f32_16x16x32_bf16 v[76:79], v[156:159], a[212:215], v[76:79]\n"
"v_mul_f32 v205, 0xbfb8aa3b, v61\n"
"v_mul_f32 v206, 0xbfb8aa3b, v62\n"
"v_mfma_f32_16x16x32_bf16 v[76:79], v[140:143], a[208:211], v[76:79]\n"
"s_waitcnt vmcnt(12)\n"
"s_xor_b32 s13, s12, 10240\n"
"v_mul_f32 v207, 0xbfb8aa3b, v63\n"
"v_mfma_f32_16x16x32_bf16 v[80:83], v[128:131], a[216:219], v[80:83]\n"
"buffer_load_dwordx4 a[208:211], v216, s[56:59], s13 offen sc1\n"
"v_mfma_f32_16x16x32_bf16 v[80:83], v[144:147], a[220:223], v[80:83]\n"
"s_xor_b32 s15, s12, 11264\n"
"v_exp_f32 v192, v192\n"
"v_mfma_f32_16x16x32_bf16 v[84:87], v[148:151], a[220:223], v[84:87]\n"
"buffer_load_dwordx4 a[212:215], v216, s[56:59], s15 offen sc1\n"
"v_mfma_f32_16x16x32_bf16 v[84:87], v[132:135], a[216:219], v[84:87]\n"
"v_exp_f32 v193, v193\n"
"v_mfma_f32_16x16x32_bf16 v[88:91], v[136:139], a[216:219], v[88:91]\n"
"v_exp_f32 v194, v194\n"
"v_mfma_f32_16x16x32_bf16 v[88:91], v[152:155], a[220:223], v[88:91]\n"
"v_exp_f32 v195, v195\n"
"v_mfma_f32_16x16x32_bf16 v[92:95], v[156:159], a[220:223], v[92:95]\n"
"v_exp_f32 v196, v196\n"
"v_mfma_f32_16x16x32_bf16 v[92:95], v[140:143], a[216:219], v[92:95]\n"
"buffer_load_dwordx4 a[216:219], v217, s[56:59], s13 offen sc1\n"
"s_waitcnt vmcnt(13)\n"
"v_mfma_f32_16x16x32_bf16 v[64:67], v[160:163], a[224:227], v[64:67]\n"
"v_exp_f32 v197, v197\n"
"v_mfma_f32_16x16x32_bf16 v[64:67], v[176:179], a[228:231], v[64:67]\n"
"buffer_load_dwordx4 a[220:223], v217, s[56:59], s15 offen sc1\n"
"v_mfma_f32_16x16x32_bf16 v[68:71], v[180:183], a[228:231], v[68:71]\n"
"v_exp_f32 v198, v198\n"
"v_mfma_f32_16x16x32_bf16 v[68:71], v[164:167], a[224:227], v[68:71]\n"
"v_exp_f32 v199, v199\n"
"v_mfma_f32_16x16x32_bf16 v[72:75], v[168:171], a[224:227], v[72:75]\n"
"v_exp_f32 v200, v200\n"
"v_mfma_f32_16x16x32_bf16 v[72:75], v[184:187], a[228:231], v[72:75]\n"
"v_exp_f32 v201, v201\n"
"v_mfma_f32_16x16x32_bf16 v[76:79], v[188:191], a[228:231], v[76:79]\n"
"v_exp_f32 v202, v202\n"
"v_mfma_f32_16x16x32_bf16 v[76:79], v[172:175], a[224:227], v[76:79]\n"
"s_waitcnt vmcnt(12)\n"
"s_xor_b32 s13, s12, 12288\n"
"v_mfma_f32_16x16x32_bf16 v[80:83], v[160:163], a[232:235], v[80:83]\n"
"buffer_load_dwordx4 a[224:227], v216, s[56:59], s13 offen sc1\n"
"v_mfma_f32_16x16x32_bf16 v[80:83], v[176:179], a[236:239], v[80:83]\n"
"s_xor_b32 s15, s12, 13312\n"
"v_exp_f32 v203, v203\n"
"v_mfma_f32_16x16x32_bf16 v[84:87], v[180:183], a[236:239], v[84:87]\n"
"buffer_load_dwordx4 a[228:231], v216, s[56:59], s15 offen sc1\n"
"v_mfma_f32_16x16x32_bf16 v[84:87], v[164:167], a[232:235], v[84:87]\n"
"v_exp_f32 v204, v204\n"
"v_mfma_f32_16x16x32_bf16 v[88:91], v[168:171], a[232:235], v[88:91]\n"
"v_exp_f32 v205, v205\n"
"v_mfma_f32_16x16x32_bf16 v[88:91], v[184:187], a[236:239], v[88:91]\n"
"v_exp_f32 v206, v206\n"
"v_mfma_f32_16x16x32_bf16 v[92:95], v[188:191], a[236:239], v[92:95]\n"
"v_exp_f32 v207, v207\n"
"v_mfma_f32_16x16x32_bf16 v[92:95], v[172:175], a[232:235], v[92:95]\n"
"buffer_load_dwordx4 a[232:235], v217, s[56:59], s13 offen sc1\n"
"s_waitcnt vmcnt(13)\n"
"v_mfma_f32_16x16x32_bf16 v[64:67], a[160:163], a[240:243], v[64:67]\n"
"s_mov_b32 s19, s18\n"
"buffer_load_dwordx4 v[220:223], v243, s[56:59], s19 offen offset:0 sc1\n"
"buffer_load_dwordx4 v[224:227], v243, s[56:59], s19 offen offset:1024 sc1\n"
"v_add_f32 v192, 1.0, v192\n"
"v_add_f32 v193, 1.0, v193\n"
"v_mfma_f32_16x16x32_bf16 v[64:67], a[176:179], a[244:247], v[64:67]\n"
"buffer_load_dwordx4 a[236:239], v217, s[56:59], s15 offen sc1\n"
"v_mfma_f32_16x16x32_bf16 v[68:71], a[180:183], a[244:247], v[68:71]\n"
"v_add_f32 v194, 1.0, v194\n"
"v_add_f32 v195, 1.0, v195\n"
"v_mfma_f32_16x16x32_bf16 v[68:71], a[164:167], a[240:243], v[68:71]\n"
"v_add_f32 v196, 1.0, v196\n"
"v_add_f32 v197, 1.0, v197\n"
"v_mfma_f32_16x16x32_bf16 v[72:75], a[168:171], a[240:243], v[72:75]\n"
"v_add_f32 v198, 1.0, v198\n"
"v_add_f32 v199, 1.0, v199\n"
"v_mfma_f32_16x16x32_bf16 v[72:75], a[184:187], a[244:247], v[72:75]\n"
"v_add_f32 v200, 1.0, v200\n"
"v_add_f32 v201, 1.0, v201\n"
"v_mfma_f32_16x16x32_bf16 v[76:79], a[188:191], a[244:247], v[76:79]\n"
"v_add_f32 v202, 1.0, v202\n"
"v_add_f32 v203, 1.0, v203\n"
"v_mfma_f32_16x16x32_bf16 v[76:79], a[172:175], a[240:243], v[76:79]\n"
"s_waitcnt vmcnt(14)\n"
"s_xor_b32 s13, s12, 14336\n"
"v_add_f32 v204, 1.0, v204\n"
"v_mfma_f32_16x16x32_bf16 v[80:83], a[160:163], a[248:251], v[80:83]\n"
"buffer_load_dwordx4 a[240:243], v216, s[56:59], s13 offen sc1\n"
"v_mfma_f32_16x16x32_bf16 v[80:83], a[176:179], a[252:255], v[80:83]\n"
"s_xor_b32 s15, s12, 15360\n"
"v_add_f32 v205, 1.0, v205\n"
"v_add_f32 v206, 1.0, v206\n"
"v_mfma_f32_16x16x32_bf16 v[84:87], a[180:183], a[252:255], v[84:87]\n"
"buffer_load_dwordx4 a[244:247], v216, s[56:59], s15 offen sc1\n"
"v_mfma_f32_16x16x32_bf16 v[84:87], a[164:167], a[248:251], v[84:87]\n"
"v_add_f32 v207, 1.0, v207\n"
"v_mfma_f32_16x16x32_bf16 v[88:91], a[168:171], a[248:251], v[88:91]\n"
"v_rcp_f32 v192, v192\n"
"v_mfma_f32_16x16x32_bf16 v[88:91], a[184:187], a[252:255], v[88:91]\n"
"v_rcp_f32 v193, v193\n"
"v_mfma_f32_16x16x32_bf16 v[92:95], a[188:191], a[252:255], v[92:95]\n"
"v_rcp_f32 v194, v194\n"
"v_mfma_f32_16x16x32_bf16 v[92:95], a[172:175], a[248:251], v[92:95]\n"
"buffer_load_dwordx4 a[248:251], v217, s[56:59], s13 offen sc1\n"
"s_waitcnt vmcnt(15)\n"
"v_mfma_f32_16x16x32_bf16 v[96:99], a[128:131], a[192:195], v[96:99]\n"
"ds_write_b128 v230, v[64:67] offset:32768\n"
"v_rcp_f32 v195, v195\n"
"v_mfma_f32_16x16x32_bf16 v[96:99], a[144:147], a[196:199], v[96:99]\n"
"buffer_load_dwordx4 a[252:255], v217, s[56:59], s15 offen sc1\n"
"v_mfma_f32_16x16x32_bf16 v[100:103], a[148:151], a[196:199], v[100:103]\n"
"ds_write_b128 v231, v[68:71] offset:32768\n"
"v_rcp_f32 v196, v196\n"
"v_mfma_f32_16x16x32_bf16 v[100:103], a[132:135], a[192:195], v[100:103]\n"
"v_rcp_f32 v197, v197\n"
"v_mfma_f32_16x16x32_bf16 v[104:107], a[136:139], a[192:195], v[104:107]\n"
"ds_write_b128 v232, v[72:75] offset:32768\n"
"v_rcp_f32 v198, v198\n"
"v_mfma_f32_16x16x32_bf16 v[104:107], a[152:155], a[196:199], v[104:107]\n"
"v_rcp_f32 v199, v199\n"
"v_mfma_f32_16x16x32_bf16 v[108:111], a[156:159], a[196:199], v[108:111]\n"
"v_rcp_f32 v200, v200\n"
"v_mfma_f32_16x16x32_bf16 v[108:111], a[140:143], a[192:195], v[108:111]\n"
"s_waitcnt vmcnt(14)\n"
"s_xor_b32 s13, s12, 0\n"
"ds_write_b128 v233, v[76:79] offset:32768\n"
"v_mfma_f32_16x16x32_bf16 v[112:115], a[128:131], a[200:203], v[112:115]\n"
"buffer_load_dwordx4 a[192:195], v218, s[56:59], s13 offen sc1\n"
"v_mfma_f32_16x16x32_bf16 v[112:115], a[144:147], a[204:207], v[112:115]\n"
"s_xor_b32 s15, s12, 1024\n"
"v_rcp_f32 v201, v201\n"
"v_mfma_f32_16x16x32_bf16 v[116:119], a[148:151], a[204:207], v[116:119]\n"
"buffer_load_dwordx4 a[196:199], v218, s[56:59], s15 offen sc1\n"
"ds_write_b128 v230, v[80:83] offset:49152\n"
"v_mfma_f32_16x16x32_bf16 v[116:119], a[132:135], a[200:203], v[116:119]\n"
"v_rcp_f32 v202, v202\n"
"v_mfma_f32_16x16x32_bf16 v[120:123], a[136:139], a[200:203], v[120:123]\n"
"ds_write_b128 v231, v[84:87] offset:49152\n"
"v_rcp_f32 v203, v203\n"
"v_mfma_f32_16x16x32_bf16 v[120:123], a[152:155], a[204:207], v[120:123]\n"
"v_rcp_f32 v204, v204\n"
"v_mfma_f32_16x16x32_bf16 v[124:127], a[156:159], a[204:207], v[124:127]\n"
"ds_write_b128 v232, v[88:91] offset:49152\n"
"v_rcp_f32 v205, v205\n"
"v_mfma_f32_16x16x32_bf16 v[124:127], a[140:143], a[200:203], v[124:127]\n"
"buffer_load_dwordx4 a[200:203], v219, s[56:59], s13 offen sc1\n"
"s_waitcnt vmcnt(15)\n"
"v_mfma_f32_16x16x32_bf16 v[96:99], v[128:131], a[208:211], v[96:99]\n"
"ds_write_b128 v233, v[92:95] offset:49152\n"
"v_rcp_f32 v206, v206\n"
"v_mfma_f32_16x16x32_bf16 v[96:99], v[144:147], a[212:215], v[96:99]\n"
"buffer_load_dwordx4 a[204:207], v219, s[56:59], s15 offen sc1\n"
"v_mfma_f32_16x16x32_bf16 v[100:103], v[148:151], a[212:215], v[100:103]\n"
"v_rcp_f32 v207, v207\n"
"v_mfma_f32_16x16x32_bf16 v[100:103], v[132:135], a[208:211], v[100:103]\n"
"v_mul_f32 v48, v48, v192\n"
"v_mul_f32 v49, v49, v193\n"
"v_mfma_f32_16x16x32_bf16 v[104:107], v[136:139], a[208:211], v[104:107]\n"
"v_mul_f32 v50, v50, v194\n"
"v_mul_f32 v51, v51, v195\n"
"v_mfma_f32_16x16x32_bf16 v[104:107], v[152:155], a[212:215], v[104:107]\n"
"v_mul_f32 v52, v52, v196\n"
"v_mul_f32 v53, v53, v197\n"
"v_mfma_f32_16x16x32_bf16 v[108:111], v[156:159], a[212:215], v[108:111]\n"
"v_mul_f32 v54, v54, v198\n"
"v_mul_f32 v55, v55, v199\n"
"v_mfma_f32_16x16x32_bf16 v[108:111], v[140:143], a[208:211], v[108:111]\n"
"s_waitcnt vmcnt(14)\n"
"s_xor_b32 s13, s12, 2048\n"
"v_mul_f32 v56, v56, v200\n"
"v_mfma_f32_16x16x32_bf16 v[112:115], v[128:131], a[216:219], v[112:115]\n"
"buffer_load_dwordx4 a[208:211], v218, s[56:59], s13 offen sc1\n"
"v_mfma_f32_16x16x32_bf16 v[112:115], v[144:147], a[220:223], v[112:115]\n"
"s_xor_b32 s15, s12, 3072\n"
"v_mul_f32 v57, v57, v201\n"
"v_mul_f32 v58, v58, v202\n"
"v_mfma_f32_16x16x32_bf16 v[116:119], v[148:151], a[220:223], v[116:119]\n"
"buffer_load_dwordx4 a[212:215], v218, s[56:59], s15 offen sc1\n"
"v_mfma_f32_16x16x32_bf16 v[116:119], v[132:135], a[216:219], v[116:119]\n"
"s_waitcnt lgkmcnt(0)\n"
"v_mul_f32 v59, v59, v203\n"
"v_mul_f32 v60, v60, v204\n"
"v_mfma_f32_16x16x32_bf16 v[120:123], v[136:139], a[216:219], v[120:123]\n"
"s_barrier\n"
"v_mfma_f32_16x16x32_bf16 v[120:123], v[152:155], a[220:223], v[120:123]\n"
"v_mul_f32 v61, v61, v205\n"
"v_mul_f32 v62, v62, v206\n"
"v_mfma_f32_16x16x32_bf16 v[124:127], v[156:159], a[220:223], v[124:127]\n"
"ds_read_b128 v[0:3], v234 offset:32768\n"
"ds_read_b128 v[4:7], v234 offset:36864\n"
"v_mul_f32 v63, v63, v207\n"
"v_mfma_f32_16x16x32_bf16 v[124:127], v[140:143], a[216:219], v[124:127]\n"
"buffer_load_dwordx4 a[216:219], v219, s[56:59], s13 offen sc1\n"
"s_waitcnt vmcnt(15)\n"
"v_mfma_f32_16x16x32_bf16 v[96:99], v[160:163], a[224:227], v[96:99]\n"
"ds_read_b128 v[8:11], v234 offset:40960\n"
"ds_read_b128 v[12:15], v234 offset:45056\n"
"v_mfma_f32_16x16x32_bf16 v[96:99], v[176:179], a[228:231], v[96:99]\n"
"buffer_load_dwordx4 a[220:223], v219, s[56:59], s15 offen sc1\n"
"v_mfma_f32_16x16x32_bf16 v[100:103], v[180:183], a[228:231], v[100:103]\n"
"ds_read_b128 v[16:19], v235 offset:32768\n"
"ds_read_b128 v[20:23], v235 offset:36864\n"
"v_mfma_f32_16x16x32_bf16 v[100:103], v[164:167], a[224:227], v[100:103]\n"
"ds_read_b128 v[24:27], v235 offset:40960\n"
"ds_read_b128 v[28:31], v235 offset:45056\n"
"v_mfma_f32_16x16x32_bf16 v[104:107], v[168:171], a[224:227], v[104:107]\n"
"v_mfma_f32_16x16x32_bf16 v[104:107], v[184:187], a[228:231], v[104:107]\n"
"v_mfma_f32_16x16x32_bf16 v[108:111], v[188:191], a[228:231], v[108:111]\n"
"s_waitcnt lgkmcnt(4)\n"
"v_add_f32 v0, v0, v4\n"
"v_add_f32 v8, v8, v12\n"
"v_mfma_f32_16x16x32_bf16 v[108:111], v[172:175], a[224:227], v[108:111]\n"
"s_waitcnt vmcnt(12)\n"
"s_xor_b32 s13, s12, 4096\n"
"v_add_f32 v1, v1, v5\n"
"v_add_f32 v9, v9, v13\n"
"v_mfma_f32_16x16x32_bf16 v[112:115], v[160:163], a[232:235], v[112:115]\n"
"buffer_load_dwordx4 a[224:227], v218, s[56:59], s13 offen sc1\n"
"v_mfma_f32_16x16x32_bf16 v[112:115], v[176:179], a[236:239], v[112:115]\n"
"s_xor_b32 s15, s12, 5120\n"
"v_add_f32 v2, v2, v6\n"
"v_add_f32 v10, v10, v14\n"
"v_add_f32 v3, v3, v7\n"
"v_mfma_f32_16x16x32_bf16 v[116:119], v[180:183], a[236:239], v[116:119]\n"
"buffer_load_dwordx4 a[228:231], v218, s[56:59], s15 offen sc1\n"
"v_mfma_f32_16x16x32_bf16 v[116:119], v[164:167], a[232:235], v[116:119]\n"
"v_add_f32 v11, v11, v15\n"
"v_add_f32 v0, v0, v8\n"
"v_mfma_f32_16x16x32_bf16 v[120:123], v[168:171], a[232:235], v[120:123]\n"
"v_add_f32 v1, v1, v9\n"
"v_add_f32 v2, v2, v10\n"
"v_mfma_f32_16x16x32_bf16 v[120:123], v[184:187], a[236:239], v[120:123]\n"
"v_add_f32 v3, v3, v11\n"
"v_mfma_f32_16x16x32_bf16 v[124:127], v[188:191], a[236:239], v[124:127]\n"
"s_waitcnt lgkmcnt(0)\n"
"v_add_f32 v16, v16, v20\n"
"v_add_f32 v24, v24, v28\n"
"v_mfma_f32_16x16x32_bf16 v[124:127], v[172:175], a[232:235], v[124:127]\n"
"buffer_load_dwordx4 a[232:235], v219, s[56:59], s13 offen sc1\n"
"s_waitcnt vmcnt(13)\n"
"v_mfma_f32_16x16x32_bf16 v[96:99], a[160:163], a[240:243], v[96:99]\n"
"v_add_f32 v17, v17, v21\n"
"v_add_f32 v25, v25, v29\n"
"v_mfma_f32_16x16x32_bf16 v[96:99], a[176:179], a[244:247], v[96:99]\n"
"buffer_load_dwordx4 a[236:239], v219, s[56:59], s15 offen sc1\n"
"v_mfma_f32_16x16x32_bf16 v[100:103], a[180:183], a[244:247], v[100:103]\n"
"v_add_f32 v18, v18, v22\n"
"v_add_f32 v26, v26, v30\n"
"v_mfma_f32_16x16x32_bf16 v[100:103], a[164:167], a[240:243], v[100:103]\n"
"v_add_f32 v19, v19, v23\n"
"v_add_f32 v27, v27, v31\n"
"v_mfma_f32_16x16x32_bf16 v[104:107], a[168:171], a[240:243], v[104:107]\n"
"v_add_f32 v16, v16, v24\n"
"v_add_f32 v17, v17, v25\n"
"v_mfma_f32_16x16x32_bf16 v[104:107], a[184:187], a[244:247], v[104:107]\n"
"v_add_f32 v18, v18, v26\n"
"v_add_f32 v19, v19, v27\n"
"v_mfma_f32_16x16x32_bf16 v[108:111], a[188:191], a[244:247], v[108:111]\n"
"s_add_u32 s96, s17, 8192\n"
"v_cvt_pk_bf16_f32 v4, v0, v1\n"
"v_cvt_pk_bf16_f32 v6, v2, v3\n"
"v_mfma_f32_16x16x32_bf16 v[108:111], a[172:175], a[240:243], v[108:111]\n"
"s_waitcnt vmcnt(12)\n"
"s_xor_b32 s13, s12, 6144\n"
"v_mov_b32 v5, s14\n"
"v_mfma_f32_16x16x32_bf16 v[112:115], a[160:163], a[248:251], v[112:115]\n"
"buffer_load_dwordx4 a[240:243], v218, s[56:59], s13 offen sc1\n"
"v_mfma_f32_16x16x32_bf16 v[112:115], a[176:179], a[252:255], v[112:115]\n"
"s_xor_b32 s15, s12, 7168\n"
"v_mov_b32 v7, s14\n"
"v_lshlrev_b32 v200, 16, v32\n"
"v_mfma_f32_16x16x32_bf16 v[116:119], a[180:183], a[252:255], v[116:119]\n"
"buffer_load_dwordx4 a[244:247], v218, s[56:59], s15 offen sc1\n"
"v_mfma_f32_16x16x32_bf16 v[116:119], a[164:167], a[248:251], v[116:119]\n"
"buffer_store_dwordx4 v[4:7], v243, s[56:59], s96 offen offset:0 sc1\n"
"v_mfma_f32_16x16x32_bf16 v[120:123], a[168:171], a[248:251], v[120:123]\n"
"v_cvt_pk_bf16_f32 v20, v16, v17\n"
"v_cvt_pk_bf16_f32 v22, v18, v19\n"
"v_mfma_f32_16x16x32_bf16 v[120:123], a[184:187], a[252:255], v[120:123]\n"
"v_mov_b32 v21, s14\n"
"v_mov_b32 v23, s14\n"
"v_mfma_f32_16x16x32_bf16 v[124:127], a[188:191], a[252:255], v[124:127]\n"
"buffer_store_dwordx4 v[20:23], v243, s[56:59], s96 offen offset:1024 sc1\n"
"v_mfma_f32_16x16x32_bf16 v[124:127], a[172:175], a[248:251], v[124:127]\n"
"buffer_load_dwordx4 a[248:251], v219, s[56:59], s13 offen sc1\n"
"s_waitcnt vmcnt(15)\n"
"v_mfma_f32_16x16x32_bf16 v[64:67], a[0:3], a[192:195], 0\n"
"ds_write_b128 v236, v[96:99] offset:0\n"
"v_and_b32 v201, 0xffff0000, v32\n"
"v_lshlrev_b32 v202, 16, v33\n"
"v_mfma_f32_16x16x32_bf16 v[64:67], a[16:19], a[196:199], v[64:67]\n"
"buffer_load_dwordx4 a[252:255], v219, s[56:59], s15 offen sc1\n"
"v_mfma_f32_16x16x32_bf16 v[68:71], a[20:23], a[196:199], 0\n"
"ds_write_b128 v237, v[100:103] offset:0\n"
"v_and_b32 v203, 0xffff0000, v33\n"
"v_lshlrev_b32 v204, 16, v34\n"
"v_mfma_f32_16x16x32_bf16 v[68:71], a[4:7], a[192:195], v[68:71]\n"
"v_and_b32 v205, 0xffff0000, v34\n"
"v_lshlrev_b32 v206, 16, v35\n"
"v_mfma_f32_16x16x32_bf16 v[72:75], a[8:11], a[192:195], 0\n"
"ds_write_b128 v238, v[104:107] offset:0\n"
"v_and_b32 v207, 0xffff0000, v35\n"
"v_mfma_f32_16x16x32_bf16 v[72:75], a[24:27], a[196:199], v[72:75]\n"
"v_min3_u32 v208, v221, v223, s14\n"
"v_max3_u32 v209, v221, v223, s14\n"
"v_mfma_f32_16x16x32_bf16 v[76:79], a[28:31], a[196:199], 0\n"
"ds_write_b128 v239, v[108:111] offset:0\n"
"v_min3_u32 v208, v208, v225, v227\n"
"v_max3_u32 v209, v209, v225, v227\n"
"v_mfma_f32_16x16x32_bf16 v[76:79], a[12:15], a[192:195], v[76:79]\n"
"s_waitcnt vmcnt(14)\n"
"s_xor_b32 s13, s12, 8192\n"
"v_cmp_ne_u32 vcc, v208, v209\n"
"v_mfma_f32_16x16x32_bf16 v[80:83], a[0:3], a[200:203], 0\n"
"buffer_load_dwordx4 a[192:195], v218, s[56:59], s13 offen sc1\n"
"v_mfma_f32_16x16x32_bf16 v[80:83], a[16:19], a[204:207], v[80:83]\n"
"s_xor_b32 s15, s12, 9216\n"
"ds_write_b128 v236, v[112:115] offset:16384\n"
"v_mfma_f32_16x16x32_bf16 v[84:87], a[20:23], a[204:207], 0\n"
"buffer_load_dwordx4 a[196:199], v218, s[56:59], s15 offen sc1\n"
"v_mfma_f32_16x16x32_bf16 v[84:87], a[4:7], a[200:203], v[84:87]\n"
"s_cbranch_vccnz L_rc0_retry\n"
"L_rc0_back:\n"
"v_lshlrev_b32 v192, 16, v220\n"
"v_and_b32 v193, 0xffff0000, v220\n"
"v_mfma_f32_16x16x32_bf16 v[88:91], a[8:11], a[200:203], 0\n"
"ds_write_b128 v237, v[116:119] offset:16384\n"
"v_lshlrev_b32 v194, 16, v222\n"
"v_and_b32 v195, 0xffff0000, v222\n"
"v_mfma_f32_16x16x32_bf16 v[88:91], a[24:27], a[204:207], v[88:91]\n"
"v_lshlrev_b32 v196, 16, v224\n"
"v_and_b32 v197, 0xffff0000, v224\n"
"v_mfma_f32_16x16x32_bf16 v[92:95], a[28:31], a[204:207], 0\n"
"ds_write_b128 v238, v[120:123] offset:16384\n"
"v_lshlrev_b32 v198, 16, v226\n"
"v_and_b32 v199, 0xffff0000, v226\n"
"v_mfma_f32_16x16x32_bf16 v[92:95], a[12:15], a[200:203], v[92:95]\n"
"s_waitcnt vmcnt(14)\n"
"s_xor_b32 s13, s12, 10240\n"
"v_fma_f32 v200, v192, v48, v200\n"
"v_fma_f32 v201, v193, v49, v201\n"
"v_mfma_f32_16x16x32_bf16 v[64:67], a[32:35], a[208:211], v[64:67]\n"
"buffer_load_dwordx4 a[200:203], v218, s[56:59], s13 offen sc1\n"
"ds_write_b128 v239, v[124:127] offset:16384\n"
"v_mfma_f32_16x16x32_bf16 v[64:67], a[48:51], a[212:215], v[64:67]\n"
"s_xor_b32 s15, s12, 11264\n"
"v_fma_f32 v202, v194, v50, v202\n"
"v_fma_f32 v203, v195, v51, v203\n"
"v_mfma_f32_16x16x32_bf16 v[68:71], a[52:55], a[212:215], v[68:71]\n"
"buffer_load_dwordx4 a[204:207], v218, s[56:59], s15 offen sc1\n"
"v_mfma_f32_16x16x32_bf16 v[68:71], a[36:39], a[208:211], v[68:71]\n"
"v_fma_f32 v204, v196, v52, v204\n"
"v_fma_f32 v205, v197, v53, v205\n"
"v_mfma_f32_16x16x32_bf16 v[72:75], a[40:43], a[208:211], v[72:75]\n"
"v_fma_f32 v206, v198, v54, v206\n"
"v_fma_f32 v207, v199, v55, v207\n"
"v_mfma_f32_16x16x32_bf16 v[72:75], a[56:59], a[212:215], v[72:75]\n"
"v_mfma_f32_16x16x32_bf16 v[76:79], a[60:63], a[212:215], v[76:79]\n"
"v_mfma_f32_16x16x32_bf16 v[76:79], a[44:47], a[208:211], v[76:79]\n"
"s_waitcnt vmcnt(14)\n"
"s_xor_b32 s13, s12, 12288\n"
"v_mfma_f32_16x16x32_bf16 v[80:83], a[32:35], a[216:219], v[80:83]\n"
"buffer_load_dwordx4 a[208:211], v218, s[56:59], s13 offen sc1\n"
"v_mfma_f32_16x16x32_bf16 v[80:83], a[48:51], a[220:223], v[80:83]\n"
"s_xor_b32 s15, s12, 13312\n"
"v_mfma_f32_16x16x32_bf16 v[84:87], a[52:55], a[220:223], v[84:87]\n"
"buffer_load_dwordx4 a[212:215], v218, s[56:59], s15 offen sc1\n"
"v_mfma_f32_16x16x32_bf16 v[84:87], a[36:39], a[216:219], v[84:87]\n"
"s_waitcnt lgkmcnt(0)\n"
"v_mfma_f32_16x16x32_bf16 v[88:91], a[40:43], a[216:219], v[88:91]\n"
"s_barrier\n"
"v_mfma_f32_16x16x32_bf16 v[88:91], a[56:59], a[220:223], v[88:91]\n"
"ds_read_b128 v[0:3], v240 offset:0\n"
"ds_read_b128 v[4:7], v240 offset:4096\n"
"v_mfma_f32_16x16x32_bf16 v[92:95], a[60:63], a[220:223], v[92:95]\n"
"ds_read_b128 v[8:11], v240 offset:8192\n"
"ds_read_b128 v[12:15], v240 offset:12288\n"
"v_mfma_f32_16x16x32_bf16 v[92:95], a[44:47], a[216:219], v[92:95]\n"
"s_waitcnt vmcnt(14)\n"
"s_xor_b32 s13, s12, 14336\n"
"ds_read_b128 v[16:19], v241 offset:0\n"
"v_mfma_f32_16x16x32_bf16 v[64:67], a[64:67], a[224:227], v[64:67]\n"
"buffer_load_dwordx4 a[216:219], v218, s[56:59], s13 offen sc1\n"
"ds_read_b128 v[20:23], v241 offset:4096\n"
"v_mfma_f32_16x16x32_bf16 v[64:67], a[80:83], a[228:231], v[64:67]\n"
"s_xor_b32 s15, s12, 15360\n"
"ds_read_b128 v[24:27], v241 offset:8192\n"
"v_mfma_f32_16x16x32_bf16 v[68:71], a[84:87], a[228:231], v[68:71]\n"
"buffer_load_dwordx4 a[220:223], v218, s[56:59], s15 offen sc1\n"
"v_mfma_f32_16x16x32_bf16 v[68:71], a[68:71], a[224:227], v[68:71]\n"
"ds_read_b128 v[28:31], v241 offset:12288\n"
"v_mfma_f32_16x16x32_bf16 v[72:75], a[72:75], a[224:227], v[72:75]\n"
"v_mfma_f32_16x16x32_bf16 v[72:75], a[88:91], a[228:231], v[72:75]\n"
"v_mfma_f32_16x16x32_bf16 v[76:79], a[92:95], a[228:231], v[76:79]\n"
"s_waitcnt lgkmcnt(4)\n"
"v_add_f32 v0, v0, v4\n"
"v_add_f32 v8, v8, v12\n"
"v_mfma_f32_16x16x32_bf16 v[76:79], a[76:79], a[224:227], v[76:79]\n"
"s_waitcnt vmcnt(14)\n"
"s_xor_b32 s13, s12, 8192\n"
"v_add_f32 v1, v1, v5\n"
"v_add_f32 v9, v9, v13\n"
"v_mfma_f32_16x16x32_bf16 v[80:83], a[64:67], a[232:235], v[80:83]\n"
"buffer_load_dwordx4 a[224:227], v219, s[56:59], s13 offen sc1\n"
"v_mfma_f32_16x16x32_bf16 v[80:83], a[80:83], a[236:239], v[80:83]\n"
"s_xor_b32 s15, s12, 9216\n"
"v_add_f32 v2, v2, v6\n"
"v_add_f32 v10, v10, v14\n"
"v_mfma_f32_16x16x32_bf16 v[84:87], a[84:87], a[236:239], v[84:87]\n"
"buffer_load_dwordx4 a[228:231], v219, s[56:59], s15 offen sc1\n"
"v_mfma_f32_16x16x32_bf16 v[84:87], a[68:71], a[232:235], v[84:87]\n"
"v_add_f32 v3, v3, v7\n"
"v_add_f32 v11, v11, v15\n"
"v_mfma_f32_16x16x32_bf16 v[88:91], a[72:75], a[232:235], v[88:91]\n"
"v_add_f32 v0, v0, v8\n"
"v_add_f32 v1, v1, v9\n"
"v_mfma_f32_16x16x32_bf16 v[88:91], a[88:91], a[236:239], v[88:91]\n"
"v_add_f32 v2, v2, v10\n"
"v_add_f32 v3, v3, v11\n"
"v_mfma_f32_16x16x32_bf16 v[92:95], a[92:95], a[236:239], v[92:95]\n"
"s_waitcnt lgkmcnt(0)\n"
"v_add_f32 v16, v16, v20\n"
"v_add_f32 v24, v24, v28\n"
"v_mfma_f32_16x16x32_bf16 v[92:95], a[76:79], a[232:235], v[92:95]\n"
"s_waitcnt vmcnt(14)\n"
"s_xor_b32 s13, s12, 10240\n"
"v_add_f32 v17, v17, v21\n"
"v_mfma_f32_16x16x32_bf16 v[64:67], a[96:99], a[240:243], v[64:67]\n"
"buffer_load_dwordx4 a[232:235], v219, s[56:59], s13 offen sc1\n"
"v_mfma_f32_16x16x32_bf16 v[64:67], a[112:115], a[244:247], v[64:67]\n"
"s_xor_b32 s15, s12, 11264\n"
"v_add_f32 v25, v25, v29\n"
"v_add_f32 v18, v18, v22\n"
"v_add_f32 v26, v26, v30\n"
"v_mfma_f32_16x16x32_bf16 v[68:71], a[116:119], a[244:247], v[68:71]\n"
"buffer_load_dwordx4 a[236:239], v219, s[56:59], s15 offen sc1\n"
"v_mfma_f32_16x16x32_bf16 v[68:71], a[100:103], a[240:243], v[68:71]\n"
"v_add_f32 v19, v19, v23\n"
"v_add_f32 v27, v27, v31\n"
"v_mfma_f32_16x16x32_bf16 v[72:75], a[104:107], a[240:243], v[72:75]\n"
"v_add_f32 v16, v16, v24\n"
"v_add_f32 v17, v17, v25\n"
"v_mfma_f32_16x16x32_bf16 v[72:75], a[120:123], a[244:247], v[72:75]\n"
"v_add_f32 v18, v18, v26\n"
"v_add_f32 v19, v19, v27\n"
"v_mfma_f32_16x16x32_bf16 v[76:79], a[124:127], a[244:247], v[76:79]\n"
"s_add_u32 s96, s8, 0\n"
"v_fma_f32 v200, v0, v48, v200\n"
"v_fma_f32 v201, v1, v49, v201\n"
"v_mfma_f32_16x16x32_bf16 v[76:79], a[108:111], a[240:243], v[76:79]\n"
"s_waitcnt vmcnt(12)\n"
"s_xor_b32 s13, s12, 12288\n"
"v_fma_f32 v202, v2, v50, v202\n"
"v_fma_f32 v203, v3, v51, v203\n"
"v_mfma_f32_16x16x32_bf16 v[80:83], a[96:99], a[248:251], v[80:83]\n"
"buffer_load_dwordx4 a[240:243], v219, s[56:59], s13 offen sc1\n"
"v_mfma_f32_16x16x32_bf16 v[80:83], a[112:115], a[252:255], v[80:83]\n"
"s_xor_b32 s15, s12, 13312\n"
"v_fma_f32 v204, v16, v52, v204\n"
"v_fma_f32 v205, v17, v53, v205\n"
"v_mfma_f32_16x16x32_bf16 v[84:87], a[116:119], a[252:255], v[84:87]\n"
"buffer_load_dwordx4 a[244:247], v219, s[56:59], s15 offen sc1\n"
"v_mfma_f32_16x16x32_bf16 v[84:87], a[100:103], a[248:251], v[84:87]\n"
"v_fma_f32 v206, v18, v54, v206\n"
"v_fma_f32 v207, v19, v55, v207\n"
"v_mfma_f32_16x16x32_bf16 v[88:91], a[104:107], a[248:251], v[88:91]\n"
"v_cvt_pk_bf16_f32 v4, v200, v201\n"
"v_cvt_pk_bf16_f32 v5, v202, v203\n"
"v_mfma_f32_16x16x32_bf16 v[88:91], a[120:123], a[252:255], v[88:91]\n"
"v_cvt_pk_bf16_f32 v6, v204, v205\n"
"v_cvt_pk_bf16_f32 v7, v206, v207\n"
"v_mfma_f32_16x16x32_bf16 v[92:95], a[124:127], a[252:255], v[92:95]\n"
"buffer_store_dwordx4 v[4:7], v44, s[60:63], s96 offen nt\n"
"v_mfma_f32_16x16x32_bf16 v[92:95], a[108:111], a[248:251], v[92:95]\n"
"s_waitcnt vmcnt(13)\n"
"s_xor_b32 s13, s12, 14336\n"
"v_mfma_f32_16x16x32_bf16 v[64:67], a[128:131], a[192:195], v[64:67]\n"
"buffer_load_dwordx4 a[248:251], v219, s[56:59], s13 offen sc1\n"
"v_mfma_f32_16x16x32_bf16 v[64:67], a[144:147], a[196:199], v[64:67]\n"
"s_xor_b32 s15, s12, 15360\n"
"v_mfma_f32_16x16x32_bf16 v[68:71], a[148:151], a[196:199], v[68:71]\n"
"buffer_load_dwordx4 a[252:255], v219, s[56:59], s15 offen sc1\n"
"v_mfma_f32_16x16x32_bf16 v[68:71], a[132:135], a[192:195], v[68:71]\n"
"v_mfma_f32_16x16x32_bf16 v[72:75], a[136:139], a[192:195], v[72:75]\n"
"v_mfma_f32_16x16x32_bf16 v[72:75], a[152:155], a[196:199], v[72:75]\n"
"v_mfma_f32_16x16x32_bf16 v[76:79], a[156:159], a[196:199], v[76:79]\n"
"v_mfma_f32_16x16x32_bf16 v[76:79], a[140:143], a[192:195], v[76:79]\n"
"s_waitcnt vmcnt(13)\n"
"v_mfma_f32_16x16x32_bf16 v[64:67], v[128:131], a[200:203], v[64:67]\n"
"v_mfma_f32_16x16x32_bf16 v[64:67], v[144:147], a[204:207], v[64:67]\n"
"v_mfma_f32_16x16x32_bf16 v[68:71], v[148:151], a[204:207], v[68:71]\n"
"v_mfma_f32_16x16x32_bf16 v[68:71], v[132:135], a[200:203], v[68:71]\n"
"v_mfma_f32_16x16x32_bf16 v[72:75], v[136:139], a[200:203], v[72:75]\n"
"v_mfma_f32_16x16x32_bf16 v[72:75], v[152:155], a[204:207], v[72:75]\n"
"v_mfma_f32_16x16x32_bf16 v[76:79], v[156:159], a[204:207], v[76:79]\n"
"v_mfma_f32_16x16x32_bf16 v[76:79], v[140:143], a[200:203], v[76:79]\n"
"s_waitcnt vmcnt(11)\n"
"v_mfma_f32_16x16x32_bf16 v[64:67], v[160:163], a[208:211], v[64:67]\n"
"v_mfma_f32_16x16x32_bf16 v[64:67], v[176:179], a[212:215], v[64:67]\n"
"v_mfma_f32_16x16x32_bf16 v[68:71], v[180:183], a[212:215], v[68:71]\n"
"v_mfma_f32_16x16x32_bf16 v[68:71], v[164:167], a[208:211], v[68:71]\n"
"v_mfma_f32_16x16x32_bf16 v[72:75], v[168:171], a[208:211], v[72:75]\n"
"v_mfma_f32_16x16x32_bf16 v[72:75], v[184:187], a[212:215], v[72:75]\n"
"v_mfma_f32_16x16x32_bf16 v[76:79], v[188:191], a[212:215], v[76:79]\n"
"s_add_u32 s19, s18, 8192\n"
"v_mfma_f32_16x16x32_bf16 v[76:79], v[172:175], a[208:211], v[76:79]\n"
"s_waitcnt vmcnt(9)\n"
"v_mfma_f32_16x16x32_bf16 v[64:67], a[160:163], a[216:219], v[64:67]\n"
"buffer_load_dwordx4 v[220:223], v243, s[56:59], s19 offen offset:0 sc1\n"
"v_mfma_f32_16x16x32_bf16 v[64:67], a[176:179], a[220:223], v[64:67]\n"
"buffer_load_dwordx4 v[224:227], v243, s[56:59], s19 offen offset:1024 sc1\n"
"v_mfma_f32_16x16x32_bf16 v[68:71], a[180:183], a[220:223], v[68:71]\n"
"v_mfma_f32_16x16x32_bf16 v[68:71], a[164:167], a[216:219], v[68:71]\n"
"v_mfma_f32_16x16x32_bf16 v[72:75], a[168:171], a[216:219], v[72:75]\n"
"v_mfma_f32_16x16x32_bf16 v[72:75], a[184:187], a[220:223], v[72:75]\n"
"v_mfma_f32_16x16x32_bf16 v[76:79], a[188:191], a[220:223], v[76:79]\n"
"v_mfma_f32_16x16x32_bf16 v[76:79], a[172:175], a[216:219], v[76:79]\n"
"s_waitcnt vmcnt(9)\n"
"v_mfma_f32_16x16x32_bf16 v[80:83], a[128:131], a[224:227], v[80:83]\n"
"v_mfma_f32_16x16x32_bf16 v[80:83], a[144:147], a[228:231], v[80:83]\n"
"v_mfma_f32_16x16x32_bf16 v[84:87], a[148:151], a[228:231], v[84:87]\n"
"v_mfma_f32_16x16x32_bf16 v[84:87], a[132:135], a[224:227], v[84:87]\n"
"v_mfma_f32_16x16x32_bf16 v[88:91], a[136:139], a[224:227], v[88:91]\n"
"v_mfma_f32_16x16x32_bf16 v[88:91], a[152:155], a[228:231], v[88:91]\n"
"ds_write_b128 v236, v[64:67] offset:32768\n"
"v_mfma_f32_16x16x32_bf16 v[92:95], a[156:159], a[228:231], v[92:95]\n"
"v_mfma_f32_16x16x32_bf16 v[92:95], a[140:143], a[224:227], v[92:95]\n"
"s_waitcnt vmcnt(7)\n"
"v_mfma_f32_16x16x32_bf16 v[80:83], v[128:131], a[232:235], v[80:83]\n"
"ds_write_b128 v237, v[68:71] offset:32768\n"
"v_mfma_f32_16x16x32_bf16 v[80:83], v[144:147], a[236:239], v[80:83]\n"
"v_mfma_f32_16x16x32_bf16 v[84:87], v[148:151], a[236:239], v[84:87]\n"
"v_lshlrev_b32 v200, 16, v36\n"
"v_and_b32 v201, 0xffff0000, v36\n"
"v_mfma_f32_16x16x32_bf16 v[84:87], v[132:135], a[232:235], v[84:87]\n"
"ds_write_b128 v238, v[72:75] offset:32768\n"
"v_mfma_f32_16x16x32_bf16 v[88:91], v[136:139], a[232:235], v[88:91]\n"
"v_lshlrev_b32 v202, 16, v37\n"
"L_ic10:\n"
"s_cbranch_execz L_ic_ret\n"
"v_and_b32 v203, 0xffff0000, v37\n"
"v_mfma_f32_16x16x32_bf16 v[88:91], v[152:155], a[236:239], v[88:91]\n"
"v_lshlrev_b32 v204, 16, v38\n"
"v_and_b32 v205, 0xffff0000, v38\n"
"v_mfma_f32_16x16x32_bf16 v[92:95], v[156:159], a[236:239], v[92:95]\n"
"ds_write_b128 v239, v[76:79] offset:32768\n"
"v_mfma_f32_16x16x32_bf16 v[92:95], v[140:143], a[232:235], v[92:95]\n"
"s_waitcnt vmcnt(5)\n"
"v_lshlrev_b32 v206, 16, v39\n"
"v_and_b32 v207, 0xffff0000, v39\n"
"v_mfma_f32_16x16x32_bf16 v[80:83], v[160:163], a[240:243], v[80:83]\n"
"v_mfma_f32_16x16x32_bf16 v[80:83], v[176:179], a[244:247], v[80:83]\n"
"v_mfma_f32_16x16x32_bf16 v[84:87], v[180:183], a[244:247], v[84:87]\n"
"v_mfma_f32_16x16x32_bf16 v[84:87], v[164:167], a[240:243], v[84:87]\n"
"v_mfma_f32_16x16x32_bf16 v[88:91], v[168:171], a[240:243], v[88:91]\n"
"v_mfma_f32_16x16x32_bf16 v[88:91], v[184:187], a[244:247], v[88:91]\n"
"v_mfma_f32_16x16x32_bf16 v[92:95], v[188:191], a[244:247], v[92:95]\n"
"v_mfma_f32_16x16x32_bf16 v[92:95], v[172:175], a[240:243], v[92:95]\n"
"s_waitcnt vmcnt(2)\n"
"v_mfma_f32_16x16x32_bf16 v[80:83], a[160:163], a[248:251], v[80:83]\n"
"v_mfma_f32_16x16x32_bf16 v[80:83], a[176:179], a[252:255], v[80:83]\n"
"v_mfma_f32_16x16x32_bf16 v[84:87], a[180:183], a[252:255], v[84:87]\n"
"v_mfma_f32_16x16x32_bf16 v[84:87], a[164:167], a[248:251], v[84:87]\n"
"v_mfma_f32_16x16x32_bf16 v[88:91], a[168:171], a[248:251], v[88:91]\n"
"v_mfma_f32_16x16x32_bf16 v[88:91], a[184:187], a[252:255], v[88:91]\n"
"v_mfma_f32_16x16x32_bf16 v[92:95], a[188:191], a[252:255], v[92:95]\n"
"v_mfma_f32_16x16x32_bf16 v[92:95], a[172:175], a[248:251], v[92:95]\n"
"s_waitcnt vmcnt(0)\n"
"v_min3_u32 v208, v221, v223, s14\n"
"v_max3_u32 v209, v221, v223, s14\n"
"v_min3_u32 v208, v208, v225, v227\n"
"v_max3_u32 v209, v209, v225, v227\n"
"v_cmp_ne_u32 vcc, v208, v209\n"
"ds_write_b128 v236, v[80:83] offset:49152\n"
"ds_write_b128 v237, v[84:87] offset:49152\n"
"ds_write_b128 v238, v[88:91] offset:49152\n"
"ds_write_b128 v239, v[92:95] offset:49152\n"
"s_cbranch_vccnz L_rc1_retry\n"
"L_rc1_back:\n"
"v_lshlrev_b32 v192, 16, v220\n"
"v_and_b32 v193, 0xffff0000, v220\n"
"v_lshlrev_b32 v194, 16, v222\n"
"v_and_b32 v195, 0xffff0000, v222\n"
"v_lshlrev_b32 v196, 16, v224\n"
"v_and_b32 v197, 0xffff0000, v224\n"
"v_lshlrev_b32 v198, 16, v226\n"
"v_and_b32 v199, 0xffff0000, v226\n"
"v_fma_f32 v200, v192, v56, v200\n"
"v_fma_f32 v201, v193, v57, v201\n"
"v_fma_f32 v202, v194, v58, v202\n"
"v_fma_f32 v203, v195, v59, v203\n"
"v_fma_f32 v204, v196, v60, v204\n"
"v_fma_f32 v205, v197, v61, v205\n"
"v_fma_f32 v206, v198, v62, v206\n"
"v_fma_f32 v207, v199, v63, v207\n"
"s_waitcnt lgkmcnt(0)\n"
"s_barrier\n"
"ds_read_b128 v[0:3], v240 offset:32768\n"
"ds_read_b128 v[4:7], v240 offset:36864\n"
"ds_read_b128 v[8:11], v240 offset:40960\n"
"ds_read_b128 v[12:15], v240 offset:45056\n"
"ds_read_b128 v[16:19], v241 offset:32768\n"
"ds_read_b128 v[20:23], v241 offset:36864\n"
"ds_read_b128 v[24:27], v241 offset:40960\n"
"ds_read_b128 v[28:31], v241 offset:45056\n"
"s_waitcnt lgkmcnt(4)\n"
"v_add_f32 v0, v0, v4\n"
"v_add_f32 v8, v8, v12\n"
"v_add_f32 v1, v1, v5\n"
"v_add_f32 v9, v9, v13\n"
"v_add_f32 v2, v2, v6\n"
"v_add_f32 v10, v10, v14\n"
"v_add_f32 v3, v3, v7\n"
"v_add_f32 v11, v11, v15\n"
"v_add_f32 v0, v0, v8\n"
"v_add_f32 v1, v1, v9\n"
"v_add_f32 v2, v2, v10\n"
"v_add_f32 v3, v3, v11\n"
"s_waitcnt lgkmcnt(0)\n"
"v_add_f32 v16, v16, v20\n"
"v_add_f32 v24, v24, v28\n"
"v_add_f32 v17, v17, v21\n"
"v_add_f32 v25, v25, v29\n"
"v_add_f32 v18, v18, v22\n"
"v_add_f32 v26, v26, v30\n"
"v_add_f32 v19, v19, v23\n"
"v_add_f32 v27, v27, v31\n"
"v_add_f32 v16, v16, v24\n"
"v_add_f32 v17, v17, v25\n"
"v_add_f32 v18, v18, v26\n"
"v_add_f32 v19, v19, v27\n"
"s_add_u32 s96, s8, 32768\n"
"v_fma_f32 v200, v0, v56, v200\n"
"v_fma_f32 v201, v1, v57, v201\n"
"v_fma_f32 v202, v2, v58, v202\n"
"v_fma_f32 v203, v3, v59, v203\n"
"v_fma_f32 v204, v16, v60, v204\n"
"v_fma_f32 v205, v17, v61, v205\n"
"v_fma_f32 v206, v18, v62, v206\n"
"v_fma_f32 v207, v19, v63, v207\n"
"v_cvt_pk_bf16_f32 v4, v200, v201\n"
"v_cvt_pk_bf16_f32 v5, v202, v203\n"
"v_cvt_pk_bf16_f32 v6, v204, v205\n"
"v_cvt_pk_bf16_f32 v7, v206, v207\n"
"buffer_store_dwordx4 v[4:7], v44, s[60:63], s96 offen nt\n"
"s_endpgm\n"
"L_ic11:\n"
"s_cbranch_execz L_ic_ret\n"
"L_yload:\n"
"s_nop 1\n"
"s_lshl_b32 s96, s85, 9\n"
"s_add_u32 s24, s96, 262144\n"
"s_add_u32 s20, s96, 266240\n"
"s_add_u32 s21, s96, 270336\n"
"s_add_u32 s25, s96, 274432\n"
"v_and_b32 v214, 0x1f0, v243\n"
"v_lshrrev_b32 v213, 5, v242\n"
"v_lshl_add_u32 v214, v213, 14, v214\n"
"s_xor_b32 s96, s82, s97\n"
"s_and_b32 s96, s96, 0xffffffd8\n"
"s_cselect_b32 s96, 2130706432, 0\n"
"s_cselect_b32 s22, 0, s14\n"
"v_add_u32 v212, s96, v214\n"
"s_xor_b32 s96, s83, s97\n"
"s_and_b32 s96, s96, 0xffffffd8\n"
"s_cselect_b32 s96, 2130706432, 0\n"
"s_cselect_b32 s23, 0, s14\n"
"v_add_u32 v213, s96, v214\n"
"v_add_u32 v213, 32768, v213\n"
"buffer_load_dwordx4 v[0:3], v212, s[56:59], s24 offen sc1\n"
"buffer_load_dwordx4 v[4:7], v212, s[56:59], s20 offen sc1\n"
"buffer_load_dwordx4 v[8:11], v212, s[56:59], s21 offen sc1\n"
"buffer_load_dwordx4 v[12:15], v212, s[56:59], s25 offen sc1\n"
"buffer_load_dwordx4 v[16:19], v213, s[56:59], s24 offen sc1\n"
"buffer_load_dwordx4 v[20:23], v213, s[56:59], s20 offen sc1\n"
"buffer_load_dwordx4 v[24:27], v213, s[56:59], s21 offen sc1\n"
"buffer_load_dwordx4 v[28:31], v213, s[56:59], s25 offen sc1\n"
"s_branch L_yload_back\n"
"L_ic12:\n"
"s_cbranch_execz L_ic_ret\n"
"L_yuse:\n"
"s_nop 1\n"
"s_mov_b32 s31, 0\n"
"L_yuse_chk:\n"
"v_mov_b32 v209, 0\n"
"v_min3_u32 v210, v1, v3, s22\n"
"v_max3_u32 v211, v1, v3, s22\n"
"v_min3_u32 v210, v210, v5, v7\n"
"v_max3_u32 v211, v211, v5, v7\n"
"v_min3_u32 v210, v210, v9, v11\n"
"v_max3_u32 v211, v211, v9, v11\n"
"v_min3_u32 v210, v210, v13, v15\n"
"v_max3_u32 v211, v211, v13, v15\n"
"v_xor_b32 v210, v210, v211\n"
"v_or_b32 v209, v209, v210\n"
"v_min3_u32 v210, v17, v19, s23\n"
"v_max3_u32 v211, v17, v19, s23\n"
"v_min3_u32 v210, v210, v21, v23\n"
"v_max3_u32 v211, v211, v21, v23\n"
"v_min3_u32 v210, v210, v25, v27\n"
"v_max3_u32 v211, v211, v25, v27\n"
"v_min3_u32 v210, v210, v29, v31\n"
"v_max3_u32 v211, v211, v29, v31\n"
"v_xor_b32 v210, v210, v211\n"
"v_or_b32 v209, v209, v210\n"
"v_cmp_ne_u32 vcc, 0, v209\n"
"s_cbranch_vccz L_yuse_ok\n"
"s_add_u32 s31, s31, 1\n"
"s_cmp_gt_u32 s31, 0x40000\n"
"s_cbranch_scc1 L_yuse_ok\n"
"s_sleep 1\n"
"buffer_load_dwordx4 v[0:3], v212, s[56:59], s24 offen sc1\n"
"buffer_load_dwordx4 v[4:7], v212, s[56:59], s20 offen sc1\n"
"buffer_load_dwordx4 v[8:11], v212, s[56:59], s21 offen sc1\n"
"buffer_load_dwordx4 v[12:15], v212, s[56:59], s25 offen sc1\n"
"buffer_load_dwordx4 v[16:19], v213, s[56:59], s24 offen sc1\n"
"buffer_load_dwordx4 v[20:23], v213, s[56:59], s20 offen sc1\n"
"buffer_load_dwordx4 v[24:27], v213, s[56:59], s21 offen sc1\n"
"buffer_load_dwordx4 v[28:31], v213, s[56:59], s25 offen sc1\n"
"s_waitcnt vmcnt(0)\n"
"s_branch L_yuse_chk\n"
"L_yuse_ok:\n"
"s_ashr_i32 s96, s80, 31\n"
"s_not_b32 s96, s96\n"
"s_or_b32 s96, s96, 0x80000000\n"
"s_xor_b32 s96, s96, s80\n"
"s_and_b32 s96, s96, 0xffff0000\n"
"v_mov_b32 v214, s96\n"
"v_mul_f32 v214, 0xbfb8aa3b, v214\n"
"v_exp_f32 v214, v214\n"
"s_nop 0\n"
"v_add_f32 v214, 1.0, v214\n"
"v_rcp_f32 v214, v214\n"
"s_ashr_i32 s96, s81, 31\n"
"s_not_b32 s96, s96\n"
"s_or_b32 s96, s96, 0x80000000\n"
"s_xor_b32 s96, s96, s81\n"
"s_and_b32 s96, s96, 0xffff0000\n"
"v_mov_b32 v215, s96\n"
"v_mul_f32 v215, 0xbfb8aa3b, v215\n"
"v_exp_f32 v215, v215\n"
"s_nop 0\n"
"v_add_f32 v215, 1.0, v215\n"
"v_rcp_f32 v215, v215\n"
"v_add_f32 v0, v0, v4\n"
"v_add_f32 v8, v8, v12\n"
"v_add_f32 v0, v0, v8\n"
"v_mul_f32 v0, v0, v214\n"
"v_mov_b32 v1, v0\n"
"v_add_f32 v2, v2, v6\n"
"v_add_f32 v10, v10, v14\n"
"v_add_f32 v2, v2, v10\n"
"v_mul_f32 v2, v2, v214\n"
"v_mov_b32 v3, v2\n"
"v_add_f32 v16, v16, v20\n"
"v_add_f32 v24, v24, v28\n"
"v_add_f32 v16, v16, v24\n"
"v_mul_f32 v16, v16, v215\n"
"v_mov_b32 v17, v16\n"
"v_add_f32 v18, v18, v22\n"
"v_add_f32 v26, v26, v30\n"
"v_add_f32 v18, v18, v26\n"
"v_mul_f32 v18, v18, v215\n"
"v_mov_b32 v19, v18\n"
"v_permlane32_swap_b32 v0, v1\n"
"v_add_f32 v4, v0, v1\n"
"v_permlane32_swap_b32 v2, v3\n"
"v_add_f32 v5, v2, v3\n"
"v_permlane32_swap_b32 v16, v17\n"
"v_add_f32 v20, v16, v17\n"
"v_permlane32_swap_b32 v18, v19\n"
"v_add_f32 v21, v18, v19\n"
"v_and_b32 v209, 31, v242\n"
"v_lshl_add_u32 v209, v209, 3, s1\n"
"v_mov_b32 v210, 0\n"
"v_mov_b32 v211, 0\n"
"ds_write_b64 v209, v[4:5]\n"
"ds_write_b64 v209, v[20:21] offset:256\n"
"ds_write_b64 v209, v[210:211] offset:512\n"
"v_and_b32 v209, 7, v242\n"
"v_lshl_add_u32 v209, v209, 5, s1\n"
"v_mov_b32 v208, 256\n"
"v_add_u32 v211, s97, v47\n"
"v_add_u32 v211, 0, v211\n"
"v_mov_b32 v212, 512\n"
"v_cmp_eq_u32 vcc, s83, v211\n"
"s_nop 1\n"
"v_cndmask_b32 v212, v212, v208, vcc\n"
"v_cmp_eq_u32 vcc, s82, v211\n"
"s_nop 1\n"
"v_cndmask_b32 v212, v212, v210, vcc\n"
"v_add_u32 v212, v209, v212\n"
"ds_read_b128 v[0:3], v212\n"
"ds_read_b128 v[4:7], v212 offset:16\n"
"v_add_u32 v211, s97, v47\n"
"v_add_u32 v211, 32, v211\n"
"v_mov_b32 v212, 512\n"
"v_cmp_eq_u32 vcc, s83, v211\n"
"s_nop 1\n"
"v_cndmask_b32 v212, v212, v208, vcc\n"
"v_cmp_eq_u32 vcc, s82, v211\n"
"s_nop 1\n"
"v_cndmask_b32 v212, v212, v210, vcc\n"
"v_add_u32 v212, v209, v212\n"
"ds_read_b128 v[8:11], v212\n"
"ds_read_b128 v[12:15], v212 offset:16\n"
"s_waitcnt lgkmcnt(0)\n"
"v_add_f32 v48, v48, v0\n"
"v_add_f32 v49, v49, v1\n"
"v_add_f32 v50, v50, v2\n"
"v_add_f32 v51, v51, v3\n"
"v_add_f32 v52, v52, v4\n"
"v_add_f32 v53, v53, v5\n"
"v_add_f32 v54, v54, v6\n"
"v_add_f32 v55, v55, v7\n"
"v_add_f32 v56, v56, v8\n"
"v_add_f32 v57, v57, v9\n"
"v_add_f32 v58, v58, v10\n"
"v_add_f32 v59, v59, v11\n"
"v_add_f32 v60, v60, v12\n"
"v_add_f32 v61, v61, v13\n"
"v_add_f32 v62, v62, v14\n"
"v_add_f32 v63, v63, v15\n"
"s_nop 1\n"
"s_branch L_yuse_back\n"
"L_hot_end:\n"
"L_keys_retry:\n"
"s_nop 7\n"
"s_mov_b32 s31, 0\n"
"L_keys_loop:\n"
"s_sleep 1\n"
"buffer_load_dwordx4 v[128:131], v243, s[56:59], s96 offen offset:0 sc1\n"
"buffer_load_dwordx4 v[132:135], v243, s[56:59], s96 offen offset:1024 sc1\n"
"buffer_load_dwordx4 v[136:139], v243, s[56:59], s96 offen offset:2048 sc1\n"
"buffer_load_dwordx4 v[140:143], v243, s[56:59], s96 offen offset:3072 sc1\n"
"s_waitcnt vmcnt(0)\n"
"v_min3_u32 v144, v129, v131, v133\n"
"v_max3_u32 v145, v129, v131, v133\n"
"v_min3_u32 v144, v144, v135, v137\n"
"v_max3_u32 v145, v145, v135, v137\n"
"v_min3_u32 v144, v144, v139, v141\n"
"v_max3_u32 v145, v145, v139, v141\n"
"v_min3_u32 v144, v144, v143, s14\n"
"v_max3_u32 v145, v145, v143, s14\n"
"v_cmp_ne_u32 vcc, v144, v145\n"
"s_add_u32 s31, s31, 1\n"
"s_cmp_gt_u32 s31, 0x40000\n"
"s_cbranch_scc1 L_keys_out\n"
"s_cbranch_vccnz L_keys_loop\n"
"L_keys_out:\n"
"s_nop 7\n"
"s_branch L_keys_back\n"
"L_stats_retry:\n"
"s_nop 7\n"
"s_mov_b32 s31, 0\n"
"L_stats_loop:\n"
"s_sleep 1\n"
"buffer_load_dwordx2 v[160:161], v168, s[56:59], s97 offen offset:0 sc1\n"
"buffer_load_dwordx2 v[162:163], v168, s[56:59], s97 offen offset:128 sc1\n"
"buffer_load_dwordx2 v[164:165], v168, s[56:59], s97 offen offset:256 sc1\n"
"buffer_load_dwordx2 v[166:167], v168, s[56:59], s97 offen offset:384 sc1\n"
"s_waitcnt vmcnt(0)\n"
"v_min3_u32 v169, v161, v163, v165\n"
"v_max3_u32 v170, v161, v163, v165\n"
"v_min3_u32 v169, v169, v167, s14\n"
"v_max3_u32 v170, v170, v167, s14\n"
"v_cmp_ne_u32 vcc, v169, v170\n"
"s_add_u32 s31, s31, 1\n"
"s_cmp_gt_u32 s31, 0x40000\n"
"s_cbranch_scc1 L_stats_out\n"
"s_cbranch_vccnz L_stats_loop\n"
"L_stats_out:\n"
"s_nop 7\n"
"s_branch L_stats_back\n"
"L_xsel_retry:\n"
"s_nop 7\n"
"s_mov_b32 s31, 0\n"
"L_xsel_loop:\n"
"s_sleep 1\n"
"buffer_load_dwordx2 v[252:253], v254, s[56:59], 0 offen sc1\n"
"s_waitcnt vmcnt(0)\n"
"v_cmp_ne_u32 vcc, s14, v253\n"
"s_add_u32 s31, s31, 1\n"
"s_cmp_gt_u32 s31, 0x40000\n"
"s_cbranch_scc1 L_xsel_out\n"
"s_cbranch_vccnz L_xsel_loop\n"
"L_xsel_out:\n"
"s_nop 7\n"
"s_branch L_xsel_back\n"
"L_gul_retry:\n"
"s_nop 7\n"
"s_mov_b32 s31, 0\n"
"L_gul_loop:\n"
"s_sleep 1\n"
"buffer_load_dwordx4 v[0:3], v46, s[56:59], s32 offen offset:0 sc1\n"
"buffer_load_dwordx4 v[4:7], v46, s[56:59], s32 offen offset:16 sc1\n"
"buffer_load_dwordx4 v[8:11], v46, s[56:59], s20 offen offset:0 sc1\n"
"buffer_load_dwordx4 v[12:15], v46, s[56:59], s20 offen offset:16 sc1\n"
"buffer_load_dwordx4 v[16:19], v46, s[56:59], s22 offen offset:0 sc1\n"
"buffer_load_dwordx4 v[20:23], v46, s[56:59], s22 offen offset:16 sc1\n"
"buffer_load_dwordx4 v[24:27], v46, s[56:59], s23 offen offset:0 sc1\n"
"buffer_load_dwordx4 v[28:31], v46, s[56:59], s23 offen offset:16 sc1\n"
"s_waitcnt vmcnt(0)\n"
"v_min3_u32 v252, v1, v3, s14\n"
"v_max3_u32 v253, v1, v3, s14\n"
"v_min3_u32 v252, v252, v5, v7\n"
"v_max3_u32 v253, v253, v5, v7\n"
"v_min3_u32 v252, v252, v9, v11\n"
"v_max3_u32 v253, v253, v9, v11\n"
"v_min3_u32 v252, v252, v13, v15\n"
"v_max3_u32 v253, v253, v13, v15\n"
"v_min3_u32 v252, v252, v17, v19\n"
"v_max3_u32 v253, v253, v17, v19\n"
"v_min3_u32 v252, v252, v21, v23\n"
"v_max3_u32 v253, v253, v21, v23\n"
"v_min3_u32 v252, v252, v25, v27\n"
"v_max3_u32 v253, v253, v25, v27\n"
"v_min3_u32 v252, v252, v29, v31\n"
"v_max3_u32 v253, v253, v29, v31\n"
"v_cmp_ne_u32 vcc, v252, v253\n"
"s_and_b32 vcc_hi, vcc_hi, 0x7ff\n"
"s_mov_b64 vcc, vcc\n"
"s_add_u32 s31, s31, 1\n"
"s_cmp_gt_u32 s31, 0x40000\n"
"s_cbranch_scc1 L_gul_out\n"
"s_cbranch_vccnz L_gul_loop\n"
"L_gul_out:\n"
"s_nop 7\n"
"s_branch L_gul_back\n"
"L_ex_retry:\n"
"s_nop 7\n"
"s_mov_b32 s31, 0\n"
"L_ex_loop:\n"
"s_sleep 1\n"
"buffer_load_dwordx2 v[40:41], v45, s[56:59], s9 offen offset:0 sc1\n"
"buffer_load_dwordx2 v[42:43], v45, s[56:59], s9 offen offset:256 sc1\n"
"s_waitcnt vmcnt(0)\n"
"v_xor_b32 v208, s14, v41\n"
"v_xor_b32 v209, s14, v43\n"
"v_or_b32 v208, v208, v209\n"
"v_cmp_ne_u32 vcc, 0, v208\n"
"s_add_u32 s31, s31, 1\n"
"s_cmp_gt_u32 s31, 0x40000\n"
"s_cbranch_scc1 L_ex_out\n"
"s_cbranch_vccnz L_ex_loop\n"
"L_ex_out:\n"
"s_nop 7\n"
"s_branch L_ex_back\n"
"L_poll0_retry:\n"
"s_mov_b32 s31, 0\n"
"s_nop 7\n"
"v_and_b32 v249, 3, v242\n"
"v_lshlrev_b32 v249, 2, v249\n"
"v_and_b32 v248, 4, v242\n"
"v_lshl_add_u32 v249, v248, 6, v249\n"
"L_poll0_vloop:\n"
"s_sleep 1\n"
"buffer_load_dword v248, v249, s[56:59], s94 offen sc1\n"
"s_waitcnt vmcnt(0)\n"
"v_cmp_ne_u32 s[64:65], s14, v248\n"
"s_add_u32 s31, s31, 1\n"
"s_cmp_gt_u32 s31, 0x40000\n"
"s_cbranch_scc1 L_poll0_back\n"
"s_cmp_lg_u64 s[64:65], 0\n"
"s_cbranch_scc1 L_poll0_vloop\n"
"s_branch L_poll0_back\n"
"L_poll1_retry:\n"
"s_mov_b32 s31, 0\n"
"s_nop 7\n"
"v_and_b32 v249, 3, v242\n"
"v_lshlrev_b32 v249, 2, v249\n"
"v_and_b32 v248, 4, v242\n"
"v_lshl_add_u32 v249, v248, 6, v249\n"
"L_poll1_vloop:\n"
"s_sleep 1\n"
"buffer_load_dword v248, v249, s[56:59], s95 offen sc1\n"
"s_waitcnt vmcnt(0)\n"
"v_cmp_ne_u32 s[64:65], s14, v248\n"
"s_add_u32 s31, s31, 1\n"
"s_cmp_gt_u32 s31, 0x40000\n"
"s_cbranch_scc1 L_poll1_back\n"
"s_cmp_lg_u64 s[64:65], 0\n"
"s_cbranch_scc1 L_poll1_vloop\n"
"s_branch L_poll1_back\n"
"L_rc0_retry:\n"
"s_nop 7\n"
"s_mov_b32 s31, 0\n"
"L_rc0_loop:\n"
"s_sleep 1\n"
"buffer_load_dwordx4 v[220:223], v243, s[56:59], s19 offen offset:0 sc1\n"
"buffer_load_dwordx4 v[224:227], v243, s[56:59], s19 offen offset:1024 sc1\n"
"s_waitcnt vmcnt(0)\n"
"v_min3_u32 v208, v221, v223, s14\n"
"v_max3_u32 v209, v221, v223, s14\n"
"v_min3_u32 v208, v208, v225, v227\n"
"v_max3_u32 v209, v209, v225, v227\n"
"v_cmp_ne_u32 vcc, v208, v209\n"
"s_add_u32 s31, s31, 1\n"
"s_cmp_gt_u32 s31, 0x40000\n"
"s_cbranch_scc1 L_rc0_out\n"
"s_cbranch_vccnz L_rc0_loop\n"
"L_rc0_out:\n"
"s_nop 7\n"
"s_branch L_rc0_back\n"
"L_rc1_retry:\n"
"s_nop 7\n"
"s_mov_b32 s31, 0\n"
"L_rc1_loop:\n"
"s_sleep 1\n"
"buffer_load_dwordx4 v[220:223], v243, s[56:59], s19 offen offset:0 sc1\n"
"buffer_load_dwordx4 v[224:227], v243, s[56:59], s19 offen offset:1024 sc1\n"
"s_waitcnt vmcnt(0)\n"
"v_min3_u32 v208, v221, v223, s14\n"
"v_max3_u32 v209, v221, v223, s14\n"
"v_min3_u32 v208, v208, v225, v227\n"
"v_max3_u32 v209, v209, v225, v227\n"
"v_cmp_ne_u32 vcc, v208, v209\n"
"s_add_u32 s31, s31, 1\n"
"s_cmp_gt_u32 s31, 0x40000\n"
"s_cbranch_scc1 L_rc1_out\n"
"s_cbranch_vccnz L_rc1_loop\n"
"L_rc1_out:\n"
"s_nop 7\n"
"s_branch L_rc1_back\n"
"L_fallback:\n"
"s_nop 15\n"
"s_waitcnt vmcnt(0) lgkmcnt(0)\n"
"s_add_u32 s21, s90, 65536\n"
"v_add_u32 v224, s21, v224\n"
"v_add_u32 v225, s21, v225\n"
"v_add_u32 v226, s21, v226\n"
"v_add_u32 v227, s21, v227\n"
"v_add_u32 v216, s21, v216\n"
"v_add_u32 v217, s21, v217\n"
"v_add_u32 v218, s21, v218\n"
"v_add_u32 v219, s21, v219\n"
"v_add_u32 v220, s21, v220\n"
"v_add_u32 v221, s21, v221\n"
"v_add_u32 v222, s21, v222\n"
"v_add_u32 v223, s21, v223\n"
"v_lshrrev_b32 v248, 4, v242\n"
"v_and_b32 v249, 15, v242\n"
"v_xor_b32 v250, v248, v249\n"
"v_lshlrev_b32 v250, 4, v250\n"
"v_lshl_add_u32 v238, v248, 8, v250\n"
"v_xor_b32 v239, 64, v238\n"
"v_xor_b32 v240, 128, v238\n"
"v_xor_b32 v241, 192, v238\n"
"v_lshlrev_b32 v250, 1, v248\n"
"v_xor_b32 v250, v250, v249\n"
"v_lshlrev_b32 v250, 4, v250\n"
"v_lshl_add_u32 v212, v248, 8, v250\n"
"v_xor_b32 v213, 128, v212\n"
"v_mov_b32 v252, 0xff800000\n"
"v_mov_b32 v253, 0xff800000\n"
"v_mov_b32 v254, 0xff800000\n"
"v_mov_b32 v251, 0xff800000\n"
"s_and_b32 s20, s19, 0xfffe0000\n"
"s_mov_b32 s31, 0\n"
"L_fb1:\n"
"s_add_u32 m0, s21, 0\n"
"s_add_u32 s22, s20, 0\n"
"buffer_load_dwordx4 v238, s[44:47], s22 offen offset:0 lds\n"
"buffer_load_dwordx4 v239, s[44:47], s22 offen offset:1024 lds\n"
"buffer_load_dwordx4 v240, s[44:47], s22 offen offset:2048 lds\n"
"buffer_load_dwordx4 v241, s[44:47], s22 offen offset:3072 lds\n"
"s_add_u32 m0, s21, 16384\n"
"s_add_u32 s22, s20, 4096\n"
"buffer_load_dwordx4 v238, s[44:47], s22 offen offset:0 lds\n"
"buffer_load_dwordx4 v239, s[44:47], s22 offen offset:1024 lds\n"
"buffer_load_dwordx4 v240, s[44:47], s22 offen offset:2048 lds\n"
"buffer_load_dwordx4 v241, s[44:47], s22 offen offset:3072 lds\n"
"s_waitcnt vmcnt(0)\n"
"ds_read_b128 a[172:175], v224 offset:0\n"
"ds_read_b128 a[176:179], v225 offset:0\n"
"ds_read_b128 a[180:183], v226 offset:0\n"
"ds_read_b128 a[184:187], v227 offset:0\n"
"ds_read_b128 a[188:191], v224 offset:16384\n"
"ds_read_b128 a[192:195], v225 offset:16384\n"
"ds_read_b128 a[196:199], v226 offset:16384\n"
"ds_read_b128 a[200:203], v227 offset:16384\n"
"s_waitcnt lgkmcnt(0)\n"
"v_mfma_f32_16x16x32_bf16 v[192:195], a[172:175], v[128:131], 0\n"
"v_mfma_f32_16x16x32_bf16 v[192:195], a[176:179], v[132:135], v[192:195]\n"
"v_mfma_f32_16x16x32_bf16 v[192:195], a[180:183], v[136:139], v[192:195]\n"
"v_mfma_f32_16x16x32_bf16 v[192:195], a[184:187], v[140:143], v[192:195]\n"
"v_mfma_f32_16x16x32_bf16 v[196:199], a[188:191], v[128:131], 0\n"
"v_mfma_f32_16x16x32_bf16 v[196:199], a[192:195], v[132:135], v[196:199]\n"
"v_mfma_f32_16x16x32_bf16 v[196:199], a[196:199], v[136:139], v[196:199]\n"
"v_mfma_f32_16x16x32_bf16 v[196:199], a[200:203], v[140:143], v[196:199]\n"
"s_nop 3\n"
"v_max3_f32 v252, v252, v192, v193\n"
"v_max3_f32 v252, v252, v194, v195\n"
"s_nop 1\n"
"v_max3_f32 v252, v252, v196, v197\n"
"v_max3_f32 v252, v252, v198, v199\n"
"v_mfma_f32_16x16x32_bf16 v[192:195], a[172:175], v[144:147], 0\n"
"v_mfma_f32_16x16x32_bf16 v[192:195], a[176:179], v[148:151], v[192:195]\n"
"v_mfma_f32_16x16x32_bf16 v[192:195], a[180:183], v[152:155], v[192:195]\n"
"v_mfma_f32_16x16x32_bf16 v[192:195], a[184:187], v[156:159], v[192:195]\n"
"v_mfma_f32_16x16x32_bf16 v[196:199], a[188:191], v[144:147], 0\n"
"v_mfma_f32_16x16x32_bf16 v[196:199], a[192:195], v[148:151], v[196:199]\n"
"v_mfma_f32_16x16x32_bf16 v[196:199], a[196:199], v[152:155], v[196:199]\n"
"v_mfma_f32_16x16x32_bf16 v[196:199], a[200:203], v[156:159], v[196:199]\n"
"s_nop 3\n"
"v_max3_f32 v253, v253, v192, v193\n"
"v_max3_f32 v253, v253, v194, v195\n"
"s_nop 1\n"
"v_max3_f32 v253, v253, v196, v197\n"
"v_max3_f32 v253, v253, v198, v199\n"
"v_mfma_f32_16x16x32_bf16 v[192:195], a[172:175], v[160:163], 0\n"
"v_mfma_f32_16x16x32_bf16 v[192:195], a[176:179], v[164:167], v[192:195]\n"
"v_mfma_f32_16x16x32_bf16 v[192:195], a[180:183], v[168:171], v[192:195]\n"
"v_mfma_f32_16x16x32_bf16 v[192:195], a[184:187], v[172:175], v[192:195]\n"
"v_mfma_f32_16x16x32_bf16 v[196:199], a[188:191], v[160:163], 0\n"
"v_mfma_f32_16x16x32_bf16 v[196:199], a[192:195], v[164:167], v[196:199]\n"
"v_mfma_f32_16x16x32_bf16 v[196:199], a[196:199], v[168:171], v[196:199]\n"
"v_mfma_f32_16x16x32_bf16 v[196:199], a[200:203], v[172:175], v[196:199]\n"
"s_nop 3\n"
"v_max3_f32 v254, v254, v192, v193\n"
"v_max3_f32 v254, v254, v194, v195\n"
"s_nop 1\n"
"v_max3_f32 v254, v254, v196, v197\n"
"v_max3_f32 v254, v254, v198, v199\n"
"v_mfma_f32_16x16x32_bf16 v[192:195], a[172:175], v[176:179], 0\n"
"v_mfma_f32_16x16x32_bf16 v[192:195], a[176:179], v[180:183], v[192:195]\n"
"v_mfma_f32_16x16x32_bf16 v[192:195], a[180:183], v[184:187], v[192:195]\n"
"v_mfma_f32_16x16x32_bf16 v[192:195], a[184:187], v[188:191], v[192:195]\n"
"v_mfma_f32_16x16x32_bf16 v[196:199], a[188:191], v[176:179], 0\n"
"v_mfma_f32_16x16x32_bf16 v[196:199], a[192:195], v[180:183], v[196:199]\n"
"v_mfma_f32_16x16x32_bf16 v[196:199], a[196:199], v[184:187], v[196:199]\n"
"v_mfma_f32_16x16x32_bf16 v[196:199], a[200:203], v[188:191], v[196:199]\n"
"s_nop 3\n"
"v_max3_f32 v251, v251, v192, v193\n"
"v_max3_f32 v251, v251, v194, v195\n"
"s_nop 1\n"
"v_max3_f32 v251, v251, v196, v197\n"
"v_max3_f32 v251, v251, v198, v199\n"
"s_nop 15\n"
"s_add_u32 s20, s20, 8192\n"
"s_add_u32 s31, s31, 1\n"
"s_cmp_lt_u32 s31, 16\n"
"s_cbranch_scc1 L_fb1\n"
"v_mov_b32 v248, v252\n"
"s_nop 1\n"
"v_permlane16_swap_b32 v252, v248\n"
"v_max_f32 v252, v252, v248\n"
"v_mov_b32 v248, v252\n"
"s_nop 1\n"
"v_permlane32_swap_b32 v252, v248\n"
"v_max_f32 v252, v252, v248\n"
"v_mov_b32 v248, v253\n"
"s_nop 1\n"
"v_permlane16_swap_b32 v253, v248\n"
"v_max_f32 v253, v253, v248\n"
"v_mov_b32 v248, v253\n"
"s_nop 1\n"
"v_permlane32_swap_b32 v253, v248\n"
"v_max_f32 v253, v253, v248\n"
"v_mov_b32 v248, v254\n"
"s_nop 1\n"
"v_permlane16_swap_b32 v254, v248\n"
"v_max_f32 v254, v254, v248\n"
"v_mov_b32 v248, v254\n"
"s_nop 1\n"
"v_permlane32_swap_b32 v254, v248\n"
"v_max_f32 v254, v254, v248\n"
"v_mov_b32 v248, v251\n"
"s_nop 1\n"
"v_permlane16_swap_b32 v251, v248\n"
"v_max_f32 v251, v251, v248\n"
"v_mov_b32 v248, v251\n"
"s_nop 1\n"
"v_permlane32_swap_b32 v251, v248\n"
"v_max_f32 v251, v251, v248\n"
"s_cmp_lg_u32 s98, 2130706432\n"
"s_cbranch_scc1 L_fb_skipA\n"
"v_mov_b32 v64, 0\n"
"v_mov_b32 v65, 0\n"
"v_mov_b32 v66, 0\n"
"v_mov_b32 v67, 0\n"
"v_mov_b32 v68, 0\n"
"v_mov_b32 v69, 0\n"
"v_mov_b32 v70, 0\n"
"v_mov_b32 v71, 0\n"
"v_mov_b32 v72, 0\n"
"v_mov_b32 v73, 0\n"
"v_mov_b32 v74, 0\n"
"v_mov_b32 v75, 0\n"
"v_mov_b32 v76, 0\n"
"v_mov_b32 v77, 0\n"
"v_mov_b32 v78, 0\n"
"v_mov_b32 v79, 0\n"
"v_mov_b32 v80, 0\n"
"v_mov_b32 v81, 0\n"
"v_mov_b32 v82, 0\n"
"v_mov_b32 v83, 0\n"
"v_mov_b32 v84, 0\n"
"v_mov_b32 v85, 0\n"
"v_mov_b32 v86, 0\n"
"v_mov_b32 v87, 0\n"
"v_mov_b32 v88, 0\n"
"v_mov_b32 v89, 0\n"
"v_mov_b32 v90, 0\n"
"v_mov_b32 v91, 0\n"
"v_mov_b32 v92, 0\n"
"v_mov_b32 v93, 0\n"
"v_mov_b32 v94, 0\n"
"v_mov_b32 v95, 0\n"
"v_mov_b32 v96, 0\n"
"v_mov_b32 v97, 0\n"
"v_mov_b32 v98, 0\n"
"v_mov_b32 v99, 0\n"
"v_mov_b32 v100, 0\n"
"v_mov_b32 v101, 0\n"
"v_mov_b32 v102, 0\n"
"v_mov_b32 v103, 0\n"
"v_mov_b32 v104, 0\n"
"v_mov_b32 v105, 0\n"
"v_mov_b32 v106, 0\n"
"v_mov_b32 v107, 0\n"
"v_mov_b32 v108, 0\n"
"v_mov_b32 v109, 0\n"
"v_mov_b32 v110, 0\n"
"v_mov_b32 v111, 0\n"
"v_mov_b32 v112, 0\n"
"v_mov_b32 v113, 0\n"
"v_mov_b32 v114, 0\n"
"v_mov_b32 v115, 0\n"
"v_mov_b32 v116, 0\n"
"v_mov_b32 v117, 0\n"
"v_mov_b32 v118, 0\n"
"v_mov_b32 v119, 0\n"
"v_mov_b32 v120, 0\n"
"v_mov_b32 v121, 0\n"
"v_mov_b32 v122, 0\n"
"v_mov_b32 v123, 0\n"
"v_mov_b32 v124, 0\n"
"v_mov_b32 v125, 0\n"
"v_mov_b32 v126, 0\n"
"v_mov_b32 v127, 0\n"
"v_accvgpr_write_b32 a236, 0\n"
"v_accvgpr_write_b32 a237, 0\n"
"v_accvgpr_write_b32 a238, 0\n"
"v_accvgpr_write_b32 a239, 0\n"
"v_accvgpr_write_b32 a240, 0\n"
"v_accvgpr_write_b32 a241, 0\n"
"v_accvgpr_write_b32 a242, 0\n"
"v_accvgpr_write_b32 a243, 0\n"
"s_and_b32 s20, s19, 0xfffe0000\n"
"s_mov_b32 s31, 0\n"
"L_fb20:\n"
"s_add_u32 m0, s21, 0\n"
"s_add_u32 s22, s20, 0\n"
"buffer_load_dwordx4 v238, s[44:47], s22 offen offset:0 lds\n"
"buffer_load_dwordx4 v239, s[44:47], s22 offen offset:1024 lds\n"
"buffer_load_dwordx4 v240, s[44:47], s22 offen offset:2048 lds\n"
"buffer_load_dwordx4 v241, s[44:47], s22 offen offset:3072 lds\n"
"s_add_u32 m0, s21, 32768\n"
"s_nop 0\n"
"buffer_load_dwordx4 v212, s[48:51], s22 offen offset:0 lds\n"
"buffer_load_dwordx4 v213, s[48:51], s22 offen offset:1024 lds\n"
"buffer_load_dwordx4 v212, s[48:51], s22 offen offset:2048 lds\n"
"buffer_load_dwordx4 v213, s[48:51], s22 offen offset:3072 lds\n"
"s_add_u32 m0, s21, 16384\n"
"s_add_u32 s22, s20, 4096\n"
"buffer_load_dwordx4 v238, s[44:47], s22 offen offset:0 lds\n"
"buffer_load_dwordx4 v239, s[44:47], s22 offen offset:1024 lds\n"
"buffer_load_dwordx4 v240, s[44:47], s22 offen offset:2048 lds\n"
"buffer_load_dwordx4 v241, s[44:47], s22 offen offset:3072 lds\n"
"s_add_u32 m0, s21, 49152\n"
"s_nop 0\n"
"buffer_load_dwordx4 v212, s[48:51], s22 offen offset:0 lds\n"
"buffer_load_dwordx4 v213, s[48:51], s22 offen offset:1024 lds\n"
"buffer_load_dwordx4 v212, s[48:51], s22 offen offset:2048 lds\n"
"buffer_load_dwordx4 v213, s[48:51], s22 offen offset:3072 lds\n"
"s_waitcnt vmcnt(0)\n"
"ds_read_b128 a[172:175], v224 offset:0\n"
"ds_read_b128 a[176:179], v225 offset:0\n"
"ds_read_b128 a[180:183], v226 offset:0\n"
"ds_read_b128 a[184:187], v227 offset:0\n"
"ds_read_b128 a[188:191], v224 offset:16384\n"
"ds_read_b128 a[192:195], v225 offset:16384\n"
"ds_read_b128 a[196:199], v226 offset:16384\n"
"ds_read_b128 a[200:203], v227 offset:16384\n"
"ds_read_b64_tr_b16 a[204:205], v216 offset:32768\n"
"ds_read_b64_tr_b16 a[206:207], v216 offset:49152\n"
"ds_read_b64_tr_b16 a[208:209], v217 offset:32768\n"
"ds_read_b64_tr_b16 a[210:211], v217 offset:49152\n"
"ds_read_b64_tr_b16 a[212:213], v218 offset:32768\n"
"ds_read_b64_tr_b16 a[214:215], v218 offset:49152\n"
"ds_read_b64_tr_b16 a[216:217], v219 offset:32768\n"
"ds_read_b64_tr_b16 a[218:219], v219 offset:49152\n"
"ds_read_b64_tr_b16 a[220:221], v220 offset:32768\n"
"ds_read_b64_tr_b16 a[222:223], v220 offset:49152\n"
"ds_read_b64_tr_b16 a[224:225], v221 offset:32768\n"
"ds_read_b64_tr_b16 a[226:227], v221 offset:49152\n"
"ds_read_b64_tr_b16 a[228:229], v222 offset:32768\n"
"ds_read_b64_tr_b16 a[230:231], v222 offset:49152\n"
"ds_read_b64_tr_b16 a[232:233], v223 offset:32768\n"
"ds_read_b64_tr_b16 a[234:235], v223 offset:49152\n"
"s_waitcnt lgkmcnt(0)\n"
"v_mfma_f32_16x16x32_bf16 v[192:195], a[172:175], v[128:131], 0\n"
"v_mfma_f32_16x16x32_bf16 v[192:195], a[176:179], v[132:135], v[192:195]\n"
"v_mfma_f32_16x16x32_bf16 v[192:195], a[180:183], v[136:139], v[192:195]\n"
"v_mfma_f32_16x16x32_bf16 v[192:195], a[184:187], v[140:143], v[192:195]\n"
"v_mfma_f32_16x16x32_bf16 v[196:199], a[188:191], v[128:131], 0\n"
"v_mfma_f32_16x16x32_bf16 v[196:199], a[192:195], v[132:135], v[196:199]\n"
"v_mfma_f32_16x16x32_bf16 v[196:199], a[196:199], v[136:139], v[196:199]\n"
"v_mfma_f32_16x16x32_bf16 v[196:199], a[200:203], v[140:143], v[196:199]\n"
"s_nop 3\n"
"v_sub_f32 v192, v192, v252\n"
"v_sub_f32 v193, v193, v252\n"
"v_sub_f32 v194, v194, v252\n"
"v_sub_f32 v195, v195, v252\n"
"v_sub_f32 v196, v196, v252\n"
"v_sub_f32 v197, v197, v252\n"
"v_sub_f32 v198, v198, v252\n"
"v_sub_f32 v199, v199, v252\n"
"v_exp_f32 v192, v192\n"
"v_exp_f32 v193, v193\n"
"v_exp_f32 v194, v194\n"
"v_exp_f32 v195, v195\n"
"v_exp_f32 v196, v196\n"
"v_exp_f32 v197, v197\n"
"v_exp_f32 v198, v198\n"
"v_exp_f32 v199, v199\n"
"v_cvt_pk_bf16_f32 v208, v192, v193\n"
"v_cvt_pk_bf16_f32 v209, v194, v195\n"
"v_cvt_pk_bf16_f32 v210, v196, v197\n"
"v_cvt_pk_bf16_f32 v211, v198, v199\n"
"s_nop 1\n"
"v_mfma_f32_16x16x32_bf16 v[64:67], a[204:207], v[208:211], v[64:67]\n"
"v_mfma_f32_16x16x32_bf16 v[68:71], a[208:211], v[208:211], v[68:71]\n"
"v_mfma_f32_16x16x32_bf16 v[72:75], a[212:215], v[208:211], v[72:75]\n"
"v_mfma_f32_16x16x32_bf16 v[76:79], a[216:219], v[208:211], v[76:79]\n"
"v_mfma_f32_16x16x32_bf16 v[80:83], a[220:223], v[208:211], v[80:83]\n"
"v_mfma_f32_16x16x32_bf16 v[84:87], a[224:227], v[208:211], v[84:87]\n"
"v_mfma_f32_16x16x32_bf16 v[88:91], a[228:231], v[208:211], v[88:91]\n"
"v_mfma_f32_16x16x32_bf16 v[92:95], a[232:235], v[208:211], v[92:95]\n"
"v_mfma_f32_16x16x32_bf16 a[236:239], a[252:255], v[208:211], a[236:239]\n"
"v_mfma_f32_16x16x32_bf16 v[192:195], a[172:175], v[144:147], 0\n"
"v_mfma_f32_16x16x32_bf16 v[192:195], a[176:179], v[148:151], v[192:195]\n"
"v_mfma_f32_16x16x32_bf16 v[192:195], a[180:183], v[152:155], v[192:195]\n"
"v_mfma_f32_16x16x32_bf16 v[192:195], a[184:187], v[156:159], v[192:195]\n"
"v_mfma_f32_16x16x32_bf16 v[196:199], a[188:191], v[144:147], 0\n"
"v_mfma_f32_16x16x32_bf16 v[196:199], a[192:195], v[148:151], v[196:199]\n"
"v_mfma_f32_16x16x32_bf16 v[196:199], a[196:199], v[152:155], v[196:199]\n"
"v_mfma_f32_16x16x32_bf16 v[196:199], a[200:203], v[156:159], v[196:199]\n"
"s_nop 3\n"
"v_sub_f32 v192, v192, v253\n"
"v_sub_f32 v193, v193, v253\n"
"v_sub_f32 v194, v194, v253\n"
"v_sub_f32 v195, v195, v253\n"
"v_sub_f32 v196, v196, v253\n"
"v_sub_f32 v197, v197, v253\n"
"v_sub_f32 v198, v198, v253\n"
"v_sub_f32 v199, v199, v253\n"
"v_exp_f32 v192, v192\n"
"v_exp_f32 v193, v193\n"
"v_exp_f32 v194, v194\n"
"v_exp_f32 v195, v195\n"
"v_exp_f32 v196, v196\n"
"v_exp_f32 v197, v197\n"
"v_exp_f32 v198, v198\n"
"v_exp_f32 v199, v199\n"
"v_cvt_pk_bf16_f32 v208, v192, v193\n"
"v_cvt_pk_bf16_f32 v209, v194, v195\n"
"v_cvt_pk_bf16_f32 v210, v196, v197\n"
"v_cvt_pk_bf16_f32 v211, v198, v199\n"
"s_nop 1\n"
"v_mfma_f32_16x16x32_bf16 v[96:99], a[204:207], v[208:211], v[96:99]\n"
"v_mfma_f32_16x16x32_bf16 v[100:103], a[208:211], v[208:211], v[100:103]\n"
"v_mfma_f32_16x16x32_bf16 v[104:107], a[212:215], v[208:211], v[104:107]\n"
"v_mfma_f32_16x16x32_bf16 v[108:111], a[216:219], v[208:211], v[108:111]\n"
"v_mfma_f32_16x16x32_bf16 v[112:115], a[220:223], v[208:211], v[112:115]\n"
"v_mfma_f32_16x16x32_bf16 v[116:119], a[224:227], v[208:211], v[116:119]\n"
"v_mfma_f32_16x16x32_bf16 v[120:123], a[228:231], v[208:211], v[120:123]\n"
"v_mfma_f32_16x16x32_bf16 v[124:127], a[232:235], v[208:211], v[124:127]\n"
"v_mfma_f32_16x16x32_bf16 a[240:243], a[252:255], v[208:211], a[240:243]\n"
"s_nop 15\n"
"s_add_u32 s20, s20, 8192\n"
"s_add_u32 s31, s31, 1\n"
"s_cmp_lt_u32 s31, 16\n"
"s_cbranch_scc1 L_fb20\n"
"v_accvgpr_read_b32 v248, a236\n"
"s_add_u32 s99, s88, 0\n"
"v_rcp_f32 v248, v248\n"
"s_nop 0\n"
"v_mul_f32 v64, v64, v248\n"
"v_mul_f32 v65, v65, v248\n"
"v_mul_f32 v66, v66, v248\n"
"v_mul_f32 v67, v67, v248\n"
"v_mul_f32 v68, v68, v248\n"
"v_mul_f32 v69, v69, v248\n"
"v_mul_f32 v70, v70, v248\n"
"v_mul_f32 v71, v71, v248\n"
"v_cvt_pk_bf16_f32 v64, v64, v65\n"
"v_cvt_pk_bf16_f32 v65, v66, v67\n"
"v_cvt_pk_bf16_f32 v66, v68, v69\n"
"v_cvt_pk_bf16_f32 v67, v70, v71\n"
"buffer_store_dwordx4 v[64:67], v243, s[56:59], s99 offen offset:0 sc1\n"
"v_mul_f32 v72, v72, v248\n"
"v_mul_f32 v73, v73, v248\n"
"v_mul_f32 v74, v74, v248\n"
"v_mul_f32 v75, v75, v248\n"
"v_mul_f32 v76, v76, v248\n"
"v_mul_f32 v77, v77, v248\n"
"v_mul_f32 v78, v78, v248\n"
"v_mul_f32 v79, v79, v248\n"
"v_cvt_pk_bf16_f32 v72, v72, v73\n"
"v_cvt_pk_bf16_f32 v73, v74, v75\n"
"v_cvt_pk_bf16_f32 v74, v76, v77\n"
"v_cvt_pk_bf16_f32 v75, v78, v79\n"
"buffer_store_dwordx4 v[72:75], v243, s[56:59], s99 offen offset:1024 sc1\n"
"v_mul_f32 v80, v80, v248\n"
"v_mul_f32 v81, v81, v248\n"
"v_mul_f32 v82, v82, v248\n"
"v_mul_f32 v83, v83, v248\n"
"v_mul_f32 v84, v84, v248\n"
"v_mul_f32 v85, v85, v248\n"
"v_mul_f32 v86, v86, v248\n"
"v_mul_f32 v87, v87, v248\n"
"v_cvt_pk_bf16_f32 v80, v80, v81\n"
"v_cvt_pk_bf16_f32 v81, v82, v83\n"
"v_cvt_pk_bf16_f32 v82, v84, v85\n"
"v_cvt_pk_bf16_f32 v83, v86, v87\n"
"buffer_store_dwordx4 v[80:83], v243, s[56:59], s99 offen offset:2048 sc1\n"
"v_mul_f32 v88, v88, v248\n"
"v_mul_f32 v89, v89, v248\n"
"v_mul_f32 v90, v90, v248\n"
"v_mul_f32 v91, v91, v248\n"
"v_mul_f32 v92, v92, v248\n"
"v_mul_f32 v93, v93, v248\n"
"v_mul_f32 v94, v94, v248\n"
"v_mul_f32 v95, v95, v248\n"
"v_cvt_pk_bf16_f32 v88, v88, v89\n"
"v_cvt_pk_bf16_f32 v89, v90, v91\n"
"v_cvt_pk_bf16_f32 v90, v92, v93\n"
"v_cvt_pk_bf16_f32 v91, v94, v95\n"
"buffer_store_dwordx4 v[88:91], v243, s[56:59], s99 offen offset:3072 sc1\n"
"v_accvgpr_read_b32 v250, a240\n"
"s_add_u32 s99, s88, 32768\n"
"v_rcp_f32 v250, v250\n"
"s_nop 0\n"
"v_mul_f32 v96, v96, v250\n"
"v_mul_f32 v97, v97, v250\n"
"v_mul_f32 v98, v98, v250\n"
"v_mul_f32 v99, v99, v250\n"
"v_mul_f32 v100, v100, v250\n"
"v_mul_f32 v101, v101, v250\n"
"v_mul_f32 v102, v102, v250\n"
"v_mul_f32 v103, v103, v250\n"
"v_cvt_pk_bf16_f32 v96, v96, v97\n"
"v_cvt_pk_bf16_f32 v97, v98, v99\n"
"v_cvt_pk_bf16_f32 v98, v100, v101\n"
"v_cvt_pk_bf16_f32 v99, v102, v103\n"
"buffer_store_dwordx4 v[96:99], v243, s[56:59], s99 offen offset:0 sc1\n"
"v_mul_f32 v104, v104, v250\n"
"v_mul_f32 v105, v105, v250\n"
"v_mul_f32 v106, v106, v250\n"
"v_mul_f32 v107, v107, v250\n"
"v_mul_f32 v108, v108, v250\n"
"v_mul_f32 v109, v109, v250\n"
"v_mul_f32 v110, v110, v250\n"
"v_mul_f32 v111, v111, v250\n"
"v_cvt_pk_bf16_f32 v104, v104, v105\n"
"v_cvt_pk_bf16_f32 v105, v106, v107\n"
"v_cvt_pk_bf16_f32 v106, v108, v109\n"
"v_cvt_pk_bf16_f32 v107, v110, v111\n"
"buffer_store_dwordx4 v[104:107], v243, s[56:59], s99 offen offset:1024 sc1\n"
"v_mul_f32 v112, v112, v250\n"
"v_mul_f32 v113, v113, v250\n"
"v_mul_f32 v114, v114, v250\n"
"v_mul_f32 v115, v115, v250\n"
"v_mul_f32 v116, v116, v250\n"
"v_mul_f32 v117, v117, v250\n"
"v_mul_f32 v118, v118, v250\n"
"v_mul_f32 v119, v119, v250\n"
"v_cvt_pk_bf16_f32 v112, v112, v113\n"
"v_cvt_pk_bf16_f32 v113, v114, v115\n"
"v_cvt_pk_bf16_f32 v114, v116, v117\n"
"v_cvt_pk_bf16_f32 v115, v118, v119\n"
"buffer_store_dwordx4 v[112:115], v243, s[56:59], s99 offen offset:2048 sc1\n"
"v_mul_f32 v120, v120, v250\n"
"v_mul_f32 v121, v121, v250\n"
"v_mul_f32 v122, v122, v250\n"
"v_mul_f32 v123, v123, v250\n"
"v_mul_f32 v124, v124, v250\n"
"v_mul_f32 v125, v125, v250\n"
"v_mul_f32 v126, v126, v250\n"
"v_mul_f32 v127, v127, v250\n"
"v_cvt_pk_bf16_f32 v120, v120, v121\n"
"v_cvt_pk_bf16_f32 v121, v122, v123\n"
"v_cvt_pk_bf16_f32 v122, v124, v125\n"
"v_cvt_pk_bf16_f32 v123, v126, v127\n"
"buffer_store_dwordx4 v[120:123], v243, s[56:59], s99 offen offset:3072 sc1\n"
"s_waitcnt vmcnt(0)\n"
"s_add_u32 s98, s91, 0\n"
"v_mov_b32 v252, s14\n"
"v_mov_b32 v253, s98\n"
"s_mov_b64 exec, 1\n"
"buffer_store_dword v252, v253, s[56:59], 0 offen sc1\n"
"s_mov_b64 exec, -1\n"
"L_fb_skipA:\n"
"s_nop 3\n"
"v_mov_b32 v64, 0\n"
"v_mov_b32 v65, 0\n"
"v_mov_b32 v66, 0\n"
"v_mov_b32 v67, 0\n"
"v_mov_b32 v68, 0\n"
"v_mov_b32 v69, 0\n"
"v_mov_b32 v70, 0\n"
"v_mov_b32 v71, 0\n"
"v_mov_b32 v72, 0\n"
"v_mov_b32 v73, 0\n"
"v_mov_b32 v74, 0\n"
"v_mov_b32 v75, 0\n"
"v_mov_b32 v76, 0\n"
"v_mov_b32 v77, 0\n"
"v_mov_b32 v78, 0\n"
"v_mov_b32 v79, 0\n"
"v_mov_b32 v80, 0\n"
"v_mov_b32 v81, 0\n"
"v_mov_b32 v82, 0\n"
"v_mov_b32 v83, 0\n"
"v_mov_b32 v84, 0\n"
"v_mov_b32 v85, 0\n"
"v_mov_b32 v86, 0\n"
"v_mov_b32 v87, 0\n"
"v_mov_b32 v88, 0\n"
"v_mov_b32 v89, 0\n"
"v_mov_b32 v90, 0\n"
"v_mov_b32 v91, 0\n"
"v_mov_b32 v92, 0\n"
"v_mov_b32 v93, 0\n"
"v_mov_b32 v94, 0\n"
"v_mov_b32 v95, 0\n"
"v_mov_b32 v96, 0\n"
"v_mov_b32 v97, 0\n"
"v_mov_b32 v98, 0\n"
"v_mov_b32 v99, 0\n"
"v_mov_b32 v100, 0\n"
"v_mov_b32 v101, 0\n"
"v_mov_b32 v102, 0\n"
"v_mov_b32 v103, 0\n"
"v_mov_b32 v104, 0\n"
"v_mov_b32 v105, 0\n"
"v_mov_b32 v106, 0\n"
"v_mov_b32 v107, 0\n"
"v_mov_b32 v108, 0\n"
"v_mov_b32 v109, 0\n"
"v_mov_b32 v110, 0\n"
"v_mov_b32 v111, 0\n"
"v_mov_b32 v112, 0\n"
"v_mov_b32 v113, 0\n"
"v_mov_b32 v114, 0\n"
"v_mov_b32 v115, 0\n"
"v_mov_b32 v116, 0\n"
"v_mov_b32 v117, 0\n"
"v_mov_b32 v118, 0\n"
"v_mov_b32 v119, 0\n"
"v_mov_b32 v120, 0\n"
"v_mov_b32 v121, 0\n"
"v_mov_b32 v122, 0\n"
"v_mov_b32 v123, 0\n"
"v_mov_b32 v124, 0\n"
"v_mov_b32 v125, 0\n"
"v_mov_b32 v126, 0\n"
"v_mov_b32 v127, 0\n"
"v_accvgpr_write_b32 a244, 0\n"
"v_accvgpr_write_b32 a245, 0\n"
"v_accvgpr_write_b32 a246, 0\n"
"v_accvgpr_write_b32 a247, 0\n"
"v_accvgpr_write_b32 a248, 0\n"
"v_accvgpr_write_b32 a249, 0\n"
"v_accvgpr_write_b32 a250, 0\n"
"v_accvgpr_write_b32 a251, 0\n"
"s_and_b32 s20, s19, 0xfffe0000\n"
"s_mov_b32 s31, 0\n"
"L_fb21:\n"
"s_add_u32 m0, s21, 0\n"
"s_add_u32 s22, s20, 0\n"
"buffer_load_dwordx4 v238, s[44:47], s22 offen offset:0 lds\n"
"buffer_load_dwordx4 v239, s[44:47], s22 offen offset:1024 lds\n"
"buffer_load_dwordx4 v240, s[44:47], s22 offen offset:2048 lds\n"
"buffer_load_dwordx4 v241, s[44:47], s22 offen offset:3072 lds\n"
"s_add_u32 m0, s21, 32768\n"
"s_nop 0\n"
"buffer_load_dwordx4 v212, s[48:51], s22 offen offset:0 lds\n"
"buffer_load_dwordx4 v213, s[48:51], s22 offen offset:1024 lds\n"
"buffer_load_dwordx4 v212, s[48:51], s22 offen offset:2048 lds\n"
"buffer_load_dwordx4 v213, s[48:51], s22 offen offset:3072 lds\n"
"s_add_u32 m0, s21, 16384\n"
"s_add_u32 s22, s20, 4096\n"
"buffer_load_dwordx4 v238, s[44:47], s22 offen offset:0 lds\n"
"buffer_load_dwordx4 v239, s[44:47], s22 offen offset:1024 lds\n"
"buffer_load_dwordx4 v240, s[44:47], s22 offen offset:2048 lds\n"
"buffer_load_dwordx4 v241, s[44:47], s22 offen offset:3072 lds\n"
"s_add_u32 m0, s21, 49152\n"
"s_nop 0\n"
"buffer_load_dwordx4 v212, s[48:51], s22 offen offset:0 lds\n"
"buffer_load_dwordx4 v213, s[48:51], s22 offen offset:1024 lds\n"
"buffer_load_dwordx4 v212, s[48:51], s22 offen offset:2048 lds\n"
"buffer_load_dwordx4 v213, s[48:51], s22 offen offset:3072 lds\n"
"s_waitcnt vmcnt(0)\n"
"ds_read_b128 a[172:175], v224 offset:0\n"
"ds_read_b128 a[176:179], v225 offset:0\n"
"ds_read_b128 a[180:183], v226 offset:0\n"
"ds_read_b128 a[184:187], v227 offset:0\n"
"ds_read_b128 a[188:191], v224 offset:16384\n"
"ds_read_b128 a[192:195], v225 offset:16384\n"
"ds_read_b128 a[196:199], v226 offset:16384\n"
"ds_read_b128 a[200:203], v227 offset:16384\n"
"ds_read_b64_tr_b16 a[204:205], v216 offset:32768\n"
"ds_read_b64_tr_b16 a[206:207], v216 offset:49152\n"
"ds_read_b64_tr_b16 a[208:209], v217 offset:32768\n"
"ds_read_b64_tr_b16 a[210:211], v217 offset:49152\n"
"ds_read_b64_tr_b16 a[212:213], v218 offset:32768\n"
"ds_read_b64_tr_b16 a[214:215], v218 offset:49152\n"
"ds_read_b64_tr_b16 a[216:217], v219 offset:32768\n"
"ds_read_b64_tr_b16 a[218:219], v219 offset:49152\n"
"ds_read_b64_tr_b16 a[220:221], v220 offset:32768\n"
"ds_read_b64_tr_b16 a[222:223], v220 offset:49152\n"
"ds_read_b64_tr_b16 a[224:225], v221 offset:32768\n"
"ds_read_b64_tr_b16 a[226:227], v221 offset:49152\n"
"ds_read_b64_tr_b16 a[228:229], v222 offset:32768\n"
"ds_read_b64_tr_b16 a[230:231], v222 offset:49152\n"
"ds_read_b64_tr_b16 a[232:233], v223 offset:32768\n"
"ds_read_b64_tr_b16 a[234:235], v223 offset:49152\n"
"s_waitcnt lgkmcnt(0)\n"
"v_mfma_f32_16x16x32_bf16 v[192:195], a[172:175], v[160:163], 0\n"
"v_mfma_f32_16x16x32_bf16 v[192:195], a[176:179], v[164:167], v[192:195]\n"
"v_mfma_f32_16x16x32_bf16 v[192:195], a[180:183], v[168:171], v[192:195]\n"
"v_mfma_f32_16x16x32_bf16 v[192:195], a[184:187], v[172:175], v[192:195]\n"
"v_mfma_f32_16x16x32_bf16 v[196:199], a[188:191], v[160:163], 0\n"
"v_mfma_f32_16x16x32_bf16 v[196:199], a[192:195], v[164:167], v[196:199]\n"
"v_mfma_f32_16x16x32_bf16 v[196:199], a[196:199], v[168:171], v[196:199]\n"
"v_mfma_f32_16x16x32_bf16 v[196:199], a[200:203], v[172:175], v[196:199]\n"
"s_nop 3\n"
"v_sub_f32 v192, v192, v254\n"
"v_sub_f32 v193, v193, v254\n"
"v_sub_f32 v194, v194, v254\n"
"v_sub_f32 v195, v195, v254\n"
"v_sub_f32 v196, v196, v254\n"
"v_sub_f32 v197, v197, v254\n"
"v_sub_f32 v198, v198, v254\n"
"v_sub_f32 v199, v199, v254\n"
"v_exp_f32 v192, v192\n"
"v_exp_f32 v193, v193\n"
"v_exp_f32 v194, v194\n"
"v_exp_f32 v195, v195\n"
"v_exp_f32 v196, v196\n"
"v_exp_f32 v197, v197\n"
"v_exp_f32 v198, v198\n"
"v_exp_f32 v199, v199\n"
"v_cvt_pk_bf16_f32 v208, v192, v193\n"
"v_cvt_pk_bf16_f32 v209, v194, v195\n"
"v_cvt_pk_bf16_f32 v210, v196, v197\n"
"v_cvt_pk_bf16_f32 v211, v198, v199\n"
"s_nop 1\n"
"v_mfma_f32_16x16x32_bf16 v[64:67], a[204:207], v[208:211], v[64:67]\n"
"v_mfma_f32_16x16x32_bf16 v[68:71], a[208:211], v[208:211], v[68:71]\n"
"v_mfma_f32_16x16x32_bf16 v[72:75], a[212:215], v[208:211], v[72:75]\n"
"v_mfma_f32_16x16x32_bf16 v[76:79], a[216:219], v[208:211], v[76:79]\n"
"v_mfma_f32_16x16x32_bf16 v[80:83], a[220:223], v[208:211], v[80:83]\n"
"v_mfma_f32_16x16x32_bf16 v[84:87], a[224:227], v[208:211], v[84:87]\n"
"v_mfma_f32_16x16x32_bf16 v[88:91], a[228:231], v[208:211], v[88:91]\n"
"v_mfma_f32_16x16x32_bf16 v[92:95], a[232:235], v[208:211], v[92:95]\n"
"v_mfma_f32_16x16x32_bf16 a[244:247], a[252:255], v[208:211], a[244:247]\n"
"v_mfma_f32_16x16x32_bf16 v[192:195], a[172:175], v[176:179], 0\n"
"v_mfma_f32_16x16x32_bf16 v[192:195], a[176:179], v[180:183], v[192:195]\n"
"v_mfma_f32_16x16x32_bf16 v[192:195], a[180:183], v[184:187], v[192:195]\n"
"v_mfma_f32_16x16x32_bf16 v[192:195], a[184:187], v[188:191], v[192:195]\n"
"v_mfma_f32_16x16x32_bf16 v[196:199], a[188:191], v[176:179], 0\n"
"v_mfma_f32_16x16x32_bf16 v[196:199], a[192:195], v[180:183], v[196:199]\n"
"v_mfma_f32_16x16x32_bf16 v[196:199], a[196:199], v[184:187], v[196:199]\n"
"v_mfma_f32_16x16x32_bf16 v[196:199], a[200:203], v[188:191], v[196:199]\n"
"s_nop 3\n"
"v_sub_f32 v192, v192, v251\n"
"v_sub_f32 v193, v193, v251\n"
"v_sub_f32 v194, v194, v251\n"
"v_sub_f32 v195, v195, v251\n"
"v_sub_f32 v196, v196, v251\n"
"v_sub_f32 v197, v197, v251\n"
"v_sub_f32 v198, v198, v251\n"
"v_sub_f32 v199, v199, v251\n"
"v_exp_f32 v192, v192\n"
"v_exp_f32 v193, v193\n"
"v_exp_f32 v194, v194\n"
"v_exp_f32 v195, v195\n"
"v_exp_f32 v196, v196\n"
"v_exp_f32 v197, v197\n"
"v_exp_f32 v198, v198\n"
"v_exp_f32 v199, v199\n"
"v_cvt_pk_bf16_f32 v208, v192, v193\n"
"v_cvt_pk_bf16_f32 v209, v194, v195\n"
"v_cvt_pk_bf16_f32 v210, v196, v197\n"
"v_cvt_pk_bf16_f32 v211, v198, v199\n"
"s_nop 1\n"
"v_mfma_f32_16x16x32_bf16 v[96:99], a[204:207], v[208:211], v[96:99]\n"
"v_mfma_f32_16x16x32_bf16 v[100:103], a[208:211], v[208:211], v[100:103]\n"
"v_mfma_f32_16x16x32_bf16 v[104:107], a[212:215], v[208:211], v[104:107]\n"
"v_mfma_f32_16x16x32_bf16 v[108:111], a[216:219], v[208:211], v[108:111]\n"
"v_mfma_f32_16x16x32_bf16 v[112:115], a[220:223], v[208:211], v[112:115]\n"
"v_mfma_f32_16x16x32_bf16 v[116:119], a[224:227], v[208:211], v[116:119]\n"
"v_mfma_f32_16x16x32_bf16 v[120:123], a[228:231], v[208:211], v[120:123]\n"
"v_mfma_f32_16x16x32_bf16 v[124:127], a[232:235], v[208:211], v[124:127]\n"
"v_mfma_f32_16x16x32_bf16 a[248:251], a[252:255], v[208:211], a[248:251]\n"
"s_nop 15\n"
"s_add_u32 s20, s20, 8192\n"
"s_add_u32 s31, s31, 1\n"
"s_cmp_lt_u32 s31, 16\n"
"s_cbranch_scc1 L_fb21\n"
"s_add_u32 m0, s90, 65536\n"
"s_xor_b32 s24, s89, 327680\n"
"buffer_load_dwordx4 v230, s[52:55], s24 offen offset:0 lds\n"
"buffer_load_dwordx4 v231, s[52:55], s24 offen offset:1024 lds\n"
"buffer_load_dwordx4 v232, s[52:55], s24 offen offset:2048 lds\n"
"buffer_load_dwordx4 v233, s[52:55], s24 offen offset:3072 lds\n"
"s_add_u32 m0, s90, 81920\n"
"s_xor_b32 s24, s89, 360448\n"
"buffer_load_dwordx4 v230, s[52:55], s24 offen offset:0 lds\n"
"buffer_load_dwordx4 v231, s[52:55], s24 offen offset:1024 lds\n"
"buffer_load_dwordx4 v232, s[52:55], s24 offen offset:2048 lds\n"
"buffer_load_dwordx4 v233, s[52:55], s24 offen offset:3072 lds\n"
"s_add_u32 m0, s90, 98304\n"
"s_xor_b32 s24, s89, 393216\n"
"buffer_load_dwordx4 v230, s[52:55], s24 offen offset:0 lds\n"
"buffer_load_dwordx4 v231, s[52:55], s24 offen offset:1024 lds\n"
"buffer_load_dwordx4 v232, s[52:55], s24 offen offset:2048 lds\n"
"buffer_load_dwordx4 v233, s[52:55], s24 offen offset:3072 lds\n"
"s_add_u32 m0, s90, 114688\n"
"s_xor_b32 s24, s89, 425984\n"
"buffer_load_dwordx4 v230, s[52:55], s24 offen offset:0 lds\n"
"buffer_load_dwordx4 v231, s[52:55], s24 offen offset:1024 lds\n"
"buffer_load_dwordx4 v232, s[52:55], s24 offen offset:2048 lds\n"
"buffer_load_dwordx4 v233, s[52:55], s24 offen offset:3072 lds\n"
"v_subrev_u32 v238, 65536, v234\n"
"v_subrev_u32 v239, 65536, v235\n"
"v_subrev_u32 v240, 65536, v236\n"
"v_subrev_u32 v241, 65536, v237\n"
"v_accvgpr_read_b32 v250, a248\n"
"v_rcp_f32 v250, v250\n"
"v_accvgpr_read_b32 v248, a244\n"
"s_add_u32 s99, s88, 8388608\n"
"v_rcp_f32 v248, v248\n"
"s_add_u32 s20, s32, 44032\n"
"s_add_u32 s22, s32, 88064\n"
"s_add_u32 s23, s32, 132096\n"
"s_waitcnt vmcnt(0)\n"
"s_nop 7\n"
"s_branch L_fb_back\n"
"L_end:\n"
".p2align 12\n"

  :: "s"(__builtin_amdgcn_kernarg_segment_ptr()), "s"(blockIdx.x), "s"(w), "s"((unsigned)(size_t)lds)
  : "v0","v1","v2","v3","v4","v5","v6","v7","v8","v9","v10","v11","v12","v13","v14","v15","v16","v17","v18","v19","v20","v21","v22","v23","v24","v25","v26","v27","v28","v29","v30","v31","v32","v33","v34","v35","v36","v37","v38","v39","v40","v41","v42","v43","v44","v45","v46","v47","v48","v49","v50","v51","v52","v53","v54","v55","v56","v57","v58","v59","v60","v61","v62","v63","v64","v65","v66","v67","v68","v69","v70","v71","v72","v73","v74","v75","v76","v77","v78","v79","v80","v81","v82","v83","v84","v85","v86","v87","v88","v89","v90","v91","v92","v93","v94","v95","v96","v97","v98","v99","v100","v101","v102","v103","v104","v105","v106","v107","v108","v109","v110","v111","v112","v113","v114","v115","v116","v117","v118","v119","v120","v121","v122","v123","v124","v125","v126","v127","v128","v129","v130","v131","v132","v133","v134","v135","v136","v137","v138","v139","v140","v141","v142","v143","v144","v145","v146","v147","v148","v149","v150","v151","v152","v153","v154","v155","v156","v157","v158","v159","v160","v161","v162","v163","v164","v165","v166","v167","v168","v169","v170","v171","v172","v173","v174","v175","v176","v177","v178","v179","v180","v181","v182","v183","v184","v185","v186","v187","v188","v189","v190","v191","v192","v193","v194","v195","v196","v197","v198","v199","v200","v201","v202","v203","v204","v205","v206","v207","v208","v209","v210","v211","v212","v213","v214","v215","v216","v217","v218","v219","v220","v221","v222","v223","v224","v225","v226","v227","v228","v229","v230","v231","v232","v233","v234","v235","v236","v237","v238","v239","v240","v241","v242","v243","v244","v245","v246","v247","v248","v249","v250","v251","v252","v253","v254","v255","a0","a1","a2","a3","a4","a5","a6","a7","a8","a9","a10","a11","a12","a13","a14","a15","a16","a17","a18","a19","a20","a21","a22","a23","a24","a25","a26","a27","a28","a29","a30","a31","a32","a33","a34","a35","a36","a37","a38","a39","a40","a41","a42","a43","a44","a45","a46","a47","a48","a49","a50","a51","a52","a53","a54","a55","a56","a57","a58","a59","a60","a61","a62","a63","a64","a65","a66","a67","a68","a69","a70","a71","a72","a73","a74","a75","a76","a77","a78","a79","a80","a81","a82","a83","a84","a85","a86","a87","a88","a89","a90","a91","a92","a93","a94","a95","a96","a97","a98","a99","a100","a101","a102","a103","a104","a105","a106","a107","a108","a109","a110","a111","a112","a113","a114","a115","a116","a117","a118","a119","a120","a121","a122","a123","a124","a125","a126","a127","a128","a129","a130","a131","a132","a133","a134","a135","a136","a137","a138","a139","a140","a141","a142","a143","a144","a145","a146","a147","a148","a149","a150","a151","a152","a153","a154","a155","a156","a157","a158","a159","a160","a161","a162","a163","a164","a165","a166","a167","a168","a169","a170","a171","a172","a173","a174","a175","a176","a177","a178","a179","a180","a181","a182","a183","a184","a185","a186","a187","a188","a189","a190","a191","a192","a193","a194","a195","a196","a197","a198","a199","a200","a201","a202","a203","a204","a205","a206","a207","a208","a209","a210","a211","a212","a213","a214","a215","a216","a217","a218","a219","a220","a221","a222","a223","a224","a225","a226","a227","a228","a229","a230","a231","a232","a233","a234","a235","a236","a237","a238","a239","a240","a241","a242","a243","a244","a245","a246","a247","a248","a249","a250","a251","a252","a253","a254","a255","s8","s9","s10","s11","s12","s13","s14","s15","s16","s17","s18","s19","s20","s21","s22","s23","s24","s25","s26","s27","s28","s29","s30","s31","s34","s35","s36","s37","s38","s39","s40","s41","s42","s43","s44","s45","s46","s47","s48","s49","s50","s51","s52","s53","s54","s55","s56","s57","s58","s59","s60","s61","s62","s63","s64","s65","s66","s67","s68","s69","s70","s71","s72","s73","s74","s75","s76","s77","s78","s79","s80","s81","s82","s83","s84","s85","s86","s87","s88","s89","s90","s91","s92","s93","s94","s95","s96","s97","s98","s99","vcc","scc","memory");
}


extern "C" void kernel_launch(void* const* d_in, const int* in_sizes, int n_in, void* d_out, int out_size, void* d_ws, size_t ws_size, hipStream_t stream) {
  k_fused<<<256, 256, 0, stream>>>(d_in[0], 2097152u, 0x20000u, d_in[3], 4194304u, 0x20000u, d_in[1], 16777216u, 0x20000u, d_in[2], 16777216u, 0x20000u, d_in[4], 4194304u, 0x20000u, (char*)d_ws + 0ull * 1048576, 22020096u, 0x20000u, d_out, 2097152u, 0x20000u, d_in[7], 1409024u, 0x20000u, d_in[8], 1409024u, 0x20000u, d_in[9], 1409024u, 0x20000u, d_in[6], 1024u, 0x20000u);
}
```
